```python
import torch

def _softmax(x, axis=-1):
    return torch.softmax(x, dim=axis)
def _gelu(x):
    return torch.nn.functional.gelu(x, approximate="tanh")
def _silu(x):
    return torch.nn.functional.silu(x)
def _sigmoid(x):
    return torch.sigmoid(x)
def _layernorm(x, axis=-1, eps=1e-5):
    m = x.mean(dim=axis, keepdim=True)
    v = x.var(dim=axis, keepdim=True, unbiased=False)
    return (x - m) / torch.sqrt(v + eps)
def _scatter_add(tgt, idx, src, axis=0):
    return tgt.clone().index_add_(axis, idx, src)
def _segment_sum(src, seg, n):
    out = torch.zeros((n,) + src.shape[1:], dtype=src.dtype)
    return out.index_add_(0, seg, src)
def _pad(x, pw):
    flat = tuple(v for pair in reversed(pw) for v in pair)
    return torch.nn.functional.pad(x, flat)

kernel_name = 'concat_heads__gqa_block_cached__mha_block_cached__near_ridge'

def setup_inputs():

    _w = (lambda a: (lambda t: t.to(torch.bfloat16) if t.is_floating_point() else t)(torch.as_tensor(a)))
    def _A_setup():
        B, Sq, D = 8, 144, 448
        Hq, Hkv, Sk, Dh = 16, 8, 1024, 256
        assert Hq % Hkv == 0, (Hq, Hkv)
        return {
            "x": _w((lambda *s: torch.randn(*s, dtype=torch.bfloat16) * 0.8)(B, Sq, D)),
            "k_cache": _w((lambda *s: torch.randn(*s, dtype=torch.bfloat16) * 0.8)(B, Hkv, Sk, Dh)),
            "v_cache": _w((lambda *s: torch.randn(*s, dtype=torch.bfloat16) * 0.05)(B, Hkv, Sk, Dh)),
            "wq": _w((lambda *s: torch.randn(*s, dtype=torch.bfloat16) * 0.8)(D, Hq * Dh) * (D ** -0.5)),
            "wo": _w((lambda *s: torch.randn(*s, dtype=torch.bfloat16) * 0.05)(Hq * Dh, D) * ((Hq * Dh) ** -0.5)),
        }
    _A_in = _A_setup()

    def _B_setup():
        B, Sq, D = 8, 144, 448
        H, Sk, Dh = 16, 1024, 256
        return {
            "x": _w((lambda *s: torch.randn(*s, dtype=torch.bfloat16) * 0.8)(B, Sq, D)),
            "k_cache": _w((lambda *s: torch.randn(*s, dtype=torch.bfloat16) * 0.8)(B, H, Sk, Dh)),
            "v_cache": _w((lambda *s: torch.randn(*s, dtype=torch.bfloat16) * 0.05)(B, H, Sk, Dh)),
            "wq": _w((lambda *s: torch.randn(*s, dtype=torch.bfloat16) * 0.8)(D, H * Dh) * (D ** -0.5)),
            "wo": _w((lambda *s: torch.randn(*s, dtype=torch.bfloat16) * 0.05)(H * Dh, D) * ((H * Dh) ** -0.5)),
        }
    _B_in = _B_setup()

    return {
        "A_x": _A_in["x"],
        "A_k_cache": _A_in["k_cache"],
        "A_v_cache": _A_in["v_cache"],
        "A_wq": _A_in["wq"],
        "A_wo": _A_in["wo"],
        "B_x": _B_in["x"],
        "B_k_cache": _B_in["k_cache"],
        "B_v_cache": _B_in["v_cache"],
        "B_wq": _B_in["wq"],
        "B_wo": _B_in["wo"],
    }

def reference(A_x, A_k_cache, A_v_cache, A_wq, A_wo, B_x, B_k_cache, B_v_cache, B_wq, B_wo):
    def _A_ref(x, k_cache, v_cache, wq, wo):
        B, Sq, D = x.shape
        Hq, Dh = 16, 256
        Hkv = k_cache.shape[1]
        rep = Hq // Hkv
        q = (lambda x, *d: x.permute(*d))((x @ wq).reshape(B, Sq, Hq, Dh), 0, 2, 1, 3)
        k = (lambda x, n, axis: torch.repeat_interleave(x, n, dim=axis))(k_cache, rep, axis=1)
        v = (lambda x, n, axis: torch.repeat_interleave(x, n, dim=axis))(v_cache, rep, axis=1)
        scores = torch.einsum("bhqd,bhkd->bhqk", q, k) * (Dh ** -0.5)
        p = _softmax(scores, axis=-1)
        o = torch.einsum("bhqk,bhkd->bhqd", p, v)
        return (lambda x, *d: x.permute(*d))(o, 0, 2, 1, 3).reshape(B, Sq, Hq * Dh) @ wo

    def _B_ref(x, k_cache, v_cache, wq, wo):
        B, Sq, D = x.shape
        H, Dh = 16, 256
        q = (lambda x, *d: x.permute(*d))((x @ wq).reshape(B, Sq, H, Dh), 0, 2, 1, 3)
        scores = torch.einsum("bhqd,bhkd->bhqk", q, k_cache) * (Dh ** -0.5)
        p = _softmax(scores, axis=-1)
        o = torch.einsum("bhqk,bhkd->bhqd", p, v_cache)
        return (lambda x, *d: x.permute(*d))(o, 0, 2, 1, 3).reshape(B, Sq, H * Dh) @ wo

    a = _A_ref(A_x, A_k_cache, A_v_cache, A_wq, A_wo)
    b = _B_ref(B_x, B_k_cache, B_v_cache, B_wq, B_wo)
    return torch.concatenate([a, b], axis=-1)
```

```cpp
#include <hip/hip_runtime.h>
typedef unsigned u32x4 __attribute__((ext_vector_type(4)));
__global__ __launch_bounds__(256, 1) void fused_attn(const void* xA, const void* kA, const void* vA, const void* wqA, const void* woA,
    const void* xB, const void* kB, const void* vB, const void* wqB, const void* woB, void* out, void* ws) {
  __shared__ __attribute__((aligned(16))) char lds[163840];
  asm volatile(
    "Lentry_%=:\n"
    "s_lshr_b32 s48, %1, 7\n"
    "s_mul_i32 s49, s48, 40\n"
    "s_mov_b64 s[50:51], %0\n"
    "s_load_dwordx4 s[76:79], s[50:51], 80\n"
    "s_add_u32 s50, s50, s49\n"
    "s_addc_u32 s51, s51, 0\n"
    "s_load_dwordx8 s[64:71], s[50:51], 0\n"
    "s_load_dwordx2 s[72:73], s[50:51], 32\n"
    "s_getpc_b64 s[8:9]\n"
    "Lpc0_%=:\n"
    "s_sub_u32 s8, s8, Lpc0_%=-Lentry_%=\n"
    "s_subb_u32 s9, s9, 0\n"
    "v_lshlrev_b32 v45, 7, %3\n"
    "s_and_b32 s9, s9, 0xffff\n"
    "s_bfe_u32 s10, %1, 0x40003\n"
    "s_lshl_b32 s10, s10, 12\n"
    "v_cmp_gt_u32 vcc, 32, %3\n"
    "v_add_u32 v45, s10, v45\n"
    "s_mov_b32 s10, Lrare0_%=-Lentry_%=\n"
    "s_mov_b32 s11, 0x20000\n"
    "v_cndmask_b32 v45, -1, v45, vcc\n"
    "buffer_load_dword v37, v45, s[8:11], 0 offen\n"
    "v_lshrrev_b32 v40, 6, %3\n"
    "v_and_b32 v1, 63, %3\n"
    "v_readfirstlane_b32 s59, v40\n"
    "s_mov_b32 s60, s48\n"
    "s_mov_b32 s98, %1\n"
    "s_mov_b32 s99, %2\n"
    "s_and_b32 s63, s98, 7\n"
    "s_bfe_u32 s61, s98, 0x30004\n"
    "s_bfe_u32 s48, s98, 0x10003\n"
    "s_lshl_b32 s62, s63, 1\n"
    "s_add_u32 s62, s62, s48\n"
    "v_and_b32 v2, 15, v1\n"
    "v_lshrrev_b32 v3, 4, v1\n"
    "s_mul_i32 s34, s48, 3\n"
    "s_add_u32 s34, s34, s61\n"
    "s_mul_i32 s48, s34, 37\n"
    "s_lshr_b32 s48, s48, 8\n"
    "s_mul_i32 s48, s48, 7\n"
    "s_sub_u32 s34, s34, s48\n"
    "s_cmp_eq_u32 s61, 7\n"
    "s_cselect_b32 s35, 0, s61\n"
    "s_cmp_lg_u32 s59, 3\n"
    "s_cbranch_scc1 Lcompute_%=\n"
    "v_lshrrev_b32 v40, 3, v1\n"
    "v_mul_u32_u24 v40, 896, v40\n"
    "v_and_b32 v41, 7, v1\n"
    "v_add_u32 v42, 0, v3\n"
    "v_xor_b32 v42, v42, v41\n"
    "v_lshl_add_u32 v84, v42, 4, v40\n"
    "v_add_u32 v42, 4, v3\n"
    "v_xor_b32 v42, v42, v41\n"
    "v_lshl_add_u32 v85, v42, 4, v40\n"
    "v_add_u32 v85, 6144, v85\n"
    "v_add_u32 v42, 0, v3\n"
    "v_xor_b32 v42, v42, v41\n"
    "v_lshl_add_u32 v86, v42, 4, v40\n"
    "v_add_u32 v86, 12288, v86\n"
    "v_add_u32 v42, 4, v3\n"
    "v_xor_b32 v42, v42, v41\n"
    "v_lshl_add_u32 v87, v42, 4, v40\n"
    "v_add_u32 v87, 18432, v87\n"
    "v_lshrrev_b32 v40, 5, v1\n"
    "v_and_b32 v41, 31, v1\n"
    "v_add_u32 v42, 0, v40\n"
    "v_lshlrev_b32 v42, 2, v42\n"
    "v_xor_b32 v42, v42, v41\n"
    "v_lshlrev_b32 v42, 4, v42\n"
    "v_lshl_add_u32 v76, v40, 13, v42\n"
    "v_add_u32 v42, 2, v40\n"
    "v_lshlrev_b32 v42, 2, v42\n"
    "v_xor_b32 v42, v42, v41\n"
    "v_lshlrev_b32 v42, 4, v42\n"
    "v_lshl_add_u32 v77, v40, 13, v42\n"
    "v_add_u32 v77, 15360, v77\n"
    "v_add_u32 v42, 0, v40\n"
    "v_lshlrev_b32 v42, 2, v42\n"
    "v_xor_b32 v42, v42, v41\n"
    "v_lshlrev_b32 v42, 4, v42\n"
    "v_lshl_add_u32 v78, v40, 13, v42\n"
    "v_add_u32 v78, 30720, v78\n"
    "v_add_u32 v42, 2, v40\n"
    "v_lshlrev_b32 v42, 2, v42\n"
    "v_xor_b32 v42, v42, v41\n"
    "v_lshlrev_b32 v42, 4, v42\n"
    "v_lshl_add_u32 v79, v40, 13, v42\n"
    "v_add_u32 v79, 46080, v79\n"
    "s_lshl_b32 s53, s34, 19\n"
    "s_lshl_b32 s55, s34, 7\n"
    "s_mul_i32 s48, s61, 129024\n"
    "s_lshl_b32 s49, s62, 9\n"
    "s_mov_b32 s26, 129024\n"
    "s_mov_b32 s27, 0x20000\n"
    "s_mov_b32 s18, 3670016\n"
    "s_mov_b32 s19, 0x20000\n"
    "s_sub_u32 s18, s18, s49\n"
    "s_waitcnt lgkmcnt(0)\n"
    "s_add_u32 s16, s70, s49\n"
    "s_addc_u32 s17, s71, 0\n"
    "s_and_b32 s17, s17, 0xffff\n"
    "s_add_u32 s24, s64, s48\n"
    "s_addc_u32 s25, s65, 0\n"
    "s_and_b32 s25, s25, 0xffff\n"
    "s_add_u32 m0, s99, 112608\n"
    "s_add_u32 s52, s53, 0\n"
    "buffer_load_dwordx4 v76, s[16:19], s52 offen offset:0  lds\n"
    "buffer_load_dwordx4 v77, s[16:19], s52 offen offset:1024  lds\n"
    "buffer_load_dwordx4 v78, s[16:19], s52 offen offset:2048  lds\n"
    "buffer_load_dwordx4 v79, s[16:19], s52 offen offset:3072  lds\n"
    "s_add_u32 m0, s99, 145408\n"
    "s_add_u32 s52, s55, 0\n"
    "buffer_load_dwordx4 v84, s[24:27], s52 offen offset:0  lds\n"
    "buffer_load_dwordx4 v85, s[24:27], s52 offen offset:1024  lds\n"
    "buffer_load_dwordx4 v86, s[24:27], s52 offen offset:2048  lds\n"
    "buffer_load_dwordx4 v87, s[24:27], s52 offen offset:3072  lds\n"
    "s_add_u32 m0, s99, 116704\n"
    "s_add_u32 s52, s53, 131072\n"
    "buffer_load_dwordx4 v76, s[16:19], s52 offen offset:0  lds\n"
    "buffer_load_dwordx4 v77, s[16:19], s52 offen offset:1024  lds\n"
    "buffer_load_dwordx4 v78, s[16:19], s52 offen offset:2048  lds\n"
    "buffer_load_dwordx4 v79, s[16:19], s52 offen offset:3072  lds\n"
    "s_add_u32 m0, s99, 149504\n"
    "s_add_u32 s52, s55, 28672\n"
    "buffer_load_dwordx4 v84, s[24:27], s52 offen offset:0  lds\n"
    "buffer_load_dwordx4 v85, s[24:27], s52 offen offset:1024  lds\n"
    "buffer_load_dwordx4 v86, s[24:27], s52 offen offset:2048  lds\n"
    "buffer_load_dwordx4 v87, s[24:27], s52 offen offset:3072  lds\n"
    "s_add_u32 m0, s99, 120808\n"
    "s_add_u32 s52, s53, 65536\n"
    "buffer_load_dwordx4 v76, s[16:19], s52 offen offset:0  lds\n"
    "buffer_load_dwordx4 v77, s[16:19], s52 offen offset:1024  lds\n"
    "buffer_load_dwordx4 v78, s[16:19], s52 offen offset:2048  lds\n"
    "buffer_load_dwordx4 v79, s[16:19], s52 offen offset:3072  lds\n"
    "s_add_u32 m0, s99, 153600\n"
    "s_add_u32 s52, s55, 57344\n"
    "buffer_load_dwordx4 v84, s[24:27], s52 offen offset:0  lds\n"
    "buffer_load_dwordx4 v85, s[24:27], s52 offen offset:1024  lds\n"
    "buffer_load_dwordx4 v86, s[24:27], s52 offen offset:2048  lds\n"
    "buffer_load_dwordx4 v87, s[24:27], s52 offen offset:3072  lds\n"
    "s_add_u32 m0, s99, 124904\n"
    "s_add_u32 s52, s53, 196608\n"
    "buffer_load_dwordx4 v76, s[16:19], s52 offen offset:0  lds\n"
    "buffer_load_dwordx4 v77, s[16:19], s52 offen offset:1024  lds\n"
    "buffer_load_dwordx4 v78, s[16:19], s52 offen offset:2048  lds\n"
    "buffer_load_dwordx4 v79, s[16:19], s52 offen offset:3072  lds\n"
    "s_add_u32 m0, s99, 157696\n"
    "s_add_u32 s52, s55, 86016\n"
    "buffer_load_dwordx4 v84, s[24:27], s52 offen offset:0  lds\n"
    "buffer_load_dwordx4 v85, s[24:27], s52 offen offset:1024  lds\n"
    "buffer_load_dwordx4 v86, s[24:27], s52 offen offset:2048  lds\n"
    "buffer_load_dwordx4 v87, s[24:27], s52 offen offset:3072  lds\n"
    "s_add_u32 m0, s99, 161792\n"
    "s_add_u32 s52, s55, 114688\n"
    "buffer_load_dwordx4 v84, s[24:27], s52 offen offset:0  lds\n"
    "buffer_load_dwordx4 v85, s[24:27], s52 offen offset:1024  lds\n"
    "s_add_u32 s55, s55, 128\n"
    "s_cmp_eq_u32 s55, 896\n"
    "s_cselect_b32 s55, 0, s55\n"
    "s_add_u32 m0, s99, 129008\n"
    "s_add_u32 s52, s53, 262144\n"
    "buffer_load_dwordx4 v76, s[16:19], s52 offen offset:0  lds\n"
    "buffer_load_dwordx4 v77, s[16:19], s52 offen offset:1024  lds\n"
    "buffer_load_dwordx4 v78, s[16:19], s52 offen offset:2048  lds\n"
    "buffer_load_dwordx4 v79, s[16:19], s52 offen offset:3072  lds\n"
    "s_add_u32 m0, s99, 133104\n"
    "s_add_u32 s52, s53, 393216\n"
    "buffer_load_dwordx4 v76, s[16:19], s52 offen offset:0  lds\n"
    "buffer_load_dwordx4 v77, s[16:19], s52 offen offset:1024  lds\n"
    "buffer_load_dwordx4 v78, s[16:19], s52 offen offset:2048  lds\n"
    "buffer_load_dwordx4 v79, s[16:19], s52 offen offset:3072  lds\n"
    "s_add_u32 m0, s99, 137208\n"
    "s_add_u32 s52, s53, 327680\n"
    "buffer_load_dwordx4 v76, s[16:19], s52 offen offset:0  lds\n"
    "buffer_load_dwordx4 v77, s[16:19], s52 offen offset:1024  lds\n"
    "buffer_load_dwordx4 v78, s[16:19], s52 offen offset:2048  lds\n"
    "buffer_load_dwordx4 v79, s[16:19], s52 offen offset:3072  lds\n"
    "s_add_u32 m0, s99, 141304\n"
    "s_add_u32 s52, s53, 458752\n"
    "buffer_load_dwordx4 v76, s[16:19], s52 offen offset:0  lds\n"
    "buffer_load_dwordx4 v77, s[16:19], s52 offen offset:1024  lds\n"
    "buffer_load_dwordx4 v78, s[16:19], s52 offen offset:2048  lds\n"
    "buffer_load_dwordx4 v79, s[16:19], s52 offen offset:3072  lds\n"
    "s_add_u32 s53, s53, 524288\n"
    "s_cmp_eq_u32 s53, 3670016\n"
    "s_cselect_b32 s53, 0, s53\n"
    "s_add_u32 m0, s99, 32800\n"
    "s_add_u32 s52, s55, 0\n"
    "buffer_load_dwordx4 v84, s[24:27], s52 offen offset:0  lds\n"
    "buffer_load_dwordx4 v85, s[24:27], s52 offen offset:1024  lds\n"
    "buffer_load_dwordx4 v86, s[24:27], s52 offen offset:2048  lds\n"
    "buffer_load_dwordx4 v87, s[24:27], s52 offen offset:3072  lds\n"
    "s_add_u32 m0, s99, 36896\n"
    "s_add_u32 s52, s55, 28672\n"
    "buffer_load_dwordx4 v84, s[24:27], s52 offen offset:0  lds\n"
    "buffer_load_dwordx4 v85, s[24:27], s52 offen offset:1024  lds\n"
    "buffer_load_dwordx4 v86, s[24:27], s52 offen offset:2048  lds\n"
    "buffer_load_dwordx4 v87, s[24:27], s52 offen offset:3072  lds\n"
    "s_add_u32 m0, s99, 40992\n"
    "s_add_u32 s52, s55, 57344\n"
    "buffer_load_dwordx4 v84, s[24:27], s52 offen offset:0  lds\n"
    "buffer_load_dwordx4 v85, s[24:27], s52 offen offset:1024  lds\n"
    "buffer_load_dwordx4 v86, s[24:27], s52 offen offset:2048  lds\n"
    "buffer_load_dwordx4 v87, s[24:27], s52 offen offset:3072  lds\n"
    "s_add_u32 m0, s99, 45088\n"
    "s_add_u32 s52, s55, 86016\n"
    "buffer_load_dwordx4 v84, s[24:27], s52 offen offset:0  lds\n"
    "buffer_load_dwordx4 v85, s[24:27], s52 offen offset:1024  lds\n"
    "buffer_load_dwordx4 v86, s[24:27], s52 offen offset:2048  lds\n"
    "buffer_load_dwordx4 v87, s[24:27], s52 offen offset:3072  lds\n"
    "s_add_u32 m0, s99, 49184\n"
    "s_add_u32 s52, s55, 114688\n"
    "buffer_load_dwordx4 v84, s[24:27], s52 offen offset:0  lds\n"
    "buffer_load_dwordx4 v85, s[24:27], s52 offen offset:1024  lds\n"
    "s_add_u32 s55, s55, 128\n"
    "s_cmp_eq_u32 s55, 896\n"
    "s_cselect_b32 s55, 0, s55\n"
    "s_waitcnt vmcnt(34)\n"
    "s_barrier\n"
    "s_add_u32 m0, s99, 0\n"
    "s_add_u32 s52, s53, 0\n"
    "buffer_load_dwordx4 v76, s[16:19], s52 offen offset:0  lds\n"
    "buffer_load_dwordx4 v77, s[16:19], s52 offen offset:1024  lds\n"
    "buffer_load_dwordx4 v78, s[16:19], s52 offen offset:2048  lds\n"
    "buffer_load_dwordx4 v79, s[16:19], s52 offen offset:3072  lds\n"
    "s_add_u32 m0, s99, 4096\n"
    "s_add_u32 s52, s53, 131072\n"
    "buffer_load_dwordx4 v76, s[16:19], s52 offen offset:0  lds\n"
    "buffer_load_dwordx4 v77, s[16:19], s52 offen offset:1024  lds\n"
    "buffer_load_dwordx4 v78, s[16:19], s52 offen offset:2048  lds\n"
    "buffer_load_dwordx4 v79, s[16:19], s52 offen offset:3072  lds\n"
    "s_add_u32 m0, s99, 8200\n"
    "s_add_u32 s52, s53, 65536\n"
    "buffer_load_dwordx4 v76, s[16:19], s52 offen offset:0  lds\n"
    "buffer_load_dwordx4 v77, s[16:19], s52 offen offset:1024  lds\n"
    "buffer_load_dwordx4 v78, s[16:19], s52 offen offset:2048  lds\n"
    "buffer_load_dwordx4 v79, s[16:19], s52 offen offset:3072  lds\n"
    "s_add_u32 m0, s99, 12296\n"
    "s_add_u32 s52, s53, 196608\n"
    "buffer_load_dwordx4 v76, s[16:19], s52 offen offset:0  lds\n"
    "buffer_load_dwordx4 v77, s[16:19], s52 offen offset:1024  lds\n"
    "buffer_load_dwordx4 v78, s[16:19], s52 offen offset:2048  lds\n"
    "buffer_load_dwordx4 v79, s[16:19], s52 offen offset:3072  lds\n"
    "s_add_u32 s28, s99, 0\n"
    "s_add_u32 s29, s99, 51232\n"
    "s_add_u32 s30, s99, 112608\n"
    "s_mov_b32 s57, 5\n"
    "Llq_%=:\n"
    "s_waitcnt vmcnt(34)\n"
    "s_barrier\n"
    "s_add_u32 m0, s28, 16400\n"
    "s_add_u32 s52, s53, 262144\n"
    "buffer_load_dwordx4 v76, s[16:19], s52 offen offset:0  lds\n"
    "buffer_load_dwordx4 v77, s[16:19], s52 offen offset:1024  lds\n"
    "buffer_load_dwordx4 v78, s[16:19], s52 offen offset:2048  lds\n"
    "buffer_load_dwordx4 v79, s[16:19], s52 offen offset:3072  lds\n"
    "s_add_u32 m0, s28, 20496\n"
    "s_add_u32 s52, s53, 393216\n"
    "buffer_load_dwordx4 v76, s[16:19], s52 offen offset:0  lds\n"
    "buffer_load_dwordx4 v77, s[16:19], s52 offen offset:1024  lds\n"
    "buffer_load_dwordx4 v78, s[16:19], s52 offen offset:2048  lds\n"
    "buffer_load_dwordx4 v79, s[16:19], s52 offen offset:3072  lds\n"
    "s_add_u32 m0, s28, 24600\n"
    "s_add_u32 s52, s53, 327680\n"
    "buffer_load_dwordx4 v76, s[16:19], s52 offen offset:0  lds\n"
    "buffer_load_dwordx4 v77, s[16:19], s52 offen offset:1024  lds\n"
    "buffer_load_dwordx4 v78, s[16:19], s52 offen offset:2048  lds\n"
    "buffer_load_dwordx4 v79, s[16:19], s52 offen offset:3072  lds\n"
    "s_add_u32 m0, s28, 28696\n"
    "s_add_u32 s52, s53, 458752\n"
    "buffer_load_dwordx4 v76, s[16:19], s52 offen offset:0  lds\n"
    "buffer_load_dwordx4 v77, s[16:19], s52 offen offset:1024  lds\n"
    "buffer_load_dwordx4 v78, s[16:19], s52 offen offset:2048  lds\n"
    "buffer_load_dwordx4 v79, s[16:19], s52 offen offset:3072  lds\n"
    "s_add_u32 s53, s53, 524288\n"
    "s_cmp_eq_u32 s53, 3670016\n"
    "s_cselect_b32 s53, 0, s53\n"
    "s_add_u32 m0, s29, 32800\n"
    "s_add_u32 s52, s55, 0\n"
    "buffer_load_dwordx4 v84, s[24:27], s52 offen offset:0  lds\n"
    "buffer_load_dwordx4 v85, s[24:27], s52 offen offset:1024  lds\n"
    "buffer_load_dwordx4 v86, s[24:27], s52 offen offset:2048  lds\n"
    "buffer_load_dwordx4 v87, s[24:27], s52 offen offset:3072  lds\n"
    "s_add_u32 m0, s29, 36896\n"
    "s_add_u32 s52, s55, 28672\n"
    "buffer_load_dwordx4 v84, s[24:27], s52 offen offset:0  lds\n"
    "buffer_load_dwordx4 v85, s[24:27], s52 offen offset:1024  lds\n"
    "buffer_load_dwordx4 v86, s[24:27], s52 offen offset:2048  lds\n"
    "buffer_load_dwordx4 v87, s[24:27], s52 offen offset:3072  lds\n"
    "s_add_u32 m0, s29, 40992\n"
    "s_add_u32 s52, s55, 57344\n"
    "buffer_load_dwordx4 v84, s[24:27], s52 offen offset:0  lds\n"
    "buffer_load_dwordx4 v85, s[24:27], s52 offen offset:1024  lds\n"
    "buffer_load_dwordx4 v86, s[24:27], s52 offen offset:2048  lds\n"
    "buffer_load_dwordx4 v87, s[24:27], s52 offen offset:3072  lds\n"
    "s_add_u32 m0, s29, 45088\n"
    "s_add_u32 s52, s55, 86016\n"
    "buffer_load_dwordx4 v84, s[24:27], s52 offen offset:0  lds\n"
    "buffer_load_dwordx4 v85, s[24:27], s52 offen offset:1024  lds\n"
    "buffer_load_dwordx4 v86, s[24:27], s52 offen offset:2048  lds\n"
    "buffer_load_dwordx4 v87, s[24:27], s52 offen offset:3072  lds\n"
    "s_add_u32 m0, s29, 49184\n"
    "s_add_u32 s52, s55, 114688\n"
    "buffer_load_dwordx4 v84, s[24:27], s52 offen offset:0  lds\n"
    "buffer_load_dwordx4 v85, s[24:27], s52 offen offset:1024  lds\n"
    "s_add_u32 s55, s55, 128\n"
    "s_cmp_eq_u32 s55, 896\n"
    "s_cselect_b32 s55, 0, s55\n"
    "s_add_u32 m0, s29, 0\n"
    "s_add_u32 s52, s53, 0\n"
    "buffer_load_dwordx4 v76, s[16:19], s52 offen offset:0  lds\n"
    "buffer_load_dwordx4 v77, s[16:19], s52 offen offset:1024  lds\n"
    "buffer_load_dwordx4 v78, s[16:19], s52 offen offset:2048  lds\n"
    "buffer_load_dwordx4 v79, s[16:19], s52 offen offset:3072  lds\n"
    "s_add_u32 m0, s29, 4096\n"
    "s_add_u32 s52, s53, 131072\n"
    "buffer_load_dwordx4 v76, s[16:19], s52 offen offset:0  lds\n"
    "buffer_load_dwordx4 v77, s[16:19], s52 offen offset:1024  lds\n"
    "buffer_load_dwordx4 v78, s[16:19], s52 offen offset:2048  lds\n"
    "buffer_load_dwordx4 v79, s[16:19], s52 offen offset:3072  lds\n"
    "s_add_u32 m0, s29, 8200\n"
    "s_add_u32 s52, s53, 65536\n"
    "buffer_load_dwordx4 v76, s[16:19], s52 offen offset:0  lds\n"
    "buffer_load_dwordx4 v77, s[16:19], s52 offen offset:1024  lds\n"
    "buffer_load_dwordx4 v78, s[16:19], s52 offen offset:2048  lds\n"
    "buffer_load_dwordx4 v79, s[16:19], s52 offen offset:3072  lds\n"
    "s_add_u32 m0, s29, 12296\n"
    "s_add_u32 s52, s53, 196608\n"
    "buffer_load_dwordx4 v76, s[16:19], s52 offen offset:0  lds\n"
    "buffer_load_dwordx4 v77, s[16:19], s52 offen offset:1024  lds\n"
    "buffer_load_dwordx4 v78, s[16:19], s52 offen offset:2048  lds\n"
    "buffer_load_dwordx4 v79, s[16:19], s52 offen offset:3072  lds\n"
    "s_mov_b32 s48, s28\n"
    "s_mov_b32 s28, s29\n"
    "s_mov_b32 s29, s30\n"
    "s_mov_b32 s30, s48\n"
    "s_sub_u32 s57, s57, 1\n"
    "s_cmp_lg_u32 s57, 0\n"
    "s_cbranch_scc1 Llq_%=\n"
    "s_lshl_b32 s48, s61, 4\n"
    "s_add_u32 s48, s48, s62\n"
    "s_lshl_b32 s49, s61, 3\n"
    "s_add_u32 s49, s49, s63\n"
    "s_cmp_eq_u32 s60, 0\n"
    "s_cselect_b32 s48, s49, s48\n"
    "s_lshl_b32 s48, s48, 19\n"
    "s_add_u32 s8, s66, s48\n"
    "s_addc_u32 s9, s67, 0\n"
    "s_and_b32 s9, s9, 0xffff\n"
    "s_mov_b32 s10, 524288\n"
    "s_mov_b32 s11, 0x20000\n"
    "s_add_u32 s12, s68, s48\n"
    "s_addc_u32 s13, s69, 0\n"
    "s_and_b32 s13, s13, 0xffff\n"
    "s_mov_b32 s14, 524288\n"
    "s_mov_b32 s15, 0x20000\n"
    "v_lshrrev_b32 v40, 5, v1\n"
    "v_and_b32 v41, 31, v1\n"
    "v_add_u32 v42, 0, v40\n"
    "v_xor_b32 v42, v42, v41\n"
    "v_lshlrev_b32 v42, 4, v42\n"
    "v_lshl_add_u32 v64, v40, 9, v42\n"
    "v_add_u32 v42, 2, v40\n"
    "v_xor_b32 v42, v42, v41\n"
    "v_lshlrev_b32 v42, 4, v42\n"
    "v_lshl_add_u32 v65, v40, 9, v42\n"
    "v_add_u32 v42, 4, v40\n"
    "v_xor_b32 v42, v42, v41\n"
    "v_lshlrev_b32 v42, 4, v42\n"
    "v_lshl_add_u32 v66, v40, 9, v42\n"
    "v_add_u32 v42, 6, v40\n"
    "v_xor_b32 v42, v42, v41\n"
    "v_lshlrev_b32 v42, 4, v42\n"
    "v_lshl_add_u32 v67, v40, 9, v42\n"
    "v_add_u32 v42, 8, v40\n"
    "v_xor_b32 v42, v42, v41\n"
    "v_lshlrev_b32 v42, 4, v42\n"
    "v_lshl_add_u32 v68, v40, 9, v42\n"
    "v_add_u32 v42, 10, v40\n"
    "v_xor_b32 v42, v42, v41\n"
    "v_lshlrev_b32 v42, 4, v42\n"
    "v_lshl_add_u32 v69, v40, 9, v42\n"
    "v_add_u32 v42, 12, v40\n"
    "v_xor_b32 v42, v42, v41\n"
    "v_lshlrev_b32 v42, 4, v42\n"
    "v_lshl_add_u32 v70, v40, 9, v42\n"
    "v_add_u32 v42, 14, v40\n"
    "v_xor_b32 v42, v42, v41\n"
    "v_lshlrev_b32 v42, 4, v42\n"
    "v_lshl_add_u32 v71, v40, 9, v42\n"
    "v_lshrrev_b32 v40, 5, v1\n"
    "v_and_b32 v41, 31, v1\n"
    "v_add_u32 v42, 0, v40\n"
    "v_lshlrev_b32 v42, 1, v42\n"
    "v_xor_b32 v42, v42, v41\n"
    "v_lshlrev_b32 v42, 4, v42\n"
    "v_lshl_add_u32 v72, v40, 9, v42\n"
    "v_add_u32 v42, 2, v40\n"
    "v_lshlrev_b32 v42, 1, v42\n"
    "v_xor_b32 v42, v42, v41\n"
    "v_lshlrev_b32 v42, 4, v42\n"
    "v_lshl_add_u32 v73, v40, 9, v42\n"
    "v_add_u32 v42, 4, v40\n"
    "v_lshlrev_b32 v42, 1, v42\n"
    "v_xor_b32 v42, v42, v41\n"
    "v_lshlrev_b32 v42, 4, v42\n"
    "v_lshl_add_u32 v74, v40, 9, v42\n"
    "v_add_u32 v42, 6, v40\n"
    "v_lshlrev_b32 v42, 1, v42\n"
    "v_xor_b32 v42, v42, v41\n"
    "v_lshlrev_b32 v42, 4, v42\n"
    "v_lshl_add_u32 v75, v40, 9, v42\n"
    "s_mov_b32 s54, 0\n"
    "s_waitcnt vmcnt(34)\n"
    "s_barrier\n"
    "s_add_u32 m0, s99, 129008\n"
    "s_add_u32 s52, s53, 262144\n"
    "buffer_load_dwordx4 v76, s[16:19], s52 offen offset:0  lds\n"
    "buffer_load_dwordx4 v77, s[16:19], s52 offen offset:1024  lds\n"
    "buffer_load_dwordx4 v78, s[16:19], s52 offen offset:2048  lds\n"
    "buffer_load_dwordx4 v79, s[16:19], s52 offen offset:3072  lds\n"
    "s_add_u32 m0, s99, 133104\n"
    "s_add_u32 s52, s53, 393216\n"
    "buffer_load_dwordx4 v76, s[16:19], s52 offen offset:0  lds\n"
    "buffer_load_dwordx4 v77, s[16:19], s52 offen offset:1024  lds\n"
    "buffer_load_dwordx4 v78, s[16:19], s52 offen offset:2048  lds\n"
    "buffer_load_dwordx4 v79, s[16:19], s52 offen offset:3072  lds\n"
    "s_add_u32 m0, s99, 137208\n"
    "s_add_u32 s52, s53, 327680\n"
    "buffer_load_dwordx4 v76, s[16:19], s52 offen offset:0  lds\n"
    "buffer_load_dwordx4 v77, s[16:19], s52 offen offset:1024  lds\n"
    "buffer_load_dwordx4 v78, s[16:19], s52 offen offset:2048  lds\n"
    "buffer_load_dwordx4 v79, s[16:19], s52 offen offset:3072  lds\n"
    "s_add_u32 m0, s99, 141304\n"
    "s_add_u32 s52, s53, 458752\n"
    "buffer_load_dwordx4 v76, s[16:19], s52 offen offset:0  lds\n"
    "buffer_load_dwordx4 v77, s[16:19], s52 offen offset:1024  lds\n"
    "buffer_load_dwordx4 v78, s[16:19], s52 offen offset:2048  lds\n"
    "buffer_load_dwordx4 v79, s[16:19], s52 offen offset:3072  lds\n"
    "s_add_u32 s53, s53, 524288\n"
    "s_cmp_eq_u32 s53, 3670016\n"
    "s_cselect_b32 s53, 0, s53\n"
    "s_add_u32 m0, s99, 0\n"
    "s_add_u32 s52, s54, 0\n"
    "buffer_load_dwordx4 v64, s[8:11], s52 offen offset:0 nt lds\n"
    "buffer_load_dwordx4 v65, s[8:11], s52 offen offset:1024 nt lds\n"
    "buffer_load_dwordx4 v66, s[8:11], s52 offen offset:2048 nt lds\n"
    "buffer_load_dwordx4 v67, s[8:11], s52 offen offset:3072 nt lds\n"
    "s_add_u32 m0, s99, 4096\n"
    "s_add_u32 s52, s54, 4096\n"
    "buffer_load_dwordx4 v68, s[8:11], s52 offen offset:0 nt lds\n"
    "buffer_load_dwordx4 v69, s[8:11], s52 offen offset:1024 nt lds\n"
    "buffer_load_dwordx4 v70, s[8:11], s52 offen offset:2048 nt lds\n"
    "buffer_load_dwordx4 v71, s[8:11], s52 offen offset:3072 nt lds\n"
    "s_add_u32 m0, s99, 8192\n"
    "s_add_u32 s52, s54, 8192\n"
    "buffer_load_dwordx4 v64, s[8:11], s52 offen offset:0 nt lds\n"
    "buffer_load_dwordx4 v65, s[8:11], s52 offen offset:1024 nt lds\n"
    "buffer_load_dwordx4 v66, s[8:11], s52 offen offset:2048 nt lds\n"
    "buffer_load_dwordx4 v67, s[8:11], s52 offen offset:3072 nt lds\n"
    "s_add_u32 m0, s99, 12288\n"
    "s_add_u32 s52, s54, 12288\n"
    "buffer_load_dwordx4 v68, s[8:11], s52 offen offset:0 nt lds\n"
    "buffer_load_dwordx4 v69, s[8:11], s52 offen offset:1024 nt lds\n"
    "buffer_load_dwordx4 v70, s[8:11], s52 offen offset:2048 nt lds\n"
    "buffer_load_dwordx4 v71, s[8:11], s52 offen offset:3072 nt lds\n"
    "s_add_u32 m0, s99, 16384\n"
    "s_add_u32 s52, s54, 16384\n"
    "buffer_load_dwordx4 v64, s[8:11], s52 offen offset:0 nt lds\n"
    "buffer_load_dwordx4 v65, s[8:11], s52 offen offset:1024 nt lds\n"
    "buffer_load_dwordx4 v66, s[8:11], s52 offen offset:2048 nt lds\n"
    "buffer_load_dwordx4 v67, s[8:11], s52 offen offset:3072 nt lds\n"
    "s_add_u32 m0, s99, 20480\n"
    "s_add_u32 s52, s54, 20480\n"
    "buffer_load_dwordx4 v68, s[8:11], s52 offen offset:0 nt lds\n"
    "buffer_load_dwordx4 v69, s[8:11], s52 offen offset:1024 nt lds\n"
    "buffer_load_dwordx4 v70, s[8:11], s52 offen offset:2048 nt lds\n"
    "buffer_load_dwordx4 v71, s[8:11], s52 offen offset:3072 nt lds\n"
    "s_add_u32 m0, s99, 24576\n"
    "s_add_u32 s52, s54, 24576\n"
    "buffer_load_dwordx4 v64, s[8:11], s52 offen offset:0 nt lds\n"
    "buffer_load_dwordx4 v65, s[8:11], s52 offen offset:1024 nt lds\n"
    "buffer_load_dwordx4 v66, s[8:11], s52 offen offset:2048 nt lds\n"
    "buffer_load_dwordx4 v67, s[8:11], s52 offen offset:3072 nt lds\n"
    "s_add_u32 m0, s99, 28672\n"
    "s_add_u32 s52, s54, 28672\n"
    "buffer_load_dwordx4 v68, s[8:11], s52 offen offset:0 nt lds\n"
    "buffer_load_dwordx4 v69, s[8:11], s52 offen offset:1024 nt lds\n"
    "buffer_load_dwordx4 v70, s[8:11], s52 offen offset:2048 nt lds\n"
    "buffer_load_dwordx4 v71, s[8:11], s52 offen offset:3072 nt lds\n"
    "s_waitcnt vmcnt(32)\n"
    "s_barrier\n"
    "s_add_u32 m0, s99, 32768\n"
    "s_add_u32 s52, s54, 32768\n"
    "buffer_load_dwordx4 v64, s[8:11], s52 offen offset:0 nt lds\n"
    "buffer_load_dwordx4 v65, s[8:11], s52 offen offset:1024 nt lds\n"
    "buffer_load_dwordx4 v66, s[8:11], s52 offen offset:2048 nt lds\n"
    "buffer_load_dwordx4 v67, s[8:11], s52 offen offset:3072 nt lds\n"
    "s_add_u32 m0, s99, 36864\n"
    "s_add_u32 s52, s54, 36864\n"
    "buffer_load_dwordx4 v68, s[8:11], s52 offen offset:0 nt lds\n"
    "buffer_load_dwordx4 v69, s[8:11], s52 offen offset:1024 nt lds\n"
    "buffer_load_dwordx4 v70, s[8:11], s52 offen offset:2048 nt lds\n"
    "buffer_load_dwordx4 v71, s[8:11], s52 offen offset:3072 nt lds\n"
    "s_add_u32 m0, s99, 40960\n"
    "s_add_u32 s52, s54, 40960\n"
    "buffer_load_dwordx4 v64, s[8:11], s52 offen offset:0 nt lds\n"
    "buffer_load_dwordx4 v65, s[8:11], s52 offen offset:1024 nt lds\n"
    "buffer_load_dwordx4 v66, s[8:11], s52 offen offset:2048 nt lds\n"
    "buffer_load_dwordx4 v67, s[8:11], s52 offen offset:3072 nt lds\n"
    "s_add_u32 m0, s99, 45056\n"
    "s_add_u32 s52, s54, 45056\n"
    "buffer_load_dwordx4 v68, s[8:11], s52 offen offset:0 nt lds\n"
    "buffer_load_dwordx4 v69, s[8:11], s52 offen offset:1024 nt lds\n"
    "buffer_load_dwordx4 v70, s[8:11], s52 offen offset:2048 nt lds\n"
    "buffer_load_dwordx4 v71, s[8:11], s52 offen offset:3072 nt lds\n"
    "s_add_u32 m0, s99, 49152\n"
    "s_add_u32 s52, s54, 49152\n"
    "buffer_load_dwordx4 v64, s[8:11], s52 offen offset:0 nt lds\n"
    "buffer_load_dwordx4 v65, s[8:11], s52 offen offset:1024 nt lds\n"
    "buffer_load_dwordx4 v66, s[8:11], s52 offen offset:2048 nt lds\n"
    "buffer_load_dwordx4 v67, s[8:11], s52 offen offset:3072 nt lds\n"
    "s_add_u32 m0, s99, 53248\n"
    "s_add_u32 s52, s54, 53248\n"
    "buffer_load_dwordx4 v68, s[8:11], s52 offen offset:0 nt lds\n"
    "buffer_load_dwordx4 v69, s[8:11], s52 offen offset:1024 nt lds\n"
    "buffer_load_dwordx4 v70, s[8:11], s52 offen offset:2048 nt lds\n"
    "buffer_load_dwordx4 v71, s[8:11], s52 offen offset:3072 nt lds\n"
    "s_add_u32 m0, s99, 57344\n"
    "s_add_u32 s52, s54, 57344\n"
    "buffer_load_dwordx4 v64, s[8:11], s52 offen offset:0 nt lds\n"
    "buffer_load_dwordx4 v65, s[8:11], s52 offen offset:1024 nt lds\n"
    "buffer_load_dwordx4 v66, s[8:11], s52 offen offset:2048 nt lds\n"
    "buffer_load_dwordx4 v67, s[8:11], s52 offen offset:3072 nt lds\n"
    "s_add_u32 m0, s99, 61440\n"
    "s_add_u32 s52, s54, 61440\n"
    "buffer_load_dwordx4 v68, s[8:11], s52 offen offset:0 nt lds\n"
    "buffer_load_dwordx4 v69, s[8:11], s52 offen offset:1024 nt lds\n"
    "buffer_load_dwordx4 v70, s[8:11], s52 offen offset:2048 nt lds\n"
    "buffer_load_dwordx4 v71, s[8:11], s52 offen offset:3072 nt lds\n"
    "s_add_u32 m0, s99, 65536\n"
    "s_add_u32 s52, s54, 0\n"
    "buffer_load_dwordx4 v72, s[12:15], s52 offen offset:0 nt lds\n"
    "buffer_load_dwordx4 v73, s[12:15], s52 offen offset:1024 nt lds\n"
    "buffer_load_dwordx4 v74, s[12:15], s52 offen offset:2048 nt lds\n"
    "buffer_load_dwordx4 v75, s[12:15], s52 offen offset:3072 nt lds\n"
    "s_add_u32 m0, s99, 69632\n"
    "s_add_u32 s52, s54, 4096\n"
    "buffer_load_dwordx4 v72, s[12:15], s52 offen offset:0 nt lds\n"
    "buffer_load_dwordx4 v73, s[12:15], s52 offen offset:1024 nt lds\n"
    "buffer_load_dwordx4 v74, s[12:15], s52 offen offset:2048 nt lds\n"
    "buffer_load_dwordx4 v75, s[12:15], s52 offen offset:3072 nt lds\n"
    "s_add_u32 m0, s99, 73728\n"
    "s_add_u32 s52, s54, 8192\n"
    "buffer_load_dwordx4 v72, s[12:15], s52 offen offset:0 nt lds\n"
    "buffer_load_dwordx4 v73, s[12:15], s52 offen offset:1024 nt lds\n"
    "buffer_load_dwordx4 v74, s[12:15], s52 offen offset:2048 nt lds\n"
    "buffer_load_dwordx4 v75, s[12:15], s52 offen offset:3072 nt lds\n"
    "s_add_u32 m0, s99, 77824\n"
    "s_add_u32 s52, s54, 12288\n"
    "buffer_load_dwordx4 v72, s[12:15], s52 offen offset:0 nt lds\n"
    "buffer_load_dwordx4 v73, s[12:15], s52 offen offset:1024 nt lds\n"
    "buffer_load_dwordx4 v74, s[12:15], s52 offen offset:2048 nt lds\n"
    "buffer_load_dwordx4 v75, s[12:15], s52 offen offset:3072 nt lds\n"
    "s_add_u32 m0, s99, 81920\n"
    "s_add_u32 s52, s54, 16384\n"
    "buffer_load_dwordx4 v72, s[12:15], s52 offen offset:0 nt lds\n"
    "buffer_load_dwordx4 v73, s[12:15], s52 offen offset:1024 nt lds\n"
    "buffer_load_dwordx4 v74, s[12:15], s52 offen offset:2048 nt lds\n"
    "buffer_load_dwordx4 v75, s[12:15], s52 offen offset:3072 nt lds\n"
    "s_add_u32 m0, s99, 86016\n"
    "s_add_u32 s52, s54, 20480\n"
    "buffer_load_dwordx4 v72, s[12:15], s52 offen offset:0 nt lds\n"
    "buffer_load_dwordx4 v73, s[12:15], s52 offen offset:1024 nt lds\n"
    "buffer_load_dwordx4 v74, s[12:15], s52 offen offset:2048 nt lds\n"
    "buffer_load_dwordx4 v75, s[12:15], s52 offen offset:3072 nt lds\n"
    "s_add_u32 m0, s99, 90112\n"
    "s_add_u32 s52, s54, 24576\n"
    "buffer_load_dwordx4 v72, s[12:15], s52 offen offset:0 nt lds\n"
    "buffer_load_dwordx4 v73, s[12:15], s52 offen offset:1024 nt lds\n"
    "buffer_load_dwordx4 v74, s[12:15], s52 offen offset:2048 nt lds\n"
    "buffer_load_dwordx4 v75, s[12:15], s52 offen offset:3072 nt lds\n"
    "s_add_u32 m0, s99, 94208\n"
    "s_add_u32 s52, s54, 28672\n"
    "buffer_load_dwordx4 v72, s[12:15], s52 offen offset:0 nt lds\n"
    "buffer_load_dwordx4 v73, s[12:15], s52 offen offset:1024 nt lds\n"
    "buffer_load_dwordx4 v74, s[12:15], s52 offen offset:2048 nt lds\n"
    "buffer_load_dwordx4 v75, s[12:15], s52 offen offset:3072 nt lds\n"
    "s_waitcnt vmcnt(63)\n"
    "s_barrier\n"
    "s_add_u32 m0, s99, 98304\n"
    "s_add_u32 s52, s54, 32768\n"
    "buffer_load_dwordx4 v72, s[12:15], s52 offen offset:0 nt lds\n"
    "buffer_load_dwordx4 v73, s[12:15], s52 offen offset:1024 nt lds\n"
    "buffer_load_dwordx4 v74, s[12:15], s52 offen offset:2048 nt lds\n"
    "buffer_load_dwordx4 v75, s[12:15], s52 offen offset:3072 nt lds\n"
    "s_add_u32 m0, s99, 102400\n"
    "s_add_u32 s52, s54, 36864\n"
    "buffer_load_dwordx4 v72, s[12:15], s52 offen offset:0 nt lds\n"
    "buffer_load_dwordx4 v73, s[12:15], s52 offen offset:1024 nt lds\n"
    "buffer_load_dwordx4 v74, s[12:15], s52 offen offset:2048 nt lds\n"
    "buffer_load_dwordx4 v75, s[12:15], s52 offen offset:3072 nt lds\n"
    "s_add_u32 m0, s99, 106496\n"
    "s_add_u32 s52, s54, 40960\n"
    "buffer_load_dwordx4 v72, s[12:15], s52 offen offset:0 nt lds\n"
    "buffer_load_dwordx4 v73, s[12:15], s52 offen offset:1024 nt lds\n"
    "buffer_load_dwordx4 v74, s[12:15], s52 offen offset:2048 nt lds\n"
    "buffer_load_dwordx4 v75, s[12:15], s52 offen offset:3072 nt lds\n"
    "s_add_u32 m0, s99, 110592\n"
    "s_add_u32 s52, s54, 45056\n"
    "buffer_load_dwordx4 v72, s[12:15], s52 offen offset:0 nt lds\n"
    "buffer_load_dwordx4 v73, s[12:15], s52 offen offset:1024 nt lds\n"
    "buffer_load_dwordx4 v74, s[12:15], s52 offen offset:2048 nt lds\n"
    "buffer_load_dwordx4 v75, s[12:15], s52 offen offset:3072 nt lds\n"
    "s_add_u32 m0, s99, 114688\n"
    "s_add_u32 s52, s54, 49152\n"
    "buffer_load_dwordx4 v72, s[12:15], s52 offen offset:0 nt lds\n"
    "buffer_load_dwordx4 v73, s[12:15], s52 offen offset:1024 nt lds\n"
    "buffer_load_dwordx4 v74, s[12:15], s52 offen offset:2048 nt lds\n"
    "buffer_load_dwordx4 v75, s[12:15], s52 offen offset:3072 nt lds\n"
    "s_add_u32 m0, s99, 118784\n"
    "s_add_u32 s52, s54, 53248\n"
    "buffer_load_dwordx4 v72, s[12:15], s52 offen offset:0 nt lds\n"
    "buffer_load_dwordx4 v73, s[12:15], s52 offen offset:1024 nt lds\n"
    "buffer_load_dwordx4 v74, s[12:15], s52 offen offset:2048 nt lds\n"
    "buffer_load_dwordx4 v75, s[12:15], s52 offen offset:3072 nt lds\n"
    "s_add_u32 m0, s99, 122880\n"
    "s_add_u32 s52, s54, 57344\n"
    "buffer_load_dwordx4 v72, s[12:15], s52 offen offset:0 nt lds\n"
    "buffer_load_dwordx4 v73, s[12:15], s52 offen offset:1024 nt lds\n"
    "buffer_load_dwordx4 v74, s[12:15], s52 offen offset:2048 nt lds\n"
    "buffer_load_dwordx4 v75, s[12:15], s52 offen offset:3072 nt lds\n"
    "s_add_u32 m0, s99, 126976\n"
    "s_add_u32 s52, s54, 61440\n"
    "buffer_load_dwordx4 v72, s[12:15], s52 offen offset:0 nt lds\n"
    "buffer_load_dwordx4 v73, s[12:15], s52 offen offset:1024 nt lds\n"
    "buffer_load_dwordx4 v74, s[12:15], s52 offen offset:2048 nt lds\n"
    "buffer_load_dwordx4 v75, s[12:15], s52 offen offset:3072 nt lds\n"
    "s_waitcnt vmcnt(63)\n"
    "s_barrier\n"
    "s_add_u32 m0, s99, 0\n"
    "s_add_u32 s52, s54, 65536\n"
    "buffer_load_dwordx4 v64, s[8:11], s52 offen offset:0 nt lds\n"
    "buffer_load_dwordx4 v65, s[8:11], s52 offen offset:1024 nt lds\n"
    "buffer_load_dwordx4 v66, s[8:11], s52 offen offset:2048 nt lds\n"
    "buffer_load_dwordx4 v67, s[8:11], s52 offen offset:3072 nt lds\n"
    "s_add_u32 m0, s99, 4096\n"
    "s_add_u32 s52, s54, 69632\n"
    "buffer_load_dwordx4 v68, s[8:11], s52 offen offset:0 nt lds\n"
    "buffer_load_dwordx4 v69, s[8:11], s52 offen offset:1024 nt lds\n"
    "buffer_load_dwordx4 v70, s[8:11], s52 offen offset:2048 nt lds\n"
    "buffer_load_dwordx4 v71, s[8:11], s52 offen offset:3072 nt lds\n"
    "s_add_u32 m0, s99, 8192\n"
    "s_add_u32 s52, s54, 73728\n"
    "buffer_load_dwordx4 v64, s[8:11], s52 offen offset:0 nt lds\n"
    "buffer_load_dwordx4 v65, s[8:11], s52 offen offset:1024 nt lds\n"
    "buffer_load_dwordx4 v66, s[8:11], s52 offen offset:2048 nt lds\n"
    "buffer_load_dwordx4 v67, s[8:11], s52 offen offset:3072 nt lds\n"
    "s_add_u32 m0, s99, 12288\n"
    "s_add_u32 s52, s54, 77824\n"
    "buffer_load_dwordx4 v68, s[8:11], s52 offen offset:0 nt lds\n"
    "buffer_load_dwordx4 v69, s[8:11], s52 offen offset:1024 nt lds\n"
    "buffer_load_dwordx4 v70, s[8:11], s52 offen offset:2048 nt lds\n"
    "buffer_load_dwordx4 v71, s[8:11], s52 offen offset:3072 nt lds\n"
    "s_waitcnt vmcnt(63)\n"
    "s_barrier\n"
    "s_add_u32 m0, s99, 16384\n"
    "s_add_u32 s52, s54, 81920\n"
    "buffer_load_dwordx4 v64, s[8:11], s52 offen offset:0 nt lds\n"
    "buffer_load_dwordx4 v65, s[8:11], s52 offen offset:1024 nt lds\n"
    "buffer_load_dwordx4 v66, s[8:11], s52 offen offset:2048 nt lds\n"
    "buffer_load_dwordx4 v67, s[8:11], s52 offen offset:3072 nt lds\n"
    "s_add_u32 m0, s99, 20480\n"
    "s_add_u32 s52, s54, 86016\n"
    "buffer_load_dwordx4 v68, s[8:11], s52 offen offset:0 nt lds\n"
    "buffer_load_dwordx4 v69, s[8:11], s52 offen offset:1024 nt lds\n"
    "buffer_load_dwordx4 v70, s[8:11], s52 offen offset:2048 nt lds\n"
    "buffer_load_dwordx4 v71, s[8:11], s52 offen offset:3072 nt lds\n"
    "s_add_u32 m0, s99, 24576\n"
    "s_add_u32 s52, s54, 90112\n"
    "buffer_load_dwordx4 v64, s[8:11], s52 offen offset:0 nt lds\n"
    "buffer_load_dwordx4 v65, s[8:11], s52 offen offset:1024 nt lds\n"
    "buffer_load_dwordx4 v66, s[8:11], s52 offen offset:2048 nt lds\n"
    "buffer_load_dwordx4 v67, s[8:11], s52 offen offset:3072 nt lds\n"
    "s_add_u32 m0, s99, 28672\n"
    "s_add_u32 s52, s54, 94208\n"
    "buffer_load_dwordx4 v68, s[8:11], s52 offen offset:0 nt lds\n"
    "buffer_load_dwordx4 v69, s[8:11], s52 offen offset:1024 nt lds\n"
    "buffer_load_dwordx4 v70, s[8:11], s52 offen offset:2048 nt lds\n"
    "buffer_load_dwordx4 v71, s[8:11], s52 offen offset:3072 nt lds\n"
    "s_waitcnt vmcnt(63)\n"
    "s_barrier\n"
    "s_add_u32 m0, s99, 32768\n"
    "s_add_u32 s52, s54, 98304\n"
    "buffer_load_dwordx4 v64, s[8:11], s52 offen offset:0 nt lds\n"
    "buffer_load_dwordx4 v65, s[8:11], s52 offen offset:1024 nt lds\n"
    "buffer_load_dwordx4 v66, s[8:11], s52 offen offset:2048 nt lds\n"
    "buffer_load_dwordx4 v67, s[8:11], s52 offen offset:3072 nt lds\n"
    "s_add_u32 m0, s99, 36864\n"
    "s_add_u32 s52, s54, 102400\n"
    "buffer_load_dwordx4 v68, s[8:11], s52 offen offset:0 nt lds\n"
    "buffer_load_dwordx4 v69, s[8:11], s52 offen offset:1024 nt lds\n"
    "buffer_load_dwordx4 v70, s[8:11], s52 offen offset:2048 nt lds\n"
    "buffer_load_dwordx4 v71, s[8:11], s52 offen offset:3072 nt lds\n"
    "s_add_u32 m0, s99, 40960\n"
    "s_add_u32 s52, s54, 106496\n"
    "buffer_load_dwordx4 v64, s[8:11], s52 offen offset:0 nt lds\n"
    "buffer_load_dwordx4 v65, s[8:11], s52 offen offset:1024 nt lds\n"
    "buffer_load_dwordx4 v66, s[8:11], s52 offen offset:2048 nt lds\n"
    "buffer_load_dwordx4 v67, s[8:11], s52 offen offset:3072 nt lds\n"
    "s_add_u32 m0, s99, 45056\n"
    "s_add_u32 s52, s54, 110592\n"
    "buffer_load_dwordx4 v68, s[8:11], s52 offen offset:0 nt lds\n"
    "buffer_load_dwordx4 v69, s[8:11], s52 offen offset:1024 nt lds\n"
    "buffer_load_dwordx4 v70, s[8:11], s52 offen offset:2048 nt lds\n"
    "buffer_load_dwordx4 v71, s[8:11], s52 offen offset:3072 nt lds\n"
    "s_add_u32 m0, s99, 49152\n"
    "s_add_u32 s52, s54, 114688\n"
    "buffer_load_dwordx4 v64, s[8:11], s52 offen offset:0 nt lds\n"
    "buffer_load_dwordx4 v65, s[8:11], s52 offen offset:1024 nt lds\n"
    "buffer_load_dwordx4 v66, s[8:11], s52 offen offset:2048 nt lds\n"
    "buffer_load_dwordx4 v67, s[8:11], s52 offen offset:3072 nt lds\n"
    "s_add_u32 m0, s99, 53248\n"
    "s_add_u32 s52, s54, 118784\n"
    "buffer_load_dwordx4 v68, s[8:11], s52 offen offset:0 nt lds\n"
    "buffer_load_dwordx4 v69, s[8:11], s52 offen offset:1024 nt lds\n"
    "buffer_load_dwordx4 v70, s[8:11], s52 offen offset:2048 nt lds\n"
    "buffer_load_dwordx4 v71, s[8:11], s52 offen offset:3072 nt lds\n"
    "s_add_u32 m0, s99, 57344\n"
    "s_add_u32 s52, s54, 122880\n"
    "buffer_load_dwordx4 v64, s[8:11], s52 offen offset:0 nt lds\n"
    "buffer_load_dwordx4 v65, s[8:11], s52 offen offset:1024 nt lds\n"
    "buffer_load_dwordx4 v66, s[8:11], s52 offen offset:2048 nt lds\n"
    "buffer_load_dwordx4 v67, s[8:11], s52 offen offset:3072 nt lds\n"
    "s_add_u32 m0, s99, 61440\n"
    "s_add_u32 s52, s54, 126976\n"
    "buffer_load_dwordx4 v68, s[8:11], s52 offen offset:0 nt lds\n"
    "buffer_load_dwordx4 v69, s[8:11], s52 offen offset:1024 nt lds\n"
    "buffer_load_dwordx4 v70, s[8:11], s52 offen offset:2048 nt lds\n"
    "buffer_load_dwordx4 v71, s[8:11], s52 offen offset:3072 nt lds\n"
    "s_mul_i32 s48, s62, 229376\n"
    "s_add_u32 s20, s72, s48\n"
    "s_addc_u32 s21, s73, 0\n"
    "s_and_b32 s21, s21, 0xffff\n"
    "s_mov_b32 s22, 229376\n"
    "s_mov_b32 s23, 0x20000\n"
    "v_lshrrev_b32 v40, 3, v1\n"
    "v_mul_u32_u24 v40, 896, v40\n"
    "v_lshlrev_b32 v42, 1, v3\n"
    "v_and_b32 v41, 7, v1\n"
    "v_xor_b32 v42, v42, v41\n"
    "v_lshl_add_u32 v42, v42, 4, v40\n"
    "v_add_u32 v80, 0, v42\n"
    "v_add_u32 v81, 6144, v42\n"
    "v_add_u32 v82, 12288, v42\n"
    "v_add_u32 v83, 18432, v42\n"
    "s_lshl_b32 s49, s60, 3\n"
    "s_add_u32 s49, s49, s61\n"
    "s_mul_i32 s48, s49, 5504\n"
    "s_add_u32 s48, s48, 33554432\n"
    "s_add_u32 s36, s78, s48\n"
    "s_addc_u32 s37, s79, 0\n"
    "s_and_b32 s37, s37, 0xffff\n"
    "s_mov_b32 s38, 5504\n"
    "s_mov_b32 s39, 0x20000\n"
    "s_mul_i32 s48, s49, 2064384\n"
    "s_add_u32 s40, s78, s48\n"
    "s_addc_u32 s41, s79, 0\n"
    "s_and_b32 s41, s41, 0xffff\n"
    "s_mov_b32 s42, 2064384\n"
    "s_mov_b32 s43, 0x20000\n"
    "s_mul_i32 s50, s62, 129024\n"
    "s_add_u32 s48, s48, s50\n"
    "s_add_u32 s28, s78, s48\n"
    "s_addc_u32 s29, s79, 0\n"
    "s_and_b32 s29, s29, 0xffff\n"
    "s_mov_b32 s30, 129024\n"
    "s_mov_b32 s31, 0x20000\n"
    "s_mul_i32 s48, s61, 258048\n"
    "s_mul_i32 s49, s60, 896\n"
    "s_add_u32 s48, s48, s49\n"
    "s_add_u32 s44, s76, s48\n"
    "s_addc_u32 s45, s77, 0\n"
    "s_and_b32 s45, s45, 0xffff\n"
    "s_mov_b32 s46, 258048\n"
    "s_mov_b32 s47, 0x20000\n"
    "s_sub_u32 s46, s46, s49\n"
    "v_lshlrev_b32 v36, 4, v1\n"
    "v_lshlrev_b32 v6, 3, v2\n"
    "v_mul_u32_u24 v7, 1792, v2\n"
    "v_lshl_add_u32 v7, v3, 4, v7\n"
    "v_min_u32 v26, 1, v1\n"
    "v_lshlrev_b32 v26, 30, v26\n"
    "v_and_b32 v52, 1, v2\n"
    "v_lshlrev_b32 v52, 4, v52\n"
    "v_mov_b32 v53, 0x3f80\n"
    "v_lshlrev_b32 v52, v52, v53\n"
    "v_bfe_u32 v53, v2, 1, 1\n"
    "v_lshrrev_b32 v54, 3, v2\n"
    "v_lshl_add_u32 v53, v54, 1, v53\n"
    "v_bfe_u32 v54, v2, 2, 1\n"
    "v_lshl_add_u32 v54, v54, 1, 0\n"
    "v_cmp_eq_u32 vcc, v54, v3\n"
    "s_nop 1\n"
    "v_cndmask_b32 v55, 0, v52, vcc\n"
    "v_cmp_eq_u32 vcc, 0, v53\n"
    "s_nop 1\n"
    "v_cndmask_b32 v56, 0, v55, vcc\n"
    "v_cmp_eq_u32 vcc, 1, v53\n"
    "s_nop 1\n"
    "v_cndmask_b32 v57, 0, v55, vcc\n"
    "v_cmp_eq_u32 vcc, 2, v53\n"
    "s_nop 1\n"
    "v_cndmask_b32 v58, 0, v55, vcc\n"
    "v_cmp_eq_u32 vcc, 3, v53\n"
    "s_nop 1\n"
    "v_cndmask_b32 v59, 0, v55, vcc\n"
    "v_bfe_u32 v54, v2, 2, 1\n"
    "v_lshl_add_u32 v54, v54, 1, 1\n"
    "v_cmp_eq_u32 vcc, v54, v3\n"
    "s_nop 1\n"
    "v_cndmask_b32 v55, 0, v52, vcc\n"
    "v_cmp_eq_u32 vcc, 0, v53\n"
    "s_nop 1\n"
    "v_cndmask_b32 v60, 0, v55, vcc\n"
    "v_cmp_eq_u32 vcc, 1, v53\n"
    "s_nop 1\n"
    "v_cndmask_b32 v61, 0, v55, vcc\n"
    "v_cmp_eq_u32 vcc, 2, v53\n"
    "s_nop 1\n"
    "v_cndmask_b32 v62, 0, v55, vcc\n"
    "v_cmp_eq_u32 vcc, 3, v53\n"
    "s_nop 1\n"
    "v_cndmask_b32 v63, 0, v55, vcc\n"
    "s_mov_b32 s53, 131072\n"
    "s_mov_b32 s57, 12\n"
    "Lld_%=:\n"
    "s_and_b32 s55, s53, 0x8000\n"
    "s_add_u32 s55, s55, s99\n"
    "s_sub_u32 s54, s53, 65536\n"
    "s_waitcnt vmcnt(32)\n"
    "s_barrier\n"
    "s_sleep 3\n"
    "s_add_u32 m0, s55, 65536\n"
    "s_add_u32 s52, s54, 0\n"
    "buffer_load_dwordx4 v72, s[12:15], s52 offen offset:0 nt lds\n"
    "buffer_load_dwordx4 v73, s[12:15], s52 offen offset:1024 nt lds\n"
    "buffer_load_dwordx4 v74, s[12:15], s52 offen offset:2048 nt lds\n"
    "buffer_load_dwordx4 v75, s[12:15], s52 offen offset:3072 nt lds\n"
    "s_sleep 3\n"
    "s_add_u32 m0, s55, 69632\n"
    "s_add_u32 s52, s54, 4096\n"
    "buffer_load_dwordx4 v72, s[12:15], s52 offen offset:0 nt lds\n"
    "buffer_load_dwordx4 v73, s[12:15], s52 offen offset:1024 nt lds\n"
    "buffer_load_dwordx4 v74, s[12:15], s52 offen offset:2048 nt lds\n"
    "buffer_load_dwordx4 v75, s[12:15], s52 offen offset:3072 nt lds\n"
    "s_sleep 3\n"
    "s_add_u32 m0, s55, 73728\n"
    "s_add_u32 s52, s54, 8192\n"
    "buffer_load_dwordx4 v72, s[12:15], s52 offen offset:0 nt lds\n"
    "buffer_load_dwordx4 v73, s[12:15], s52 offen offset:1024 nt lds\n"
    "buffer_load_dwordx4 v74, s[12:15], s52 offen offset:2048 nt lds\n"
    "buffer_load_dwordx4 v75, s[12:15], s52 offen offset:3072 nt lds\n"
    "s_sleep 3\n"
    "s_add_u32 m0, s55, 77824\n"
    "s_add_u32 s52, s54, 12288\n"
    "buffer_load_dwordx4 v72, s[12:15], s52 offen offset:0 nt lds\n"
    "buffer_load_dwordx4 v73, s[12:15], s52 offen offset:1024 nt lds\n"
    "buffer_load_dwordx4 v74, s[12:15], s52 offen offset:2048 nt lds\n"
    "buffer_load_dwordx4 v75, s[12:15], s52 offen offset:3072 nt lds\n"
    "s_sleep 3\n"
    "s_add_u32 m0, s55, 81920\n"
    "s_add_u32 s52, s54, 16384\n"
    "buffer_load_dwordx4 v72, s[12:15], s52 offen offset:0 nt lds\n"
    "buffer_load_dwordx4 v73, s[12:15], s52 offen offset:1024 nt lds\n"
    "buffer_load_dwordx4 v74, s[12:15], s52 offen offset:2048 nt lds\n"
    "buffer_load_dwordx4 v75, s[12:15], s52 offen offset:3072 nt lds\n"
    "s_sleep 3\n"
    "s_add_u32 m0, s55, 86016\n"
    "s_add_u32 s52, s54, 20480\n"
    "buffer_load_dwordx4 v72, s[12:15], s52 offen offset:0 nt lds\n"
    "buffer_load_dwordx4 v73, s[12:15], s52 offen offset:1024 nt lds\n"
    "buffer_load_dwordx4 v74, s[12:15], s52 offen offset:2048 nt lds\n"
    "buffer_load_dwordx4 v75, s[12:15], s52 offen offset:3072 nt lds\n"
    "s_sleep 3\n"
    "s_add_u32 m0, s55, 90112\n"
    "s_add_u32 s52, s54, 24576\n"
    "buffer_load_dwordx4 v72, s[12:15], s52 offen offset:0 nt lds\n"
    "buffer_load_dwordx4 v73, s[12:15], s52 offen offset:1024 nt lds\n"
    "buffer_load_dwordx4 v74, s[12:15], s52 offen offset:2048 nt lds\n"
    "buffer_load_dwordx4 v75, s[12:15], s52 offen offset:3072 nt lds\n"
    "s_sleep 3\n"
    "s_add_u32 m0, s55, 94208\n"
    "s_add_u32 s52, s54, 28672\n"
    "buffer_load_dwordx4 v72, s[12:15], s52 offen offset:0 nt lds\n"
    "buffer_load_dwordx4 v73, s[12:15], s52 offen offset:1024 nt lds\n"
    "buffer_load_dwordx4 v74, s[12:15], s52 offen offset:2048 nt lds\n"
    "buffer_load_dwordx4 v75, s[12:15], s52 offen offset:3072 nt lds\n"
    "s_waitcnt vmcnt(63)\n"
    "s_barrier\n"
    "s_sleep 3\n"
    "s_add_u32 m0, s55, 0\n"
    "s_add_u32 s52, s53, 0\n"
    "buffer_load_dwordx4 v64, s[8:11], s52 offen offset:0 nt lds\n"
    "buffer_load_dwordx4 v65, s[8:11], s52 offen offset:1024 nt lds\n"
    "buffer_load_dwordx4 v66, s[8:11], s52 offen offset:2048 nt lds\n"
    "buffer_load_dwordx4 v67, s[8:11], s52 offen offset:3072 nt lds\n"
    "s_sleep 3\n"
    "s_add_u32 m0, s55, 4096\n"
    "s_add_u32 s52, s53, 4096\n"
    "buffer_load_dwordx4 v68, s[8:11], s52 offen offset:0 nt lds\n"
    "buffer_load_dwordx4 v69, s[8:11], s52 offen offset:1024 nt lds\n"
    "buffer_load_dwordx4 v70, s[8:11], s52 offen offset:2048 nt lds\n"
    "buffer_load_dwordx4 v71, s[8:11], s52 offen offset:3072 nt lds\n"
    "s_sleep 3\n"
    "s_add_u32 m0, s55, 8192\n"
    "s_add_u32 s52, s53, 8192\n"
    "buffer_load_dwordx4 v64, s[8:11], s52 offen offset:0 nt lds\n"
    "buffer_load_dwordx4 v65, s[8:11], s52 offen offset:1024 nt lds\n"
    "buffer_load_dwordx4 v66, s[8:11], s52 offen offset:2048 nt lds\n"
    "buffer_load_dwordx4 v67, s[8:11], s52 offen offset:3072 nt lds\n"
    "s_sleep 3\n"
    "s_add_u32 m0, s55, 12288\n"
    "s_add_u32 s52, s53, 12288\n"
    "buffer_load_dwordx4 v68, s[8:11], s52 offen offset:0 nt lds\n"
    "buffer_load_dwordx4 v69, s[8:11], s52 offen offset:1024 nt lds\n"
    "buffer_load_dwordx4 v70, s[8:11], s52 offen offset:2048 nt lds\n"
    "buffer_load_dwordx4 v71, s[8:11], s52 offen offset:3072 nt lds\n"
    "s_sleep 3\n"
    "s_add_u32 m0, s55, 16384\n"
    "s_add_u32 s52, s53, 16384\n"
    "buffer_load_dwordx4 v64, s[8:11], s52 offen offset:0 nt lds\n"
    "buffer_load_dwordx4 v65, s[8:11], s52 offen offset:1024 nt lds\n"
    "buffer_load_dwordx4 v66, s[8:11], s52 offen offset:2048 nt lds\n"
    "buffer_load_dwordx4 v67, s[8:11], s52 offen offset:3072 nt lds\n"
    "s_sleep 3\n"
    "s_add_u32 m0, s55, 20480\n"
    "s_add_u32 s52, s53, 20480\n"
    "buffer_load_dwordx4 v68, s[8:11], s52 offen offset:0 nt lds\n"
    "buffer_load_dwordx4 v69, s[8:11], s52 offen offset:1024 nt lds\n"
    "buffer_load_dwordx4 v70, s[8:11], s52 offen offset:2048 nt lds\n"
    "buffer_load_dwordx4 v71, s[8:11], s52 offen offset:3072 nt lds\n"
    "s_sleep 3\n"
    "s_add_u32 m0, s55, 24576\n"
    "s_add_u32 s52, s53, 24576\n"
    "buffer_load_dwordx4 v64, s[8:11], s52 offen offset:0 nt lds\n"
    "buffer_load_dwordx4 v65, s[8:11], s52 offen offset:1024 nt lds\n"
    "buffer_load_dwordx4 v66, s[8:11], s52 offen offset:2048 nt lds\n"
    "buffer_load_dwordx4 v67, s[8:11], s52 offen offset:3072 nt lds\n"
    "s_sleep 3\n"
    "s_add_u32 m0, s55, 28672\n"
    "s_add_u32 s52, s53, 28672\n"
    "buffer_load_dwordx4 v68, s[8:11], s52 offen offset:0 nt lds\n"
    "buffer_load_dwordx4 v69, s[8:11], s52 offen offset:1024 nt lds\n"
    "buffer_load_dwordx4 v70, s[8:11], s52 offen offset:2048 nt lds\n"
    "buffer_load_dwordx4 v71, s[8:11], s52 offen offset:3072 nt lds\n"
    "s_add_u32 s53, s53, 32768\n"
    "s_sub_u32 s57, s57, 1\n"
    "s_cmp_lg_u32 s57, 0\n"
    "s_cbranch_scc1 Lld_%=\n"
    "s_mov_b32 s54, 0\n"
    "s_lshl_b32 s53, s35, 7\n"
    "s_waitcnt vmcnt(63)\n"
    "s_barrier\n"
    "s_add_u32 m0, s99, 65536\n"
    "s_add_u32 s52, s54, 458752\n"
    "buffer_load_dwordx4 v72, s[12:15], s52 offen offset:0 nt lds\n"
    "buffer_load_dwordx4 v73, s[12:15], s52 offen offset:1024 nt lds\n"
    "buffer_load_dwordx4 v74, s[12:15], s52 offen offset:2048 nt lds\n"
    "buffer_load_dwordx4 v75, s[12:15], s52 offen offset:3072 nt lds\n"
    "s_add_u32 m0, s99, 69632\n"
    "s_add_u32 s52, s54, 462848\n"
    "buffer_load_dwordx4 v72, s[12:15], s52 offen offset:0 nt lds\n"
    "buffer_load_dwordx4 v73, s[12:15], s52 offen offset:1024 nt lds\n"
    "buffer_load_dwordx4 v74, s[12:15], s52 offen offset:2048 nt lds\n"
    "buffer_load_dwordx4 v75, s[12:15], s52 offen offset:3072 nt lds\n"
    "s_add_u32 m0, s99, 73728\n"
    "s_add_u32 s52, s54, 466944\n"
    "buffer_load_dwordx4 v72, s[12:15], s52 offen offset:0 nt lds\n"
    "buffer_load_dwordx4 v73, s[12:15], s52 offen offset:1024 nt lds\n"
    "buffer_load_dwordx4 v74, s[12:15], s52 offen offset:2048 nt lds\n"
    "buffer_load_dwordx4 v75, s[12:15], s52 offen offset:3072 nt lds\n"
    "s_add_u32 m0, s99, 77824\n"
    "s_add_u32 s52, s54, 471040\n"
    "buffer_load_dwordx4 v72, s[12:15], s52 offen offset:0 nt lds\n"
    "buffer_load_dwordx4 v73, s[12:15], s52 offen offset:1024 nt lds\n"
    "buffer_load_dwordx4 v74, s[12:15], s52 offen offset:2048 nt lds\n"
    "buffer_load_dwordx4 v75, s[12:15], s52 offen offset:3072 nt lds\n"
    "s_add_u32 m0, s99, 81920\n"
    "s_add_u32 s52, s54, 475136\n"
    "buffer_load_dwordx4 v72, s[12:15], s52 offen offset:0 nt lds\n"
    "buffer_load_dwordx4 v73, s[12:15], s52 offen offset:1024 nt lds\n"
    "buffer_load_dwordx4 v74, s[12:15], s52 offen offset:2048 nt lds\n"
    "buffer_load_dwordx4 v75, s[12:15], s52 offen offset:3072 nt lds\n"
    "s_add_u32 m0, s99, 86016\n"
    "s_add_u32 s52, s54, 479232\n"
    "buffer_load_dwordx4 v72, s[12:15], s52 offen offset:0 nt lds\n"
    "buffer_load_dwordx4 v73, s[12:15], s52 offen offset:1024 nt lds\n"
    "buffer_load_dwordx4 v74, s[12:15], s52 offen offset:2048 nt lds\n"
    "buffer_load_dwordx4 v75, s[12:15], s52 offen offset:3072 nt lds\n"
    "s_add_u32 m0, s99, 90112\n"
    "s_add_u32 s52, s54, 483328\n"
    "buffer_load_dwordx4 v72, s[12:15], s52 offen offset:0 nt lds\n"
    "buffer_load_dwordx4 v73, s[12:15], s52 offen offset:1024 nt lds\n"
    "buffer_load_dwordx4 v74, s[12:15], s52 offen offset:2048 nt lds\n"
    "buffer_load_dwordx4 v75, s[12:15], s52 offen offset:3072 nt lds\n"
    "s_add_u32 m0, s99, 94208\n"
    "s_add_u32 s52, s54, 487424\n"
    "buffer_load_dwordx4 v72, s[12:15], s52 offen offset:0 nt lds\n"
    "buffer_load_dwordx4 v73, s[12:15], s52 offen offset:1024 nt lds\n"
    "buffer_load_dwordx4 v74, s[12:15], s52 offen offset:2048 nt lds\n"
    "buffer_load_dwordx4 v75, s[12:15], s52 offen offset:3072 nt lds\n"
    "s_waitcnt vmcnt(63)\n"
    "s_barrier\n"
    "s_add_u32 m0, s99, 131072\n"
    "s_add_u32 s52, s53, 0\n"
    "buffer_load_dwordx4 v80, s[20:23], s52 offen offset:0  lds\n"
    "buffer_load_dwordx4 v81, s[20:23], s52 offen offset:1024  lds\n"
    "buffer_load_dwordx4 v82, s[20:23], s52 offen offset:2048  lds\n"
    "buffer_load_dwordx4 v83, s[20:23], s52 offen offset:3072  lds\n"
    "s_add_u32 m0, s99, 135168\n"
    "s_add_u32 s52, s53, 28672\n"
    "buffer_load_dwordx4 v80, s[20:23], s52 offen offset:0  lds\n"
    "buffer_load_dwordx4 v81, s[20:23], s52 offen offset:1024  lds\n"
    "buffer_load_dwordx4 v82, s[20:23], s52 offen offset:2048  lds\n"
    "buffer_load_dwordx4 v83, s[20:23], s52 offen offset:3072  lds\n"
    "s_add_u32 m0, s99, 139264\n"
    "s_add_u32 s52, s53, 57344\n"
    "buffer_load_dwordx4 v80, s[20:23], s52 offen offset:0  lds\n"
    "buffer_load_dwordx4 v81, s[20:23], s52 offen offset:1024  lds\n"
    "buffer_load_dwordx4 v82, s[20:23], s52 offen offset:2048  lds\n"
    "buffer_load_dwordx4 v83, s[20:23], s52 offen offset:3072  lds\n"
    "s_add_u32 m0, s99, 143360\n"
    "s_add_u32 s52, s53, 86016\n"
    "buffer_load_dwordx4 v80, s[20:23], s52 offen offset:0  lds\n"
    "buffer_load_dwordx4 v81, s[20:23], s52 offen offset:1024  lds\n"
    "buffer_load_dwordx4 v82, s[20:23], s52 offen offset:2048  lds\n"
    "buffer_load_dwordx4 v83, s[20:23], s52 offen offset:3072  lds\n"
    "s_add_u32 m0, s99, 147456\n"
    "s_add_u32 s52, s53, 114688\n"
    "buffer_load_dwordx4 v80, s[20:23], s52 offen offset:0  lds\n"
    "buffer_load_dwordx4 v81, s[20:23], s52 offen offset:1024  lds\n"
    "buffer_load_dwordx4 v82, s[20:23], s52 offen offset:2048  lds\n"
    "buffer_load_dwordx4 v83, s[20:23], s52 offen offset:3072  lds\n"
    "s_add_u32 m0, s99, 151552\n"
    "s_add_u32 s52, s53, 143360\n"
    "buffer_load_dwordx4 v80, s[20:23], s52 offen offset:0  lds\n"
    "buffer_load_dwordx4 v81, s[20:23], s52 offen offset:1024  lds\n"
    "buffer_load_dwordx4 v82, s[20:23], s52 offen offset:2048  lds\n"
    "buffer_load_dwordx4 v83, s[20:23], s52 offen offset:3072  lds\n"
    "s_add_u32 m0, s99, 155648\n"
    "s_add_u32 s52, s53, 172032\n"
    "buffer_load_dwordx4 v80, s[20:23], s52 offen offset:0  lds\n"
    "buffer_load_dwordx4 v81, s[20:23], s52 offen offset:1024  lds\n"
    "buffer_load_dwordx4 v82, s[20:23], s52 offen offset:2048  lds\n"
    "buffer_load_dwordx4 v83, s[20:23], s52 offen offset:3072  lds\n"
    "s_add_u32 m0, s99, 159744\n"
    "s_add_u32 s52, s53, 200704\n"
    "buffer_load_dwordx4 v80, s[20:23], s52 offen offset:0  lds\n"
    "buffer_load_dwordx4 v81, s[20:23], s52 offen offset:1024  lds\n"
    "buffer_load_dwordx4 v82, s[20:23], s52 offen offset:2048  lds\n"
    "buffer_load_dwordx4 v83, s[20:23], s52 offen offset:3072  lds\n"
    "s_add_u32 s53, s53, 128\n"
    "s_cmp_eq_u32 s53, 896\n"
    "s_cselect_b32 s53, 0, s53\n"
    "s_waitcnt vmcnt(63)\n"
    "s_barrier\n"
    "s_add_u32 m0, s99, 98304\n"
    "s_add_u32 s52, s54, 491520\n"
    "buffer_load_dwordx4 v72, s[12:15], s52 offen offset:0 nt lds\n"
    "buffer_load_dwordx4 v73, s[12:15], s52 offen offset:1024 nt lds\n"
    "buffer_load_dwordx4 v74, s[12:15], s52 offen offset:2048 nt lds\n"
    "buffer_load_dwordx4 v75, s[12:15], s52 offen offset:3072 nt lds\n"
    "s_add_u32 m0, s99, 102400\n"
    "s_add_u32 s52, s54, 495616\n"
    "buffer_load_dwordx4 v72, s[12:15], s52 offen offset:0 nt lds\n"
    "buffer_load_dwordx4 v73, s[12:15], s52 offen offset:1024 nt lds\n"
    "buffer_load_dwordx4 v74, s[12:15], s52 offen offset:2048 nt lds\n"
    "buffer_load_dwordx4 v75, s[12:15], s52 offen offset:3072 nt lds\n"
    "s_add_u32 m0, s99, 106496\n"
    "s_add_u32 s52, s54, 499712\n"
    "buffer_load_dwordx4 v72, s[12:15], s52 offen offset:0 nt lds\n"
    "buffer_load_dwordx4 v73, s[12:15], s52 offen offset:1024 nt lds\n"
    "buffer_load_dwordx4 v74, s[12:15], s52 offen offset:2048 nt lds\n"
    "buffer_load_dwordx4 v75, s[12:15], s52 offen offset:3072 nt lds\n"
    "s_add_u32 m0, s99, 110592\n"
    "s_add_u32 s52, s54, 503808\n"
    "buffer_load_dwordx4 v72, s[12:15], s52 offen offset:0 nt lds\n"
    "buffer_load_dwordx4 v73, s[12:15], s52 offen offset:1024 nt lds\n"
    "buffer_load_dwordx4 v74, s[12:15], s52 offen offset:2048 nt lds\n"
    "buffer_load_dwordx4 v75, s[12:15], s52 offen offset:3072 nt lds\n"
    "s_add_u32 m0, s99, 114688\n"
    "s_add_u32 s52, s54, 507904\n"
    "buffer_load_dwordx4 v72, s[12:15], s52 offen offset:0 nt lds\n"
    "buffer_load_dwordx4 v73, s[12:15], s52 offen offset:1024 nt lds\n"
    "buffer_load_dwordx4 v74, s[12:15], s52 offen offset:2048 nt lds\n"
    "buffer_load_dwordx4 v75, s[12:15], s52 offen offset:3072 nt lds\n"
    "s_add_u32 m0, s99, 118784\n"
    "s_add_u32 s52, s54, 512000\n"
    "buffer_load_dwordx4 v72, s[12:15], s52 offen offset:0 nt lds\n"
    "buffer_load_dwordx4 v73, s[12:15], s52 offen offset:1024 nt lds\n"
    "buffer_load_dwordx4 v74, s[12:15], s52 offen offset:2048 nt lds\n"
    "buffer_load_dwordx4 v75, s[12:15], s52 offen offset:3072 nt lds\n"
    "s_add_u32 m0, s99, 122880\n"
    "s_add_u32 s52, s54, 516096\n"
    "buffer_load_dwordx4 v72, s[12:15], s52 offen offset:0 nt lds\n"
    "buffer_load_dwordx4 v73, s[12:15], s52 offen offset:1024 nt lds\n"
    "buffer_load_dwordx4 v74, s[12:15], s52 offen offset:2048 nt lds\n"
    "buffer_load_dwordx4 v75, s[12:15], s52 offen offset:3072 nt lds\n"
    "s_add_u32 m0, s99, 126976\n"
    "s_add_u32 s52, s54, 520192\n"
    "buffer_load_dwordx4 v72, s[12:15], s52 offen offset:0 nt lds\n"
    "buffer_load_dwordx4 v73, s[12:15], s52 offen offset:1024 nt lds\n"
    "buffer_load_dwordx4 v74, s[12:15], s52 offen offset:2048 nt lds\n"
    "buffer_load_dwordx4 v75, s[12:15], s52 offen offset:3072 nt lds\n"
    "s_waitcnt vmcnt(63)\n"
    "s_barrier\n"
    "v_mov_b32 v38, 5376\n"
    "buffer_load_dwordx2 v[38:39], v38, s[36:39], 0 offen sc1\n"
    "s_add_u32 m0, s99, 0\n"
    "s_add_u32 s52, s53, 0\n"
    "buffer_load_dwordx4 v80, s[20:23], s52 offen offset:0  lds\n"
    "buffer_load_dwordx4 v81, s[20:23], s52 offen offset:1024  lds\n"
    "buffer_load_dwordx4 v82, s[20:23], s52 offen offset:2048  lds\n"
    "buffer_load_dwordx4 v83, s[20:23], s52 offen offset:3072  lds\n"
    "s_add_u32 m0, s99, 4096\n"
    "s_add_u32 s52, s53, 28672\n"
    "buffer_load_dwordx4 v80, s[20:23], s52 offen offset:0  lds\n"
    "buffer_load_dwordx4 v81, s[20:23], s52 offen offset:1024  lds\n"
    "buffer_load_dwordx4 v82, s[20:23], s52 offen offset:2048  lds\n"
    "buffer_load_dwordx4 v83, s[20:23], s52 offen offset:3072  lds\n"
    "s_add_u32 m0, s99, 8192\n"
    "s_add_u32 s52, s53, 57344\n"
    "buffer_load_dwordx4 v80, s[20:23], s52 offen offset:0  lds\n"
    "buffer_load_dwordx4 v81, s[20:23], s52 offen offset:1024  lds\n"
    "buffer_load_dwordx4 v82, s[20:23], s52 offen offset:2048  lds\n"
    "buffer_load_dwordx4 v83, s[20:23], s52 offen offset:3072  lds\n"
    "s_add_u32 m0, s99, 12288\n"
    "s_add_u32 s52, s53, 86016\n"
    "buffer_load_dwordx4 v80, s[20:23], s52 offen offset:0  lds\n"
    "buffer_load_dwordx4 v81, s[20:23], s52 offen offset:1024  lds\n"
    "buffer_load_dwordx4 v82, s[20:23], s52 offen offset:2048  lds\n"
    "buffer_load_dwordx4 v83, s[20:23], s52 offen offset:3072  lds\n"
    "s_add_u32 m0, s99, 16384\n"
    "s_add_u32 s52, s53, 114688\n"
    "buffer_load_dwordx4 v80, s[20:23], s52 offen offset:0  lds\n"
    "buffer_load_dwordx4 v81, s[20:23], s52 offen offset:1024  lds\n"
    "buffer_load_dwordx4 v82, s[20:23], s52 offen offset:2048  lds\n"
    "buffer_load_dwordx4 v83, s[20:23], s52 offen offset:3072  lds\n"
    "s_add_u32 m0, s99, 20480\n"
    "s_add_u32 s52, s53, 143360\n"
    "buffer_load_dwordx4 v80, s[20:23], s52 offen offset:0  lds\n"
    "buffer_load_dwordx4 v81, s[20:23], s52 offen offset:1024  lds\n"
    "buffer_load_dwordx4 v82, s[20:23], s52 offen offset:2048  lds\n"
    "buffer_load_dwordx4 v83, s[20:23], s52 offen offset:3072  lds\n"
    "s_add_u32 m0, s99, 24576\n"
    "s_add_u32 s52, s53, 172032\n"
    "buffer_load_dwordx4 v80, s[20:23], s52 offen offset:0  lds\n"
    "buffer_load_dwordx4 v81, s[20:23], s52 offen offset:1024  lds\n"
    "buffer_load_dwordx4 v82, s[20:23], s52 offen offset:2048  lds\n"
    "buffer_load_dwordx4 v83, s[20:23], s52 offen offset:3072  lds\n"
    "s_add_u32 m0, s99, 28672\n"
    "s_add_u32 s52, s53, 200704\n"
    "buffer_load_dwordx4 v80, s[20:23], s52 offen offset:0  lds\n"
    "buffer_load_dwordx4 v81, s[20:23], s52 offen offset:1024  lds\n"
    "buffer_load_dwordx4 v82, s[20:23], s52 offen offset:2048  lds\n"
    "buffer_load_dwordx4 v83, s[20:23], s52 offen offset:3072  lds\n"
    "s_add_u32 s53, s53, 128\n"
    "s_cmp_eq_u32 s53, 896\n"
    "s_cselect_b32 s53, 0, s53\n"
    "s_add_u32 m0, s99, 32768\n"
    "s_add_u32 s52, s53, 0\n"
    "buffer_load_dwordx4 v80, s[20:23], s52 offen offset:0  lds\n"
    "buffer_load_dwordx4 v81, s[20:23], s52 offen offset:1024  lds\n"
    "buffer_load_dwordx4 v82, s[20:23], s52 offen offset:2048  lds\n"
    "buffer_load_dwordx4 v83, s[20:23], s52 offen offset:3072  lds\n"
    "s_add_u32 m0, s99, 36864\n"
    "s_add_u32 s52, s53, 28672\n"
    "buffer_load_dwordx4 v80, s[20:23], s52 offen offset:0  lds\n"
    "buffer_load_dwordx4 v81, s[20:23], s52 offen offset:1024  lds\n"
    "buffer_load_dwordx4 v82, s[20:23], s52 offen offset:2048  lds\n"
    "buffer_load_dwordx4 v83, s[20:23], s52 offen offset:3072  lds\n"
    "s_add_u32 m0, s99, 40960\n"
    "s_add_u32 s52, s53, 57344\n"
    "buffer_load_dwordx4 v80, s[20:23], s52 offen offset:0  lds\n"
    "buffer_load_dwordx4 v81, s[20:23], s52 offen offset:1024  lds\n"
    "buffer_load_dwordx4 v82, s[20:23], s52 offen offset:2048  lds\n"
    "buffer_load_dwordx4 v83, s[20:23], s52 offen offset:3072  lds\n"
    "s_add_u32 m0, s99, 45056\n"
    "s_add_u32 s52, s53, 86016\n"
    "buffer_load_dwordx4 v80, s[20:23], s52 offen offset:0  lds\n"
    "buffer_load_dwordx4 v81, s[20:23], s52 offen offset:1024  lds\n"
    "buffer_load_dwordx4 v82, s[20:23], s52 offen offset:2048  lds\n"
    "buffer_load_dwordx4 v83, s[20:23], s52 offen offset:3072  lds\n"
    "s_waitcnt vmcnt(49)\n"
    "s_barrier\n"
    "s_add_u32 m0, s99, 49152\n"
    "s_add_u32 s52, s53, 114688\n"
    "buffer_load_dwordx4 v80, s[20:23], s52 offen offset:0  lds\n"
    "buffer_load_dwordx4 v81, s[20:23], s52 offen offset:1024  lds\n"
    "buffer_load_dwordx4 v82, s[20:23], s52 offen offset:2048  lds\n"
    "buffer_load_dwordx4 v83, s[20:23], s52 offen offset:3072  lds\n"
    "s_add_u32 m0, s99, 53248\n"
    "s_add_u32 s52, s53, 143360\n"
    "buffer_load_dwordx4 v80, s[20:23], s52 offen offset:0  lds\n"
    "buffer_load_dwordx4 v81, s[20:23], s52 offen offset:1024  lds\n"
    "buffer_load_dwordx4 v82, s[20:23], s52 offen offset:2048  lds\n"
    "buffer_load_dwordx4 v83, s[20:23], s52 offen offset:3072  lds\n"
    "s_add_u32 m0, s99, 57344\n"
    "s_add_u32 s52, s53, 172032\n"
    "buffer_load_dwordx4 v80, s[20:23], s52 offen offset:0  lds\n"
    "buffer_load_dwordx4 v81, s[20:23], s52 offen offset:1024  lds\n"
    "buffer_load_dwordx4 v82, s[20:23], s52 offen offset:2048  lds\n"
    "buffer_load_dwordx4 v83, s[20:23], s52 offen offset:3072  lds\n"
    "s_add_u32 m0, s99, 61440\n"
    "s_add_u32 s52, s53, 200704\n"
    "buffer_load_dwordx4 v80, s[20:23], s52 offen offset:0  lds\n"
    "buffer_load_dwordx4 v81, s[20:23], s52 offen offset:1024  lds\n"
    "buffer_load_dwordx4 v82, s[20:23], s52 offen offset:2048  lds\n"
    "buffer_load_dwordx4 v83, s[20:23], s52 offen offset:3072  lds\n"
    "s_add_u32 s53, s53, 128\n"
    "s_cmp_eq_u32 s53, 896\n"
    "s_cselect_b32 s53, 0, s53\n"
    "s_add_u32 m0, s99, 65536\n"
    "s_add_u32 s52, s53, 0\n"
    "buffer_load_dwordx4 v80, s[20:23], s52 offen offset:0  lds\n"
    "buffer_load_dwordx4 v81, s[20:23], s52 offen offset:1024  lds\n"
    "buffer_load_dwordx4 v82, s[20:23], s52 offen offset:2048  lds\n"
    "buffer_load_dwordx4 v83, s[20:23], s52 offen offset:3072  lds\n"
    "s_add_u32 m0, s99, 69632\n"
    "s_add_u32 s52, s53, 28672\n"
    "buffer_load_dwordx4 v80, s[20:23], s52 offen offset:0  lds\n"
    "buffer_load_dwordx4 v81, s[20:23], s52 offen offset:1024  lds\n"
    "buffer_load_dwordx4 v82, s[20:23], s52 offen offset:2048  lds\n"
    "buffer_load_dwordx4 v83, s[20:23], s52 offen offset:3072  lds\n"
    "s_add_u32 m0, s99, 73728\n"
    "s_add_u32 s52, s53, 57344\n"
    "buffer_load_dwordx4 v80, s[20:23], s52 offen offset:0  lds\n"
    "buffer_load_dwordx4 v81, s[20:23], s52 offen offset:1024  lds\n"
    "buffer_load_dwordx4 v82, s[20:23], s52 offen offset:2048  lds\n"
    "buffer_load_dwordx4 v83, s[20:23], s52 offen offset:3072  lds\n"
    "s_add_u32 m0, s99, 77824\n"
    "s_add_u32 s52, s53, 86016\n"
    "buffer_load_dwordx4 v80, s[20:23], s52 offen offset:0  lds\n"
    "buffer_load_dwordx4 v81, s[20:23], s52 offen offset:1024  lds\n"
    "buffer_load_dwordx4 v82, s[20:23], s52 offen offset:2048  lds\n"
    "buffer_load_dwordx4 v83, s[20:23], s52 offen offset:3072  lds\n"
    "s_add_u32 m0, s99, 81920\n"
    "s_add_u32 s52, s53, 114688\n"
    "buffer_load_dwordx4 v80, s[20:23], s52 offen offset:0  lds\n"
    "buffer_load_dwordx4 v81, s[20:23], s52 offen offset:1024  lds\n"
    "buffer_load_dwordx4 v82, s[20:23], s52 offen offset:2048  lds\n"
    "buffer_load_dwordx4 v83, s[20:23], s52 offen offset:3072  lds\n"
    "s_add_u32 m0, s99, 86016\n"
    "s_add_u32 s52, s53, 143360\n"
    "buffer_load_dwordx4 v80, s[20:23], s52 offen offset:0  lds\n"
    "buffer_load_dwordx4 v81, s[20:23], s52 offen offset:1024  lds\n"
    "buffer_load_dwordx4 v82, s[20:23], s52 offen offset:2048  lds\n"
    "buffer_load_dwordx4 v83, s[20:23], s52 offen offset:3072  lds\n"
    "s_add_u32 m0, s99, 90112\n"
    "s_add_u32 s52, s53, 172032\n"
    "buffer_load_dwordx4 v80, s[20:23], s52 offen offset:0  lds\n"
    "buffer_load_dwordx4 v81, s[20:23], s52 offen offset:1024  lds\n"
    "buffer_load_dwordx4 v82, s[20:23], s52 offen offset:2048  lds\n"
    "buffer_load_dwordx4 v83, s[20:23], s52 offen offset:3072  lds\n"
    "s_add_u32 m0, s99, 94208\n"
    "s_add_u32 s52, s53, 200704\n"
    "buffer_load_dwordx4 v80, s[20:23], s52 offen offset:0  lds\n"
    "buffer_load_dwordx4 v81, s[20:23], s52 offen offset:1024  lds\n"
    "buffer_load_dwordx4 v82, s[20:23], s52 offen offset:2048  lds\n"
    "buffer_load_dwordx4 v83, s[20:23], s52 offen offset:3072  lds\n"
    "s_add_u32 s53, s53, 128\n"
    "s_cmp_eq_u32 s53, 896\n"
    "s_cselect_b32 s53, 0, s53\n"
    "s_waitcnt vmcnt(63)\n"
    "v_add_co_u32 v38, vcc, 1, v38\n"
    "s_nop 1\n"
    "v_addc_co_u32 v39, vcc, 0, v39, vcc\n"
    "v_or_b32 v39, 0x80000000, v39\n"
    "s_waitcnt vmcnt(63)\n"
    "s_barrier\n"
    "s_add_u32 m0, s99, 98304\n"
    "s_add_u32 s52, s53, 0\n"
    "buffer_load_dwordx4 v80, s[20:23], s52 offen offset:0  lds\n"
    "buffer_load_dwordx4 v81, s[20:23], s52 offen offset:1024  lds\n"
    "buffer_load_dwordx4 v82, s[20:23], s52 offen offset:2048  lds\n"
    "buffer_load_dwordx4 v83, s[20:23], s52 offen offset:3072  lds\n"
    "s_add_u32 m0, s99, 102400\n"
    "s_add_u32 s52, s53, 28672\n"
    "buffer_load_dwordx4 v80, s[20:23], s52 offen offset:0  lds\n"
    "buffer_load_dwordx4 v81, s[20:23], s52 offen offset:1024  lds\n"
    "buffer_load_dwordx4 v82, s[20:23], s52 offen offset:2048  lds\n"
    "buffer_load_dwordx4 v83, s[20:23], s52 offen offset:3072  lds\n"
    "s_add_u32 m0, s99, 106496\n"
    "s_add_u32 s52, s53, 57344\n"
    "buffer_load_dwordx4 v80, s[20:23], s52 offen offset:0  lds\n"
    "buffer_load_dwordx4 v81, s[20:23], s52 offen offset:1024  lds\n"
    "buffer_load_dwordx4 v82, s[20:23], s52 offen offset:2048  lds\n"
    "buffer_load_dwordx4 v83, s[20:23], s52 offen offset:3072  lds\n"
    "s_add_u32 m0, s99, 110592\n"
    "s_add_u32 s52, s53, 86016\n"
    "buffer_load_dwordx4 v80, s[20:23], s52 offen offset:0  lds\n"
    "buffer_load_dwordx4 v81, s[20:23], s52 offen offset:1024  lds\n"
    "buffer_load_dwordx4 v82, s[20:23], s52 offen offset:2048  lds\n"
    "buffer_load_dwordx4 v83, s[20:23], s52 offen offset:3072  lds\n"
    "s_add_u32 m0, s99, 114688\n"
    "s_add_u32 s52, s53, 114688\n"
    "buffer_load_dwordx4 v80, s[20:23], s52 offen offset:0  lds\n"
    "buffer_load_dwordx4 v81, s[20:23], s52 offen offset:1024  lds\n"
    "buffer_load_dwordx4 v82, s[20:23], s52 offen offset:2048  lds\n"
    "buffer_load_dwordx4 v83, s[20:23], s52 offen offset:3072  lds\n"
    "s_add_u32 m0, s99, 118784\n"
    "s_add_u32 s52, s53, 143360\n"
    "buffer_load_dwordx4 v80, s[20:23], s52 offen offset:0  lds\n"
    "buffer_load_dwordx4 v81, s[20:23], s52 offen offset:1024  lds\n"
    "buffer_load_dwordx4 v82, s[20:23], s52 offen offset:2048  lds\n"
    "buffer_load_dwordx4 v83, s[20:23], s52 offen offset:3072  lds\n"
    "s_add_u32 m0, s99, 122880\n"
    "s_add_u32 s52, s53, 172032\n"
    "buffer_load_dwordx4 v80, s[20:23], s52 offen offset:0  lds\n"
    "buffer_load_dwordx4 v81, s[20:23], s52 offen offset:1024  lds\n"
    "buffer_load_dwordx4 v82, s[20:23], s52 offen offset:2048  lds\n"
    "buffer_load_dwordx4 v83, s[20:23], s52 offen offset:3072  lds\n"
    "s_add_u32 m0, s99, 126976\n"
    "s_add_u32 s52, s53, 200704\n"
    "buffer_load_dwordx4 v80, s[20:23], s52 offen offset:0  lds\n"
    "buffer_load_dwordx4 v81, s[20:23], s52 offen offset:1024  lds\n"
    "buffer_load_dwordx4 v82, s[20:23], s52 offen offset:2048  lds\n"
    "buffer_load_dwordx4 v83, s[20:23], s52 offen offset:3072  lds\n"
    "s_add_u32 s53, s53, 128\n"
    "s_cmp_eq_u32 s53, 896\n"
    "s_cselect_b32 s53, 0, s53\n"
    "s_waitcnt vmcnt(63)\n"
    "s_barrier\n"
    "s_add_u32 m0, s99, 131072\n"
    "s_add_u32 s52, s53, 0\n"
    "buffer_load_dwordx4 v80, s[20:23], s52 offen offset:0  lds\n"
    "buffer_load_dwordx4 v81, s[20:23], s52 offen offset:1024  lds\n"
    "buffer_load_dwordx4 v82, s[20:23], s52 offen offset:2048  lds\n"
    "buffer_load_dwordx4 v83, s[20:23], s52 offen offset:3072  lds\n"
    "s_add_u32 m0, s99, 135168\n"
    "s_add_u32 s52, s53, 28672\n"
    "buffer_load_dwordx4 v80, s[20:23], s52 offen offset:0  lds\n"
    "buffer_load_dwordx4 v81, s[20:23], s52 offen offset:1024  lds\n"
    "buffer_load_dwordx4 v82, s[20:23], s52 offen offset:2048  lds\n"
    "buffer_load_dwordx4 v83, s[20:23], s52 offen offset:3072  lds\n"
    "s_add_u32 m0, s99, 139264\n"
    "s_add_u32 s52, s53, 57344\n"
    "buffer_load_dwordx4 v80, s[20:23], s52 offen offset:0  lds\n"
    "buffer_load_dwordx4 v81, s[20:23], s52 offen offset:1024  lds\n"
    "buffer_load_dwordx4 v82, s[20:23], s52 offen offset:2048  lds\n"
    "buffer_load_dwordx4 v83, s[20:23], s52 offen offset:3072  lds\n"
    "s_add_u32 m0, s99, 143360\n"
    "s_add_u32 s52, s53, 86016\n"
    "buffer_load_dwordx4 v80, s[20:23], s52 offen offset:0  lds\n"
    "buffer_load_dwordx4 v81, s[20:23], s52 offen offset:1024  lds\n"
    "buffer_load_dwordx4 v82, s[20:23], s52 offen offset:2048  lds\n"
    "buffer_load_dwordx4 v83, s[20:23], s52 offen offset:3072  lds\n"
    "s_add_u32 m0, s99, 147456\n"
    "s_add_u32 s52, s53, 114688\n"
    "buffer_load_dwordx4 v80, s[20:23], s52 offen offset:0  lds\n"
    "buffer_load_dwordx4 v81, s[20:23], s52 offen offset:1024  lds\n"
    "buffer_load_dwordx4 v82, s[20:23], s52 offen offset:2048  lds\n"
    "buffer_load_dwordx4 v83, s[20:23], s52 offen offset:3072  lds\n"
    "s_add_u32 m0, s99, 151552\n"
    "s_add_u32 s52, s53, 143360\n"
    "buffer_load_dwordx4 v80, s[20:23], s52 offen offset:0  lds\n"
    "buffer_load_dwordx4 v81, s[20:23], s52 offen offset:1024  lds\n"
    "buffer_load_dwordx4 v82, s[20:23], s52 offen offset:2048  lds\n"
    "buffer_load_dwordx4 v83, s[20:23], s52 offen offset:3072  lds\n"
    "s_add_u32 m0, s99, 155648\n"
    "s_add_u32 s52, s53, 172032\n"
    "buffer_load_dwordx4 v80, s[20:23], s52 offen offset:0  lds\n"
    "buffer_load_dwordx4 v81, s[20:23], s52 offen offset:1024  lds\n"
    "buffer_load_dwordx4 v82, s[20:23], s52 offen offset:2048  lds\n"
    "buffer_load_dwordx4 v83, s[20:23], s52 offen offset:3072  lds\n"
    "s_add_u32 m0, s99, 159744\n"
    "s_add_u32 s52, s53, 200704\n"
    "buffer_load_dwordx4 v80, s[20:23], s52 offen offset:0  lds\n"
    "buffer_load_dwordx4 v81, s[20:23], s52 offen offset:1024  lds\n"
    "buffer_load_dwordx4 v82, s[20:23], s52 offen offset:2048  lds\n"
    "buffer_load_dwordx4 v83, s[20:23], s52 offen offset:3072  lds\n"
    "s_add_u32 s53, s53, 128\n"
    "s_cmp_eq_u32 s53, 896\n"
    "s_cselect_b32 s53, 0, s53\n"
    "s_waitcnt vmcnt(63)\n"
    "s_barrier\n"
    "s_add_u32 m0, s99, 0\n"
    "s_add_u32 s52, s53, 0\n"
    "buffer_load_dwordx4 v80, s[20:23], s52 offen offset:0  lds\n"
    "buffer_load_dwordx4 v81, s[20:23], s52 offen offset:1024  lds\n"
    "buffer_load_dwordx4 v82, s[20:23], s52 offen offset:2048  lds\n"
    "buffer_load_dwordx4 v83, s[20:23], s52 offen offset:3072  lds\n"
    "s_add_u32 m0, s99, 4096\n"
    "s_add_u32 s52, s53, 28672\n"
    "buffer_load_dwordx4 v80, s[20:23], s52 offen offset:0  lds\n"
    "buffer_load_dwordx4 v81, s[20:23], s52 offen offset:1024  lds\n"
    "buffer_load_dwordx4 v82, s[20:23], s52 offen offset:2048  lds\n"
    "buffer_load_dwordx4 v83, s[20:23], s52 offen offset:3072  lds\n"
    "s_add_u32 m0, s99, 8192\n"
    "s_add_u32 s52, s53, 57344\n"
    "buffer_load_dwordx4 v80, s[20:23], s52 offen offset:0  lds\n"
    "buffer_load_dwordx4 v81, s[20:23], s52 offen offset:1024  lds\n"
    "buffer_load_dwordx4 v82, s[20:23], s52 offen offset:2048  lds\n"
    "buffer_load_dwordx4 v83, s[20:23], s52 offen offset:3072  lds\n"
    "s_add_u32 m0, s99, 12288\n"
    "s_add_u32 s52, s53, 86016\n"
    "buffer_load_dwordx4 v80, s[20:23], s52 offen offset:0  lds\n"
    "buffer_load_dwordx4 v81, s[20:23], s52 offen offset:1024  lds\n"
    "buffer_load_dwordx4 v82, s[20:23], s52 offen offset:2048  lds\n"
    "buffer_load_dwordx4 v83, s[20:23], s52 offen offset:3072  lds\n"
    "s_add_u32 m0, s99, 16384\n"
    "s_add_u32 s52, s53, 114688\n"
    "buffer_load_dwordx4 v80, s[20:23], s52 offen offset:0  lds\n"
    "buffer_load_dwordx4 v81, s[20:23], s52 offen offset:1024  lds\n"
    "buffer_load_dwordx4 v82, s[20:23], s52 offen offset:2048  lds\n"
    "buffer_load_dwordx4 v83, s[20:23], s52 offen offset:3072  lds\n"
    "s_add_u32 m0, s99, 20480\n"
    "s_add_u32 s52, s53, 143360\n"
    "buffer_load_dwordx4 v80, s[20:23], s52 offen offset:0  lds\n"
    "buffer_load_dwordx4 v81, s[20:23], s52 offen offset:1024  lds\n"
    "buffer_load_dwordx4 v82, s[20:23], s52 offen offset:2048  lds\n"
    "buffer_load_dwordx4 v83, s[20:23], s52 offen offset:3072  lds\n"
    "s_add_u32 m0, s99, 24576\n"
    "s_add_u32 s52, s53, 172032\n"
    "buffer_load_dwordx4 v80, s[20:23], s52 offen offset:0  lds\n"
    "buffer_load_dwordx4 v81, s[20:23], s52 offen offset:1024  lds\n"
    "buffer_load_dwordx4 v82, s[20:23], s52 offen offset:2048  lds\n"
    "buffer_load_dwordx4 v83, s[20:23], s52 offen offset:3072  lds\n"
    "s_add_u32 m0, s99, 28672\n"
    "s_add_u32 s52, s53, 200704\n"
    "buffer_load_dwordx4 v80, s[20:23], s52 offen offset:0  lds\n"
    "buffer_load_dwordx4 v81, s[20:23], s52 offen offset:1024  lds\n"
    "buffer_load_dwordx4 v82, s[20:23], s52 offen offset:2048  lds\n"
    "buffer_load_dwordx4 v83, s[20:23], s52 offen offset:3072  lds\n"
    "s_add_u32 s53, s53, 128\n"
    "s_cmp_eq_u32 s53, 896\n"
    "s_cselect_b32 s53, 0, s53\n"
    "s_waitcnt vmcnt(0)\n"
    "s_barrier\n"
    "s_mul_i32 s73, s62, 57\n"
    "s_lshr_b32 s73, s73, 9\n"
    "s_mul_i32 s48, s73, 9\n"
    "s_sub_u32 s74, s62, s48\n"
    "s_mov_b32 s75, 5\n"
    "s_mov_b32 s69, 0\n"
    "s_mov_b32 s76, 0x40000\n"
    "Lred_%=:\n"
    "s_mul_i32 s49, s74, 43\n"
    "s_lshr_b32 s49, s49, 7\n"
    "s_mul_i32 s51, s73, 3\n"
    "s_add_u32 s49, s49, s51\n"
    "s_lshl_b32 s49, s49, 7\n"
    "Lpoll_%=:\n"
    "buffer_load_dwordx2 v[212:213], v6, s[36:39], s49 offen sc1\n"
    "s_waitcnt vmcnt(0)\n"
    "Lchk_%=:\n"
    "v_cmp_ne_u64 vcc, v[212:213], v[38:39]\n"
    "s_cbranch_vccz Lgo_%=\n"
    "s_sub_u32 s76, s76, 1\n"
    "s_cmp_eq_u32 s76, 0\n"
    "s_cbranch_scc1 Lgo_%=\n"
    "s_sleep 2\n"
    "s_branch Lpoll_%=\n"
    "Lgo_%=:\n"
    "s_lshl_b32 s77, s35, 1\n"
    "s_add_u32 s77, s77, s73\n"
    "s_sub_u32 s48, s77, 14\n"
    "s_cmp_ge_u32 s77, 14\n"
    "s_cselect_b32 s77, s48, s77\n"
    "s_mul_i32 s48, s77, 9\n"
    "s_add_u32 s48, s48, s74\n"
    "s_lshl_b32 s48, s48, 10\n"
    "buffer_load_dwordx4 v[64:67], v36, s[40:43], s48 offen sc1\n"
    "s_add_u32 s48, s48, 129024\n"
    "buffer_load_dwordx4 v[68:71], v36, s[40:43], s48 offen sc1\n"
    "s_add_u32 s48, s48, 129024\n"
    "buffer_load_dwordx4 v[72:75], v36, s[40:43], s48 offen sc1\n"
    "s_add_u32 s48, s48, 129024\n"
    "buffer_load_dwordx4 v[76:79], v36, s[40:43], s48 offen sc1\n"
    "s_add_u32 s48, s48, 129024\n"
    "buffer_load_dwordx4 v[80:83], v36, s[40:43], s48 offen sc1\n"
    "s_add_u32 s48, s48, 129024\n"
    "buffer_load_dwordx4 v[84:87], v36, s[40:43], s48 offen sc1\n"
    "s_add_u32 s48, s48, 129024\n"
    "buffer_load_dwordx4 v[88:91], v36, s[40:43], s48 offen sc1\n"
    "s_add_u32 s48, s48, 129024\n"
    "buffer_load_dwordx4 v[92:95], v36, s[40:43], s48 offen sc1\n"
    "s_add_u32 s48, s48, 129024\n"
    "buffer_load_dwordx4 v[96:99], v36, s[40:43], s48 offen sc1\n"
    "s_add_u32 s48, s48, 129024\n"
    "buffer_load_dwordx4 v[100:103], v36, s[40:43], s48 offen sc1\n"
    "s_add_u32 s48, s48, 129024\n"
    "buffer_load_dwordx4 v[104:107], v36, s[40:43], s48 offen sc1\n"
    "s_add_u32 s48, s48, 129024\n"
    "buffer_load_dwordx4 v[108:111], v36, s[40:43], s48 offen sc1\n"
    "s_add_u32 s48, s48, 129024\n"
    "buffer_load_dwordx4 v[112:115], v36, s[40:43], s48 offen sc1\n"
    "s_add_u32 s48, s48, 129024\n"
    "buffer_load_dwordx4 v[116:119], v36, s[40:43], s48 offen sc1\n"
    "s_add_u32 s48, s48, 129024\n"
    "buffer_load_dwordx4 v[120:123], v36, s[40:43], s48 offen sc1\n"
    "s_add_u32 s48, s48, 129024\n"
    "buffer_load_dwordx4 v[124:127], v36, s[40:43], s48 offen sc1\n"
    "s_mov_b32 s70, s73\n"
    "s_mov_b32 s71, s74\n"
    "s_mul_i32 s50, s74, 28672\n"
    "s_lshl_b32 s77, s77, 6\n"
    "s_add_u32 s50, s50, s77\n"
    "s_add_u32 s73, s73, 1\n"
    "s_add_u32 s74, s74, 7\n"
    "s_sub_u32 s48, s74, 9\n"
    "s_cmp_ge_u32 s74, 9\n"
    "s_cselect_b32 s74, s48, s74\n"
    "s_addc_u32 s73, s73, 0\n"
    "s_mul_i32 s49, s74, 43\n"
    "s_lshr_b32 s49, s49, 7\n"
    "s_mul_i32 s51, s73, 3\n"
    "s_add_u32 s49, s49, s51\n"
    "s_lshl_b32 s49, s49, 7\n"
    "buffer_load_dwordx2 v[212:213], v6, s[36:39], s49 offen sc1\n"
    "s_waitcnt vmcnt(15)\n"
    "v_mfma_f32_16x16x32_bf16 v[200:203], v[56:59], v[64:67], 0\n"
    "v_mfma_f32_16x16x32_bf16 v[204:207], v[60:63], v[64:67], 0\n"
    "v_mfma_f32_16x16x32_bf16 v[200:203], v[56:59], v[68:71], v[200:203]\n"
    "v_mfma_f32_16x16x32_bf16 v[204:207], v[60:63], v[68:71], v[204:207]\n"
    "s_waitcnt vmcnt(13)\n"
    "v_mfma_f32_16x16x32_bf16 v[200:203], v[56:59], v[72:75], v[200:203]\n"
    "v_mfma_f32_16x16x32_bf16 v[204:207], v[60:63], v[72:75], v[204:207]\n"
    "v_mfma_f32_16x16x32_bf16 v[200:203], v[56:59], v[76:79], v[200:203]\n"
    "v_mfma_f32_16x16x32_bf16 v[204:207], v[60:63], v[76:79], v[204:207]\n"
    "s_waitcnt vmcnt(11)\n"
    "v_mfma_f32_16x16x32_bf16 v[200:203], v[56:59], v[80:83], v[200:203]\n"
    "v_mfma_f32_16x16x32_bf16 v[204:207], v[60:63], v[80:83], v[204:207]\n"
    "v_mfma_f32_16x16x32_bf16 v[200:203], v[56:59], v[84:87], v[200:203]\n"
    "v_mfma_f32_16x16x32_bf16 v[204:207], v[60:63], v[84:87], v[204:207]\n"
    "s_waitcnt vmcnt(9)\n"
    "v_mfma_f32_16x16x32_bf16 v[200:203], v[56:59], v[88:91], v[200:203]\n"
    "v_mfma_f32_16x16x32_bf16 v[204:207], v[60:63], v[88:91], v[204:207]\n"
    "v_mfma_f32_16x16x32_bf16 v[200:203], v[56:59], v[92:95], v[200:203]\n"
    "v_mfma_f32_16x16x32_bf16 v[204:207], v[60:63], v[92:95], v[204:207]\n"
    "s_waitcnt vmcnt(7)\n"
    "v_mfma_f32_16x16x32_bf16 v[200:203], v[56:59], v[96:99], v[200:203]\n"
    "v_mfma_f32_16x16x32_bf16 v[204:207], v[60:63], v[96:99], v[204:207]\n"
    "v_mfma_f32_16x16x32_bf16 v[200:203], v[56:59], v[100:103], v[200:203]\n"
    "v_mfma_f32_16x16x32_bf16 v[204:207], v[60:63], v[100:103], v[204:207]\n"
    "s_waitcnt vmcnt(5)\n"
    "v_mfma_f32_16x16x32_bf16 v[200:203], v[56:59], v[104:107], v[200:203]\n"
    "v_mfma_f32_16x16x32_bf16 v[204:207], v[60:63], v[104:107], v[204:207]\n"
    "v_mfma_f32_16x16x32_bf16 v[200:203], v[56:59], v[108:111], v[200:203]\n"
    "v_mfma_f32_16x16x32_bf16 v[204:207], v[60:63], v[108:111], v[204:207]\n"
    "s_waitcnt vmcnt(3)\n"
    "v_mfma_f32_16x16x32_bf16 v[200:203], v[56:59], v[112:115], v[200:203]\n"
    "v_mfma_f32_16x16x32_bf16 v[204:207], v[60:63], v[112:115], v[204:207]\n"
    "v_mfma_f32_16x16x32_bf16 v[200:203], v[56:59], v[116:119], v[200:203]\n"
    "v_mfma_f32_16x16x32_bf16 v[204:207], v[60:63], v[116:119], v[204:207]\n"
    "s_waitcnt vmcnt(1)\n"
    "v_mfma_f32_16x16x32_bf16 v[200:203], v[56:59], v[120:123], v[200:203]\n"
    "v_mfma_f32_16x16x32_bf16 v[204:207], v[60:63], v[120:123], v[204:207]\n"
    "v_mfma_f32_16x16x32_bf16 v[200:203], v[56:59], v[124:127], v[200:203]\n"
    "v_mfma_f32_16x16x32_bf16 v[204:207], v[60:63], v[124:127], v[204:207]\n"
    "s_sub_u32 s75, s75, 1\n"
    "s_nop 5\n"
    "v_cvt_pk_bf16_f32 v208, v200, v201\n"
    "v_cvt_pk_bf16_f32 v209, v202, v203\n"
    "v_cvt_pk_bf16_f32 v210, v204, v205\n"
    "v_cvt_pk_bf16_f32 v211, v206, v207\n"
    "buffer_store_dwordx4 v[208:211], v7, s[44:47], s50 offen\n"
    "s_cmp_eq_u32 s75, 0\n"
    "s_cbranch_scc1 Lrdone_%=\n"
    "s_waitcnt vmcnt(1)\n"
    "s_branch Lchk_%=\n"
    "Lrdone_%=:\n"
    "s_lshl_b32 s48, s62, 2\n"
    "s_add_u32 s48, s48, s59\n"
    "s_cmp_lg_u32 s48, 3\n"
    "s_cbranch_scc1 Lfin_%=\n"
    "s_mov_b32 s48, 5376\n"
    "buffer_store_dwordx2 v[38:39], v26, s[36:39], s48 offen sc1\n"
    "Lfin_%=:\n"
    "s_branch Lend_%=\n"
    "Lcompute_%=:\n"
    "v_lshrrev_b32 v4, 2, v2\n"
    "v_and_b32 v5, 3, v1\n"
    "s_getpc_b64 s[50:51]\n"
    "Lpc_%=:\n"
    "s_add_u32 s52, s50, Lrare0_%=-Lpc_%=\n"
    "s_addc_u32 s53, s51, 0\n"
    "s_add_u32 s54, s50, Lrare1_%=-Lpc_%=\n"
    "s_addc_u32 s55, s51, 0\n"
    "v_lshrrev_b32 v40, 1, v3\n"
    "v_lshl_add_u32 v40, v40, 3, v4\n"
    "v_lshlrev_b32 v41, 9, v40\n"
    "v_and_b32 v40, 1, v3\n"
    "v_mul_u32_u24 v40, 8200, v40\n"
    "v_add_u32 v41, v40, v41\n"
    "v_lshl_add_u32 v41, v5, 4, v41\n"
    "v_add_u32 v41, s99, v41\n"
    "v_add_u32 v41, 112608, v41\n"
    "v_xor_b32 v42, 0, v4\n"
    "v_lshl_add_u32 v20, v42, 6, v41\n"
    "v_xor_b32 v42, 1, v4\n"
    "v_lshl_add_u32 v21, v42, 6, v41\n"
    "v_xor_b32 v42, 2, v4\n"
    "v_lshl_add_u32 v22, v42, 6, v41\n"
    "v_xor_b32 v42, 3, v4\n"
    "v_lshl_add_u32 v23, v42, 6, v41\n"
    "s_mul_i32 s48, s59, 6144\n"
    "s_add_u32 s48, s48, s99\n"
    "s_add_u32 s48, s48, 145408\n"
    "v_lshl_add_u32 v41, v2, 7, s48\n"
    "v_lshrrev_b32 v40, 1, v2\n"
    "v_xor_b32 v40, v40, v3\n"
    "v_lshl_add_u32 v24, v40, 4, v41\n"
    "v_xor_b32 v40, 4, v40\n"
    "v_lshl_add_u32 v25, v40, 4, v41\n"
    "s_mov_b32 s58, 0x47800000\n"
    "v_mov_b64 v[64:65], 0\n"
    "v_mov_b64 v[66:67], 0\n"
    "v_mov_b64 v[68:69], 0\n"
    "v_mov_b64 v[70:71], 0\n"
    "v_mov_b64 v[72:73], 0\n"
    "v_mov_b64 v[74:75], 0\n"
    "v_mov_b64 v[76:77], 0\n"
    "v_mov_b64 v[78:79], 0\n"
    "v_mov_b64 v[80:81], 0\n"
    "v_mov_b64 v[82:83], 0\n"
    "v_mov_b64 v[84:85], 0\n"
    "v_mov_b64 v[86:87], 0\n"
    "v_mov_b64 v[88:89], 0\n"
    "v_mov_b64 v[90:91], 0\n"
    "v_mov_b64 v[92:93], 0\n"
    "v_mov_b64 v[94:95], 0\n"
    "v_mov_b64 v[96:97], 0\n"
    "v_mov_b64 v[98:99], 0\n"
    "v_mov_b64 v[100:101], 0\n"
    "v_mov_b64 v[102:103], 0\n"
    "v_mov_b64 v[104:105], 0\n"
    "v_mov_b64 v[106:107], 0\n"
    "v_mov_b64 v[108:109], 0\n"
    "v_mov_b64 v[110:111], 0\n"
    "v_mov_b64 v[112:113], 0\n"
    "v_mov_b64 v[114:115], 0\n"
    "v_mov_b64 v[116:117], 0\n"
    "v_mov_b64 v[118:119], 0\n"
    "v_mov_b64 v[120:121], 0\n"
    "v_mov_b64 v[122:123], 0\n"
    "v_mov_b64 v[124:125], 0\n"
    "v_mov_b64 v[126:127], 0\n"
    "v_mov_b64 v[128:129], 0\n"
    "v_mov_b64 v[130:131], 0\n"
    "v_mov_b64 v[132:133], 0\n"
    "v_mov_b64 v[134:135], 0\n"
    "v_mov_b64 v[136:137], 0\n"
    "v_mov_b64 v[138:139], 0\n"
    "v_mov_b64 v[140:141], 0\n"
    "v_mov_b64 v[142:143], 0\n"
    "v_mov_b64 v[144:145], 0\n"
    "v_mov_b64 v[146:147], 0\n"
    "v_mov_b64 v[148:149], 0\n"
    "v_mov_b64 v[150:151], 0\n"
    "v_mov_b64 v[152:153], 0\n"
    "v_mov_b64 v[154:155], 0\n"
    "v_mov_b64 v[156:157], 0\n"
    "v_mov_b64 v[158:159], 0\n"
    "v_mov_b64 v[160:161], 0\n"
    "v_mov_b64 v[162:163], 0\n"
    "v_mov_b64 v[164:165], 0\n"
    "v_mov_b64 v[166:167], 0\n"
    "v_mov_b64 v[168:169], 0\n"
    "v_mov_b64 v[170:171], 0\n"
    "v_mov_b64 v[172:173], 0\n"
    "v_mov_b64 v[174:175], 0\n"
    "v_mov_b64 v[176:177], 0\n"
    "v_mov_b64 v[178:179], 0\n"
    "v_mov_b64 v[180:181], 0\n"
    "v_mov_b64 v[182:183], 0\n"
    "v_mov_b64 v[184:185], 0\n"
    "v_mov_b64 v[186:187], 0\n"
    "v_mov_b64 v[188:189], 0\n"
    "v_mov_b64 v[190:191], 0\n"
    "v_mov_b64 v[192:193], 0\n"
    "v_mov_b64 v[194:195], 0\n"
    "v_mov_b64 v[196:197], 0\n"
    "v_mov_b64 v[198:199], 0\n"
    "v_mov_b64 v[200:201], 0\n"
    "v_mov_b64 v[202:203], 0\n"
    "v_mov_b64 v[204:205], 0\n"
    "v_mov_b64 v[206:207], 0\n"
    "v_mov_b64 v[208:209], 0\n"
    "v_mov_b64 v[210:211], 0\n"
    "v_mov_b64 v[212:213], 0\n"
    "v_mov_b64 v[214:215], 0\n"
    "v_mov_b64 v[216:217], 0\n"
    "v_mov_b64 v[218:219], 0\n"
    "v_mov_b64 v[220:221], 0\n"
    "v_mov_b64 v[222:223], 0\n"
    "v_mov_b64 v[224:225], 0\n"
    "v_mov_b64 v[226:227], 0\n"
    "v_mov_b64 v[228:229], 0\n"
    "v_mov_b64 v[230:231], 0\n"
    "v_mov_b64 v[232:233], 0\n"
    "v_mov_b64 v[234:235], 0\n"
    "v_mov_b64 v[236:237], 0\n"
    "v_mov_b64 v[238:239], 0\n"
    "v_mov_b64 v[240:241], 0\n"
    "v_mov_b64 v[242:243], 0\n"
    "v_mov_b64 v[244:245], 0\n"
    "v_mov_b64 v[246:247], 0\n"
    "v_mov_b64 v[248:249], 0\n"
    "v_mov_b64 v[250:251], 0\n"
    "v_mov_b64 v[252:253], 0\n"
    "v_mov_b64 v[254:255], 0\n"
    "s_waitcnt lgkmcnt(0)\n"
    "s_mov_b32 s65, 0x3db8aa3b\n"
    "s_mov_b32 s69, 0xfffe4820\n"
    "s_mov_b32 s70, 0xc820\n"
    "s_mov_b32 s71, 0xefc0\n"
    "s_mov_b32 s64, 6\n"
    "s_barrier\n"
    "ds_read_b128 a[0:3], v24 offset:0\n"
    "ds_read_b64_tr_b16 a[192:193], v20 offset:0\n"
    "ds_read_b64_tr_b16 a[194:195], v20 offset:2048\n"
    "ds_read_b128 a[4:7], v24 offset:2048\n"
    "ds_read_b128 a[8:11], v24 offset:4096\n"
    "ds_read_b64_tr_b16 a[196:197], v20 offset:8\n"
    "ds_read_b64_tr_b16 a[198:199], v20 offset:2056\n"
    "ds_read_b64_tr_b16 a[200:201], v21 offset:0\n"
    "ds_read_b64_tr_b16 a[202:203], v21 offset:2048\n"
    "ds_read_b64_tr_b16 a[204:205], v21 offset:8\n"
    "ds_read_b64_tr_b16 a[206:207], v21 offset:2056\n"
    "s_waitcnt lgkmcnt(7)\n"
    "Lq_%=:\n"
    "v_mfma_f32_16x16x32_bf16 v[64:67], a[192:195], a[0:3], v[64:67]\n"
    "s_waitcnt lgkmcnt(6)\n"
    "ds_read_b64_tr_b16 a[208:209], v22 offset:0\n"
    "v_mfma_f32_16x16x32_bf16 v[68:71], a[192:195], a[4:7], v[68:71]\n"
    "ds_read_b64_tr_b16 a[210:211], v22 offset:2048\n"
    "v_mfma_f32_16x16x32_bf16 v[72:75], a[192:195], a[8:11], v[72:75]\n"
    "s_waitcnt lgkmcnt(6)\n"
    "ds_read_b64_tr_b16 a[212:213], v22 offset:8\n"
    "v_mfma_f32_16x16x32_bf16 v[84:87], a[196:199], a[8:11], v[84:87]\n"
    "ds_read_b64_tr_b16 a[214:215], v22 offset:2056\n"
    "v_mfma_f32_16x16x32_bf16 v[80:83], a[196:199], a[4:7], v[80:83]\n"
    "ds_read_b64_tr_b16 a[216:217], v23 offset:0\n"
    "v_mfma_f32_16x16x32_bf16 v[76:79], a[196:199], a[0:3], v[76:79]\n"
    "s_waitcnt lgkmcnt(7)\n"
    "ds_read_b64_tr_b16 a[218:219], v23 offset:2048\n"
    "v_mfma_f32_16x16x32_bf16 v[88:91], a[200:203], a[0:3], v[88:91]\n"
    "ds_read_b64_tr_b16 a[220:221], v23 offset:8\n"
    "v_mfma_f32_16x16x32_bf16 v[92:95], a[200:203], a[4:7], v[92:95]\n"
    "ds_read_b64_tr_b16 a[222:223], v23 offset:2056\n"
    "v_mfma_f32_16x16x32_bf16 v[96:99], a[200:203], a[8:11], v[96:99]\n"
    "s_waitcnt lgkmcnt(8)\n"
    "ds_read_b64_tr_b16 a[224:225], v20 offset:256\n"
    "v_mfma_f32_16x16x32_bf16 v[108:111], a[204:207], a[8:11], v[108:111]\n"
    "ds_read_b64_tr_b16 a[226:227], v20 offset:2304\n"
    "v_mfma_f32_16x16x32_bf16 v[104:107], a[204:207], a[4:7], v[104:107]\n"
    "ds_read_b64_tr_b16 a[228:229], v20 offset:264\n"
    "v_mfma_f32_16x16x32_bf16 v[100:103], a[204:207], a[0:3], v[100:103]\n"
    "s_waitcnt lgkmcnt(9)\n"
    "ds_read_b64_tr_b16 a[230:231], v20 offset:2312\n"
    "v_mfma_f32_16x16x32_bf16 v[112:115], a[208:211], a[0:3], v[112:115]\n"
    "ds_read_b64_tr_b16 a[232:233], v21 offset:256\n"
    "v_mfma_f32_16x16x32_bf16 v[116:119], a[208:211], a[4:7], v[116:119]\n"
    "ds_read_b64_tr_b16 a[234:235], v21 offset:2304\n"
    "v_mfma_f32_16x16x32_bf16 v[120:123], a[208:211], a[8:11], v[120:123]\n"
    "s_waitcnt lgkmcnt(10)\n"
    "ds_read_b64_tr_b16 a[236:237], v21 offset:264\n"
    "v_mfma_f32_16x16x32_bf16 v[132:135], a[212:215], a[8:11], v[132:135]\n"
    "ds_read_b64_tr_b16 a[238:239], v21 offset:2312\n"
    "v_mfma_f32_16x16x32_bf16 v[128:131], a[212:215], a[4:7], v[128:131]\n"
    "ds_read_b64_tr_b16 a[240:241], v22 offset:256\n"
    "v_mfma_f32_16x16x32_bf16 v[124:127], a[212:215], a[0:3], v[124:127]\n"
    "s_waitcnt lgkmcnt(11)\n"
    "ds_read_b64_tr_b16 a[242:243], v22 offset:2304\n"
    "v_mfma_f32_16x16x32_bf16 v[136:139], a[216:219], a[0:3], v[136:139]\n"
    "ds_read_b64_tr_b16 a[244:245], v22 offset:264\n"
    "v_mfma_f32_16x16x32_bf16 v[140:143], a[216:219], a[4:7], v[140:143]\n"
    "ds_read_b64_tr_b16 a[246:247], v22 offset:2312\n"
    "v_mfma_f32_16x16x32_bf16 v[144:147], a[216:219], a[8:11], v[144:147]\n"
    "s_waitcnt lgkmcnt(12)\n"
    "ds_read_b64_tr_b16 a[248:249], v23 offset:256\n"
    "v_mfma_f32_16x16x32_bf16 v[156:159], a[220:223], a[8:11], v[156:159]\n"
    "ds_read_b64_tr_b16 a[250:251], v23 offset:2304\n"
    "v_mfma_f32_16x16x32_bf16 v[152:155], a[220:223], a[4:7], v[152:155]\n"
    "v_mfma_f32_16x16x32_bf16 v[148:151], a[220:223], a[0:3], v[148:151]\n"
    "s_waitcnt lgkmcnt(12)\n"
    "ds_read_b64_tr_b16 a[252:253], v23 offset:264\n"
    "v_mfma_f32_16x16x32_bf16 v[160:163], a[224:227], a[0:3], v[160:163]\n"
    "ds_read_b64_tr_b16 a[254:255], v23 offset:2312\n"
    "v_mfma_f32_16x16x32_bf16 v[164:167], a[224:227], a[4:7], v[164:167]\n"
    "s_cmp_lg_u32 s64, 6\n"
    "s_cbranch_scc1 Lq1_%=\n"
    "s_barrier\n"
    "Lq1_%=:\n"
    "v_mfma_f32_16x16x32_bf16 v[168:171], a[224:227], a[8:11], v[168:171]\n"
    "s_waitcnt lgkmcnt(12)\n"
    "ds_read_b128 a[12:15], v25 offset:0\n"
    "v_mfma_f32_16x16x32_bf16 v[180:183], a[228:231], a[8:11], v[180:183]\n"
    "ds_read_b64_tr_b16 a[192:193], v20 offset:16400\n"
    "v_mfma_f32_16x16x32_bf16 v[176:179], a[228:231], a[4:7], v[176:179]\n"
    "ds_read_b64_tr_b16 a[194:195], v20 offset:18448\n"
    "v_mfma_f32_16x16x32_bf16 v[172:175], a[228:231], a[0:3], v[172:175]\n"
    "s_waitcnt lgkmcnt(13)\n"
    "ds_read_b128 a[16:19], v25 offset:2048\n"
    "v_mfma_f32_16x16x32_bf16 v[184:187], a[232:235], a[0:3], v[184:187]\n"
    "ds_read_b128 a[20:23], v25 offset:4096\n"
    "v_mfma_f32_16x16x32_bf16 v[188:191], a[232:235], a[4:7], v[188:191]\n"
    "ds_read_b64_tr_b16 a[196:197], v20 offset:16408\n"
    "v_mfma_f32_16x16x32_bf16 v[192:195], a[232:235], a[8:11], v[192:195]\n"
    "s_waitcnt lgkmcnt(14)\n"
    "ds_read_b64_tr_b16 a[198:199], v20 offset:18456\n"
    "v_mfma_f32_16x16x32_bf16 v[204:207], a[236:239], a[8:11], v[204:207]\n"
    "ds_read_b64_tr_b16 a[200:201], v21 offset:16400\n"
    "v_mfma_f32_16x16x32_bf16 v[200:203], a[236:239], a[4:7], v[200:203]\n"
    "ds_read_b64_tr_b16 a[202:203], v21 offset:18448\n"
    "v_mfma_f32_16x16x32_bf16 v[196:199], a[236:239], a[0:3], v[196:199]\n"
    "s_waitcnt lgkmcnt(15)\n"
    "ds_read_b64_tr_b16 a[204:205], v21 offset:16408\n"
    "v_mfma_f32_16x16x32_bf16 v[208:211], a[240:243], a[0:3], v[208:211]\n"
    "ds_read_b64_tr_b16 a[206:207], v21 offset:18456\n"
    "v_mfma_f32_16x16x32_bf16 v[212:215], a[240:243], a[4:7], v[212:215]\n"
    "v_mfma_f32_16x16x32_bf16 v[216:219], a[240:243], a[8:11], v[216:219]\n"
    "s_waitcnt lgkmcnt(15)\n"
    "ds_read_b64_tr_b16 a[208:209], v22 offset:16400\n"
    "v_mfma_f32_16x16x32_bf16 v[228:231], a[244:247], a[8:11], v[228:231]\n"
    "ds_read_b64_tr_b16 a[210:211], v22 offset:18448\n"
    "v_mfma_f32_16x16x32_bf16 v[224:227], a[244:247], a[4:7], v[224:227]\n"
    "v_mfma_f32_16x16x32_bf16 v[220:223], a[244:247], a[0:3], v[220:223]\n"
    "s_waitcnt lgkmcnt(15)\n"
    "ds_read_b64_tr_b16 a[212:213], v22 offset:16408\n"
    "v_mfma_f32_16x16x32_bf16 v[232:235], a[248:251], a[0:3], v[232:235]\n"
    "ds_read_b64_tr_b16 a[214:215], v22 offset:18456\n"
    "v_mfma_f32_16x16x32_bf16 v[236:239], a[248:251], a[4:7], v[236:239]\n"
    "v_mfma_f32_16x16x32_bf16 v[240:243], a[248:251], a[8:11], v[240:243]\n"
    "s_waitcnt lgkmcnt(15)\n"
    "ds_read_b64_tr_b16 a[216:217], v23 offset:16400\n"
    "v_mfma_f32_16x16x32_bf16 v[252:255], a[252:255], a[8:11], v[252:255]\n"
    "ds_read_b64_tr_b16 a[218:219], v23 offset:18448\n"
    "v_mfma_f32_16x16x32_bf16 v[248:251], a[252:255], a[4:7], v[248:251]\n"
    "v_mfma_f32_16x16x32_bf16 v[244:247], a[252:255], a[0:3], v[244:247]\n"
    "s_waitcnt lgkmcnt(13)\n"
    "ds_read_b64_tr_b16 a[220:221], v23 offset:16408\n"
    "v_mfma_f32_16x16x32_bf16 v[64:67], a[192:195], a[12:15], v[64:67]\n"
    "ds_read_b64_tr_b16 a[222:223], v23 offset:18456\n"
    "v_mfma_f32_16x16x32_bf16 v[68:71], a[192:195], a[16:19], v[68:71]\n"
    "s_waitcnt lgkmcnt(12)\n"
    "v_mfma_f32_16x16x32_bf16 v[72:75], a[192:195], a[20:23], v[72:75]\n"
    "ds_read_b64_tr_b16 a[224:225], v20 offset:16656\n"
    "v_mfma_f32_16x16x32_bf16 v[84:87], a[196:199], a[20:23], v[84:87]\n"
    "ds_read_b64_tr_b16 a[226:227], v20 offset:18704\n"
    "v_mfma_f32_16x16x32_bf16 v[80:83], a[196:199], a[16:19], v[80:83]\n"
    "v_mfma_f32_16x16x32_bf16 v[76:79], a[196:199], a[12:15], v[76:79]\n"
    "s_waitcnt lgkmcnt(12)\n"
    "ds_read_b64_tr_b16 a[228:229], v20 offset:16664\n"
    "v_mfma_f32_16x16x32_bf16 v[88:91], a[200:203], a[12:15], v[88:91]\n"
    "ds_read_b64_tr_b16 a[230:231], v20 offset:18712\n"
    "v_mfma_f32_16x16x32_bf16 v[92:95], a[200:203], a[16:19], v[92:95]\n"
    "v_mfma_f32_16x16x32_bf16 v[96:99], a[200:203], a[20:23], v[96:99]\n"
    "s_waitcnt lgkmcnt(12)\n"
    "ds_read_b64_tr_b16 a[232:233], v21 offset:16656\n"
    "v_mfma_f32_16x16x32_bf16 v[108:111], a[204:207], a[20:23], v[108:111]\n"
    "ds_read_b64_tr_b16 a[234:235], v21 offset:18704\n"
    "v_mfma_f32_16x16x32_bf16 v[104:107], a[204:207], a[16:19], v[104:107]\n"
    "v_mfma_f32_16x16x32_bf16 v[100:103], a[204:207], a[12:15], v[100:103]\n"
    "s_waitcnt lgkmcnt(12)\n"
    "ds_read_b64_tr_b16 a[236:237], v21 offset:16664\n"
    "v_mfma_f32_16x16x32_bf16 v[112:115], a[208:211], a[12:15], v[112:115]\n"
    "ds_read_b64_tr_b16 a[238:239], v21 offset:18712\n"
    "v_mfma_f32_16x16x32_bf16 v[116:119], a[208:211], a[16:19], v[116:119]\n"
    "v_mfma_f32_16x16x32_bf16 v[120:123], a[208:211], a[20:23], v[120:123]\n"
    "s_waitcnt lgkmcnt(12)\n"
    "ds_read_b64_tr_b16 a[240:241], v22 offset:16656\n"
    "v_mfma_f32_16x16x32_bf16 v[132:135], a[212:215], a[20:23], v[132:135]\n"
    "ds_read_b64_tr_b16 a[242:243], v22 offset:18704\n"
    "v_mfma_f32_16x16x32_bf16 v[128:131], a[212:215], a[16:19], v[128:131]\n"
    "v_mfma_f32_16x16x32_bf16 v[124:127], a[212:215], a[12:15], v[124:127]\n"
    "s_waitcnt lgkmcnt(12)\n"
    "ds_read_b64_tr_b16 a[244:245], v22 offset:16664\n"
    "v_mfma_f32_16x16x32_bf16 v[136:139], a[216:219], a[12:15], v[136:139]\n"
    "ds_read_b64_tr_b16 a[246:247], v22 offset:18712\n"
    "v_mfma_f32_16x16x32_bf16 v[140:143], a[216:219], a[16:19], v[140:143]\n"
    "v_mfma_f32_16x16x32_bf16 v[144:147], a[216:219], a[20:23], v[144:147]\n"
    "s_waitcnt lgkmcnt(12)\n"
    "ds_read_b64_tr_b16 a[248:249], v23 offset:16656\n"
    "v_mfma_f32_16x16x32_bf16 v[156:159], a[220:223], a[20:23], v[156:159]\n"
    "ds_read_b64_tr_b16 a[250:251], v23 offset:18704\n"
    "v_mfma_f32_16x16x32_bf16 v[152:155], a[220:223], a[16:19], v[152:155]\n"
    "v_mfma_f32_16x16x32_bf16 v[148:151], a[220:223], a[12:15], v[148:151]\n"
    "s_waitcnt lgkmcnt(12)\n"
    "ds_read_b64_tr_b16 a[252:253], v23 offset:16664\n"
    "v_mfma_f32_16x16x32_bf16 v[160:163], a[224:227], a[12:15], v[160:163]\n"
    "ds_read_b64_tr_b16 a[254:255], v23 offset:18712\n"
    "v_add_u32 v20, s69, v20\n"
    "v_mfma_f32_16x16x32_bf16 v[164:167], a[224:227], a[16:19], v[164:167]\n"
    "v_add_u32 v21, s69, v21\n"
    "v_add_u32 v22, s69, v22\n"
    "v_mfma_f32_16x16x32_bf16 v[168:171], a[224:227], a[20:23], v[168:171]\n"
    "s_waitcnt lgkmcnt(12)\n"
    "v_add_u32 v23, s69, v23\n"
    "v_add_u32 v24, s69, v24\n"
    "v_mfma_f32_16x16x32_bf16 v[180:183], a[228:231], a[20:23], v[180:183]\n"
    "v_add_u32 v25, s69, v25\n"
    "v_mfma_f32_16x16x32_bf16 v[176:179], a[228:231], a[16:19], v[176:179]\n"
    "v_mfma_f32_16x16x32_bf16 v[172:175], a[228:231], a[12:15], v[172:175]\n"
    "s_waitcnt lgkmcnt(10)\n"
    "v_mfma_f32_16x16x32_bf16 v[184:187], a[232:235], a[12:15], v[184:187]\n"
    "v_mfma_f32_16x16x32_bf16 v[188:191], a[232:235], a[16:19], v[188:191]\n"
    "v_mfma_f32_16x16x32_bf16 v[192:195], a[232:235], a[20:23], v[192:195]\n"
    "s_waitcnt lgkmcnt(8)\n"
    "v_mfma_f32_16x16x32_bf16 v[204:207], a[236:239], a[20:23], v[204:207]\n"
    "v_mfma_f32_16x16x32_bf16 v[200:203], a[236:239], a[16:19], v[200:203]\n"
    "v_mfma_f32_16x16x32_bf16 v[196:199], a[236:239], a[12:15], v[196:199]\n"
    "s_waitcnt lgkmcnt(0)\n"
    "s_barrier\n"
    "v_mfma_f32_16x16x32_bf16 v[208:211], a[240:243], a[12:15], v[208:211]\n"
    "s_mov_b32 s48, s69\n"
    "ds_read_b128 a[0:3], v24 offset:0\n"
    "s_mov_b32 s69, s70\n"
    "v_mfma_f32_16x16x32_bf16 v[212:215], a[240:243], a[16:19], v[212:215]\n"
    "s_mov_b32 s70, s71\n"
    "ds_read_b64_tr_b16 a[192:193], v20 offset:0\n"
    "s_mov_b32 s71, s48\n"
    "v_mfma_f32_16x16x32_bf16 v[216:219], a[240:243], a[20:23], v[216:219]\n"
    "s_sub_u32 s64, s64, 1\n"
    "ds_read_b64_tr_b16 a[194:195], v20 offset:2048\n"
    "s_cmp_lg_u32 s64, 0\n"
    "v_mfma_f32_16x16x32_bf16 v[228:231], a[244:247], a[20:23], v[228:231]\n"
    "ds_read_b128 a[4:7], v24 offset:2048\n"
    "v_mfma_f32_16x16x32_bf16 v[224:227], a[244:247], a[16:19], v[224:227]\n"
    "ds_read_b128 a[8:11], v24 offset:4096\n"
    "v_mfma_f32_16x16x32_bf16 v[220:223], a[244:247], a[12:15], v[220:223]\n"
    "ds_read_b64_tr_b16 a[196:197], v20 offset:8\n"
    "v_mfma_f32_16x16x32_bf16 v[232:235], a[248:251], a[12:15], v[232:235]\n"
    "ds_read_b64_tr_b16 a[198:199], v20 offset:2056\n"
    "v_mfma_f32_16x16x32_bf16 v[236:239], a[248:251], a[16:19], v[236:239]\n"
    "ds_read_b64_tr_b16 a[200:201], v21 offset:0\n"
    "v_mfma_f32_16x16x32_bf16 v[240:243], a[248:251], a[20:23], v[240:243]\n"
    "ds_read_b64_tr_b16 a[202:203], v21 offset:2048\n"
    "v_mfma_f32_16x16x32_bf16 v[252:255], a[252:255], a[20:23], v[252:255]\n"
    "ds_read_b64_tr_b16 a[204:205], v21 offset:8\n"
    "v_mfma_f32_16x16x32_bf16 v[248:251], a[252:255], a[16:19], v[248:251]\n"
    "ds_read_b64_tr_b16 a[206:207], v21 offset:2056\n"
    "v_mfma_f32_16x16x32_bf16 v[244:247], a[252:255], a[12:15], v[244:247]\n"
    "s_waitcnt lgkmcnt(7)\n"
    "s_cbranch_scc1 Lq_%=\n"
    "v_mfma_f32_16x16x32_bf16 v[64:67], a[192:195], a[0:3], v[64:67]\n"
    "s_waitcnt lgkmcnt(6)\n"
    "ds_read_b128 a[12:15], v25 offset:0\n"
    "v_xor_b32 v43, v3, v2\n"
    "v_mfma_f32_16x16x32_bf16 v[68:71], a[192:195], a[4:7], v[68:71]\n"
    "ds_read_b64_tr_b16 a[208:209], v20 offset:16400\n"
    "v_lshl_add_u32 v44, v2, 9, s99\n"
    "v_mfma_f32_16x16x32_bf16 v[72:75], a[192:195], a[8:11], v[72:75]\n"
    "s_waitcnt lgkmcnt(6)\n"
    "ds_read_b64_tr_b16 a[210:211], v20 offset:18448\n"
    "v_xor_b32 v45, 0, v43\n"
    "v_mfma_f32_16x16x32_bf16 v[84:87], a[196:199], a[8:11], v[84:87]\n"
    "ds_read_b128 a[16:19], v25 offset:2048\n"
    "v_lshl_add_u32 v8, v45, 4, v44\n"
    "v_mfma_f32_16x16x32_bf16 v[80:83], a[196:199], a[4:7], v[80:83]\n"
    "ds_read_b128 a[20:23], v25 offset:4096\n"
    "v_xor_b32 v45, 4, v43\n"
    "v_mfma_f32_16x16x32_bf16 v[76:79], a[196:199], a[0:3], v[76:79]\n"
    "s_waitcnt lgkmcnt(7)\n"
    "ds_read_b64_tr_b16 a[212:213], v20 offset:16408\n"
    "v_lshl_add_u32 v9, v45, 4, v44\n"
    "v_mfma_f32_16x16x32_bf16 v[88:91], a[200:203], a[0:3], v[88:91]\n"
    "ds_read_b64_tr_b16 a[214:215], v20 offset:18456\n"
    "v_xor_b32 v45, 8, v43\n"
    "v_mfma_f32_16x16x32_bf16 v[92:95], a[200:203], a[4:7], v[92:95]\n"
    "ds_read_b64_tr_b16 a[216:217], v21 offset:16400\n"
    "v_lshl_add_u32 v10, v45, 4, v44\n"
    "v_mfma_f32_16x16x32_bf16 v[96:99], a[200:203], a[8:11], v[96:99]\n"
    "s_waitcnt lgkmcnt(8)\n"
    "ds_read_b64_tr_b16 a[218:219], v21 offset:18448\n"
    "v_xor_b32 v45, 12, v43\n"
    "v_mfma_f32_16x16x32_bf16 v[108:111], a[204:207], a[8:11], v[108:111]\n"
    "ds_read_b64_tr_b16 a[220:221], v21 offset:16408\n"
    "v_lshl_add_u32 v11, v45, 4, v44\n"
    "v_mfma_f32_16x16x32_bf16 v[104:107], a[204:207], a[4:7], v[104:107]\n"
    "ds_read_b64_tr_b16 a[222:223], v21 offset:18456\n"
    "v_lshl_add_u32 v43, v3, 2, v4\n"
    "v_mfma_f32_16x16x32_bf16 v[100:103], a[204:207], a[0:3], v[100:103]\n"
    "s_waitcnt lgkmcnt(7)\n"
    "ds_read_b64_tr_b16 a[224:225], v22 offset:0\n"
    "v_lshlrev_b32 v44, 9, v43\n"
    "v_mfma_f32_16x16x32_bf16 v[64:67], a[208:211], a[12:15], v[64:67]\n"
    "ds_read_b64_tr_b16 a[226:227], v22 offset:2048\n"
    "v_lshl_add_u32 v44, v5, 3, v44\n"
    "v_mfma_f32_16x16x32_bf16 v[68:71], a[208:211], a[16:19], v[68:71]\n"
    "s_waitcnt lgkmcnt(6)\n"
    "ds_read_b64_tr_b16 a[228:229], v22 offset:8\n"
    "v_add_u32 v44, s99, v44\n"
    "v_mfma_f32_16x16x32_bf16 v[72:75], a[208:211], a[20:23], v[72:75]\n"
    "ds_read_b64_tr_b16 a[230:231], v22 offset:2056\n"
    "v_add_u32 v44, 0x10000, v44\n"
    "v_mfma_f32_16x16x32_bf16 v[84:87], a[212:215], a[20:23], v[84:87]\n"
    "ds_read_b64_tr_b16 a[232:233], v23 offset:0\n"
    "v_and_b32 v43, 7, v43\n"
    "v_mfma_f32_16x16x32_bf16 v[80:83], a[212:215], a[16:19], v[80:83]\n"
    "ds_read_b64_tr_b16 a[234:235], v23 offset:2048\n"
    "v_xor_b32 v45, 0, v43\n"
    "v_mfma_f32_16x16x32_bf16 v[76:79], a[212:215], a[12:15], v[76:79]\n"
    "s_waitcnt lgkmcnt(8)\n"
    "ds_read_b64_tr_b16 a[236:237], v23 offset:8\n"
    "v_lshl_add_u32 v12, v45, 5, v44\n"
    "v_mfma_f32_16x16x32_bf16 v[88:91], a[216:219], a[12:15], v[88:91]\n"
    "ds_read_b64_tr_b16 a[238:239], v23 offset:2056\n"
    "v_xor_b32 v45, 1, v43\n"
    "v_mfma_f32_16x16x32_bf16 v[92:95], a[216:219], a[16:19], v[92:95]\n"
    "ds_read_b64_tr_b16 a[240:241], v22 offset:16400\n"
    "v_lshl_add_u32 v13, v45, 5, v44\n"
    "v_mfma_f32_16x16x32_bf16 v[96:99], a[216:219], a[20:23], v[96:99]\n"
    "s_waitcnt lgkmcnt(9)\n"
    "ds_read_b64_tr_b16 a[242:243], v22 offset:18448\n"
    "v_xor_b32 v45, 2, v43\n"
    "v_mfma_f32_16x16x32_bf16 v[108:111], a[220:223], a[20:23], v[108:111]\n"
    "ds_read_b64_tr_b16 a[244:245], v22 offset:16408\n"
    "v_lshl_add_u32 v14, v45, 5, v44\n"
    "v_mfma_f32_16x16x32_bf16 v[104:107], a[220:223], a[16:19], v[104:107]\n"
    "ds_read_b64_tr_b16 a[246:247], v22 offset:18456\n"
    "v_xor_b32 v45, 3, v43\n"
    "v_mfma_f32_16x16x32_bf16 v[100:103], a[220:223], a[12:15], v[100:103]\n"
    "s_waitcnt lgkmcnt(10)\n"
    "ds_read_b64_tr_b16 a[248:249], v23 offset:16400\n"
    "v_lshl_add_u32 v15, v45, 5, v44\n"
    "v_mfma_f32_16x16x32_bf16 v[112:115], a[224:227], a[0:3], v[112:115]\n"
    "ds_read_b64_tr_b16 a[250:251], v23 offset:18448\n"
    "v_xor_b32 v45, 4, v43\n"
    "v_mfma_f32_16x16x32_bf16 v[116:119], a[224:227], a[4:7], v[116:119]\n"
    "ds_read_b64_tr_b16 a[252:253], v23 offset:16408\n"
    "v_lshl_add_u32 v16, v45, 5, v44\n"
    "v_mfma_f32_16x16x32_bf16 v[120:123], a[224:227], a[8:11], v[120:123]\n"
    "s_waitcnt lgkmcnt(11)\n"
    "ds_read_b64_tr_b16 a[254:255], v23 offset:18456\n"
    "v_xor_b32 v45, 5, v43\n"
    "v_mfma_f32_16x16x32_bf16 v[132:135], a[228:231], a[8:11], v[132:135]\n"
    "ds_read_b64_tr_b16 a[192:193], v20 offset:256\n"
    "v_lshl_add_u32 v17, v45, 5, v44\n"
    "v_mfma_f32_16x16x32_bf16 v[128:131], a[228:231], a[4:7], v[128:131]\n"
    "ds_read_b64_tr_b16 a[194:195], v20 offset:2304\n"
    "v_xor_b32 v45, 6, v43\n"
    "v_mfma_f32_16x16x32_bf16 v[124:127], a[228:231], a[0:3], v[124:127]\n"
    "s_waitcnt lgkmcnt(12)\n"
    "ds_read_b64_tr_b16 a[196:197], v20 offset:264\n"
    "v_lshl_add_u32 v18, v45, 5, v44\n"
    "v_mfma_f32_16x16x32_bf16 v[136:139], a[232:235], a[0:3], v[136:139]\n"
    "ds_read_b64_tr_b16 a[198:199], v20 offset:2312\n"
    "v_xor_b32 v45, 7, v43\n"
    "v_mfma_f32_16x16x32_bf16 v[140:143], a[232:235], a[4:7], v[140:143]\n"
    "v_lshl_add_u32 v19, v45, 5, v44\n"
    "v_mul_f32 v64, s65, v64\n"
    "v_mfma_f32_16x16x32_bf16 v[144:147], a[232:235], a[8:11], v[144:147]\n"
    "s_waitcnt lgkmcnt(12)\n"
    "ds_read_b64_tr_b16 a[200:201], v21 offset:256\n"
    "v_mul_f32 v65, s65, v65\n"
    "v_mfma_f32_16x16x32_bf16 v[156:159], a[236:239], a[8:11], v[156:159]\n"
    "ds_read_b64_tr_b16 a[202:203], v21 offset:2304\n"
    "v_mul_f32 v66, s65, v66\n"
    "v_mfma_f32_16x16x32_bf16 v[152:155], a[236:239], a[4:7], v[152:155]\n"
    "v_mul_f32 v67, s65, v67\n"
    "v_cvt_pk_bf16_f32 v64, v64, v65\n"
    "v_mfma_f32_16x16x32_bf16 v[148:151], a[236:239], a[0:3], v[148:151]\n"
    "s_waitcnt lgkmcnt(12)\n"
    "ds_read_b64_tr_b16 a[204:205], v21 offset:264\n"
    "v_cvt_pk_bf16_f32 v65, v66, v67\n"
    "v_mfma_f32_16x16x32_bf16 v[112:115], a[240:243], a[12:15], v[112:115]\n"
    "ds_read_b64_tr_b16 a[206:207], v21 offset:2312\n"
    "v_mul_f32 v76, s65, v76\n"
    "v_mfma_f32_16x16x32_bf16 v[116:119], a[240:243], a[16:19], v[116:119]\n"
    "v_mul_f32 v77, s65, v77\n"
    "v_mul_f32 v78, s65, v78\n"
    "v_mfma_f32_16x16x32_bf16 v[120:123], a[240:243], a[20:23], v[120:123]\n"
    "s_waitcnt lgkmcnt(12)\n"
    "ds_read_b64_tr_b16 a[208:209], v20 offset:16656\n"
    "v_mul_f32 v79, s65, v79\n"
    "v_mfma_f32_16x16x32_bf16 v[132:135], a[244:247], a[20:23], v[132:135]\n"
    "ds_read_b64_tr_b16 a[210:211], v20 offset:18704\n"
    "v_cvt_pk_bf16_f32 v66, v76, v77\n"
    "v_mfma_f32_16x16x32_bf16 v[128:131], a[244:247], a[16:19], v[128:131]\n"
    "v_cvt_pk_bf16_f32 v67, v78, v79\n"
    "v_mul_f32 v68, s65, v68\n"
    "v_mfma_f32_16x16x32_bf16 v[124:127], a[244:247], a[12:15], v[124:127]\n"
    "s_waitcnt lgkmcnt(12)\n"
    "ds_read_b64_tr_b16 a[212:213], v20 offset:16664\n"
    "v_mul_f32 v69, s65, v69\n"
    "v_mfma_f32_16x16x32_bf16 v[136:139], a[248:251], a[12:15], v[136:139]\n"
    "ds_read_b64_tr_b16 a[214:215], v20 offset:18712\n"
    "v_mul_f32 v70, s65, v70\n"
    "v_mfma_f32_16x16x32_bf16 v[140:143], a[248:251], a[16:19], v[140:143]\n"
    "v_mul_f32 v71, s65, v71\n"
    "v_cvt_pk_bf16_f32 v68, v68, v69\n"
    "v_mfma_f32_16x16x32_bf16 v[144:147], a[248:251], a[20:23], v[144:147]\n"
    "s_waitcnt lgkmcnt(12)\n"
    "ds_read_b64_tr_b16 a[216:217], v21 offset:16656\n"
    "v_cvt_pk_bf16_f32 v69, v70, v71\n"
    "v_mfma_f32_16x16x32_bf16 v[156:159], a[252:255], a[20:23], v[156:159]\n"
    "ds_read_b64_tr_b16 a[218:219], v21 offset:18704\n"
    "v_mul_f32 v80, s65, v80\n"
    "v_mfma_f32_16x16x32_bf16 v[152:155], a[252:255], a[16:19], v[152:155]\n"
    "v_mul_f32 v81, s65, v81\n"
    "v_mul_f32 v82, s65, v82\n"
    "v_mfma_f32_16x16x32_bf16 v[148:151], a[252:255], a[12:15], v[148:151]\n"
    "s_waitcnt lgkmcnt(12)\n"
    "ds_read_b64_tr_b16 a[220:221], v21 offset:16664\n"
    "v_mul_f32 v83, s65, v83\n"
    "v_mfma_f32_16x16x32_bf16 v[160:163], a[192:195], a[0:3], v[160:163]\n"
    "ds_read_b64_tr_b16 a[222:223], v21 offset:18712\n"
    "v_cvt_pk_bf16_f32 v70, v80, v81\n"
    "v_mfma_f32_16x16x32_bf16 v[164:167], a[192:195], a[4:7], v[164:167]\n"
    "v_cvt_pk_bf16_f32 v71, v82, v83\n"
    "v_mul_f32 v72, s65, v72\n"
    "v_mfma_f32_16x16x32_bf16 v[168:171], a[192:195], a[8:11], v[168:171]\n"
    "s_waitcnt lgkmcnt(12)\n"
    "ds_read_b64_tr_b16 a[224:225], v22 offset:256\n"
    "v_mul_f32 v73, s65, v73\n"
    "v_mfma_f32_16x16x32_bf16 v[180:183], a[196:199], a[8:11], v[180:183]\n"
    "ds_read_b64_tr_b16 a[226:227], v22 offset:2304\n"
    "v_mul_f32 v74, s65, v74\n"
    "v_mfma_f32_16x16x32_bf16 v[176:179], a[196:199], a[4:7], v[176:179]\n"
    "v_mul_f32 v75, s65, v75\n"
    "v_cvt_pk_bf16_f32 v72, v72, v73\n"
    "v_mfma_f32_16x16x32_bf16 v[172:175], a[196:199], a[0:3], v[172:175]\n"
    "s_waitcnt lgkmcnt(12)\n"
    "ds_read_b64_tr_b16 a[228:229], v22 offset:264\n"
    "v_cvt_pk_bf16_f32 v73, v74, v75\n"
    "v_mfma_f32_16x16x32_bf16 v[184:187], a[200:203], a[0:3], v[184:187]\n"
    "ds_read_b64_tr_b16 a[230:231], v22 offset:2312\n"
    "v_mul_f32 v84, s65, v84\n"
    "v_mfma_f32_16x16x32_bf16 v[188:191], a[200:203], a[4:7], v[188:191]\n"
    "v_mul_f32 v85, s65, v85\n"
    "v_mul_f32 v86, s65, v86\n"
    "v_mfma_f32_16x16x32_bf16 v[192:195], a[200:203], a[8:11], v[192:195]\n"
    "s_waitcnt lgkmcnt(12)\n"
    "ds_read_b64_tr_b16 a[232:233], v23 offset:256\n"
    "v_mul_f32 v87, s65, v87\n"
    "v_mfma_f32_16x16x32_bf16 v[204:207], a[204:207], a[8:11], v[204:207]\n"
    "ds_read_b64_tr_b16 a[234:235], v23 offset:2304\n"
    "v_cvt_pk_bf16_f32 v74, v84, v85\n"
    "v_mfma_f32_16x16x32_bf16 v[200:203], a[204:207], a[4:7], v[200:203]\n"
    "v_cvt_pk_bf16_f32 v75, v86, v87\n"
    "v_mul_f32 v88, s65, v88\n"
    "v_mfma_f32_16x16x32_bf16 v[196:199], a[204:207], a[0:3], v[196:199]\n"
    "s_waitcnt lgkmcnt(12)\n"
    "ds_read_b64_tr_b16 a[236:237], v23 offset:264\n"
    "v_mul_f32 v89, s65, v89\n"
    "v_mfma_f32_16x16x32_bf16 v[160:163], a[208:211], a[12:15], v[160:163]\n"
    "ds_read_b64_tr_b16 a[238:239], v23 offset:2312\n"
    "v_mul_f32 v90, s65, v90\n"
    "v_mfma_f32_16x16x32_bf16 v[164:167], a[208:211], a[16:19], v[164:167]\n"
    "v_mul_f32 v91, s65, v91\n"
    "v_cvt_pk_bf16_f32 v76, v88, v89\n"
    "v_mfma_f32_16x16x32_bf16 v[168:171], a[208:211], a[20:23], v[168:171]\n"
    "s_waitcnt lgkmcnt(12)\n"
    "ds_read_b64_tr_b16 a[240:241], v22 offset:16656\n"
    "v_cvt_pk_bf16_f32 v77, v90, v91\n"
    "v_mfma_f32_16x16x32_bf16 v[180:183], a[212:215], a[20:23], v[180:183]\n"
    "ds_read_b64_tr_b16 a[242:243], v22 offset:18704\n"
    "v_mul_f32 v100, s65, v100\n"
    "v_mfma_f32_16x16x32_bf16 v[176:179], a[212:215], a[16:19], v[176:179]\n"
    "v_mul_f32 v101, s65, v101\n"
    "v_mul_f32 v102, s65, v102\n"
    "v_mfma_f32_16x16x32_bf16 v[172:175], a[212:215], a[12:15], v[172:175]\n"
    "s_waitcnt lgkmcnt(12)\n"
    "ds_read_b64_tr_b16 a[244:245], v22 offset:16664\n"
    "v_mul_f32 v103, s65, v103\n"
    "v_mfma_f32_16x16x32_bf16 v[184:187], a[216:219], a[12:15], v[184:187]\n"
    "ds_read_b64_tr_b16 a[246:247], v22 offset:18712\n"
    "v_cvt_pk_bf16_f32 v78, v100, v101\n"
    "v_mfma_f32_16x16x32_bf16 v[188:191], a[216:219], a[16:19], v[188:191]\n"
    "v_cvt_pk_bf16_f32 v79, v102, v103\n"
    "v_mul_f32 v92, s65, v92\n"
    "v_mfma_f32_16x16x32_bf16 v[192:195], a[216:219], a[20:23], v[192:195]\n"
    "s_waitcnt lgkmcnt(12)\n"
    "ds_read_b64_tr_b16 a[248:249], v23 offset:16656\n"
    "v_mul_f32 v93, s65, v93\n"
    "v_mfma_f32_16x16x32_bf16 v[204:207], a[220:223], a[20:23], v[204:207]\n"
    "ds_read_b64_tr_b16 a[250:251], v23 offset:18704\n"
    "v_mul_f32 v94, s65, v94\n"
    "v_mfma_f32_16x16x32_bf16 v[200:203], a[220:223], a[16:19], v[200:203]\n"
    "v_mul_f32 v95, s65, v95\n"
    "v_cvt_pk_bf16_f32 v80, v92, v93\n"
    "v_mfma_f32_16x16x32_bf16 v[196:199], a[220:223], a[12:15], v[196:199]\n"
    "s_waitcnt lgkmcnt(12)\n"
    "ds_read_b64_tr_b16 a[252:253], v23 offset:16664\n"
    "v_cvt_pk_bf16_f32 v81, v94, v95\n"
    "v_mfma_f32_16x16x32_bf16 v[208:211], a[224:227], a[0:3], v[208:211]\n"
    "ds_read_b64_tr_b16 a[254:255], v23 offset:18712\n"
    "v_mul_f32 v104, s65, v104\n"
    "v_mfma_f32_16x16x32_bf16 v[212:215], a[224:227], a[4:7], v[212:215]\n"
    "v_mul_f32 v105, s65, v105\n"
    "v_mul_f32 v106, s65, v106\n"
    "v_mfma_f32_16x16x32_bf16 v[216:219], a[224:227], a[8:11], v[216:219]\n"
    "s_waitcnt lgkmcnt(12)\n"
    "v_mul_f32 v107, s65, v107\n"
    "v_cvt_pk_bf16_f32 v82, v104, v105\n"
    "v_mfma_f32_16x16x32_bf16 v[228:231], a[228:231], a[8:11], v[228:231]\n"
    "v_cvt_pk_bf16_f32 v83, v106, v107\n"
    "v_mul_f32 v96, s65, v96\n"
    "v_mfma_f32_16x16x32_bf16 v[224:227], a[228:231], a[4:7], v[224:227]\n"
    "v_mul_f32 v97, s65, v97\n"
    "v_mul_f32 v98, s65, v98\n"
    "v_mfma_f32_16x16x32_bf16 v[220:223], a[228:231], a[0:3], v[220:223]\n"
    "s_waitcnt lgkmcnt(10)\n"
    "v_mul_f32 v99, s65, v99\n"
    "v_cvt_pk_bf16_f32 v84, v96, v97\n"
    "v_mfma_f32_16x16x32_bf16 v[232:235], a[232:235], a[0:3], v[232:235]\n"
    "v_cvt_pk_bf16_f32 v85, v98, v99\n"
    "v_mul_f32 v108, s65, v108\n"
    "v_mfma_f32_16x16x32_bf16 v[236:239], a[232:235], a[4:7], v[236:239]\n"
    "v_mul_f32 v109, s65, v109\n"
    "v_mul_f32 v110, s65, v110\n"
    "v_mfma_f32_16x16x32_bf16 v[240:243], a[232:235], a[8:11], v[240:243]\n"
    "s_waitcnt lgkmcnt(8)\n"
    "v_mul_f32 v111, s65, v111\n"
    "v_cvt_pk_bf16_f32 v86, v108, v109\n"
    "v_mfma_f32_16x16x32_bf16 v[252:255], a[236:239], a[8:11], v[252:255]\n"
    "v_cvt_pk_bf16_f32 v87, v110, v111\n"
    "v_mul_f32 v112, s65, v112\n"
    "v_mfma_f32_16x16x32_bf16 v[248:251], a[236:239], a[4:7], v[248:251]\n"
    "v_mul_f32 v113, s65, v113\n"
    "v_mul_f32 v114, s65, v114\n"
    "v_mfma_f32_16x16x32_bf16 v[244:247], a[236:239], a[0:3], v[244:247]\n"
    "s_waitcnt lgkmcnt(0)\n"
    "s_barrier\n"
    "v_mul_f32 v115, s65, v115\n"
    "v_mfma_f32_16x16x32_bf16 v[208:211], a[240:243], a[12:15], v[208:211]\n"
    "ds_read_b128 a[192:195], v8 offset:0\n"
    "v_cvt_pk_bf16_f32 v112, v112, v113\n"
    "v_mfma_f32_16x16x32_bf16 v[212:215], a[240:243], a[16:19], v[212:215]\n"
    "ds_read_b128 a[224:227], v8 offset:8192\n"
    "v_cvt_pk_bf16_f32 v113, v114, v115\n"
    "v_mfma_f32_16x16x32_bf16 v[216:219], a[240:243], a[20:23], v[216:219]\n"
    "ds_read_b128 a[196:199], v9 offset:0\n"
    "v_mul_f32 v124, s65, v124\n"
    "v_mfma_f32_16x16x32_bf16 v[228:231], a[244:247], a[20:23], v[228:231]\n"
    "ds_read_b128 a[228:231], v9 offset:8192\n"
    "v_mul_f32 v125, s65, v125\n"
    "v_mfma_f32_16x16x32_bf16 v[224:227], a[244:247], a[16:19], v[224:227]\n"
    "ds_read_b128 a[200:203], v10 offset:0\n"
    "v_mul_f32 v126, s65, v126\n"
    "v_mfma_f32_16x16x32_bf16 v[220:223], a[244:247], a[12:15], v[220:223]\n"
    "ds_read_b128 a[232:235], v10 offset:8192\n"
    "v_mul_f32 v127, s65, v127\n"
    "v_mfma_f32_16x16x32_bf16 v[232:235], a[248:251], a[12:15], v[232:235]\n"
    "v_cvt_pk_bf16_f32 v114, v124, v125\n"
    "v_cvt_pk_bf16_f32 v115, v126, v127\n"
    "v_mfma_f32_16x16x32_bf16 v[236:239], a[248:251], a[16:19], v[236:239]\n"
    "v_mul_f32 v116, s65, v116\n"
    "v_mul_f32 v117, s65, v117\n"
    "v_mfma_f32_16x16x32_bf16 v[240:243], a[248:251], a[20:23], v[240:243]\n"
    "ds_read_b128 a[204:207], v11 offset:0\n"
    "v_mul_f32 v118, s65, v118\n"
    "v_mfma_f32_16x16x32_bf16 v[252:255], a[252:255], a[20:23], v[252:255]\n"
    "v_mul_f32 v119, s65, v119\n"
    "v_cvt_pk_bf16_f32 v116, v116, v117\n"
    "v_mfma_f32_16x16x32_bf16 v[248:251], a[252:255], a[16:19], v[248:251]\n"
    "v_cvt_pk_bf16_f32 v117, v118, v119\n"
    "v_mul_f32 v128, s65, v128\n"
    "v_mfma_f32_16x16x32_bf16 v[244:247], a[252:255], a[12:15], v[244:247]\n"
    "s_waitcnt lgkmcnt(6)\n"
    "ds_read_b128 a[236:239], v11 offset:8192\n"
    "v_mul_f32 v129, s65, v129\n"
    "v_mfma_f32_16x16x32_bf16 v[88:91], a[192:195], v[64:67], 0\n"
    "v_mul_f32 v130, s65, v130\n"
    "v_mul_f32 v131, s65, v131\n"
    "v_mfma_f32_16x16x32_bf16 v[92:95], a[192:195], v[68:71], 0\n"
    "v_cvt_pk_bf16_f32 v118, v128, v129\n"
    "v_cvt_pk_bf16_f32 v119, v130, v131\n"
    "v_mfma_f32_16x16x32_bf16 v[96:99], a[192:195], v[72:75], 0\n"
    "s_waitcnt lgkmcnt(6)\n"
    "ds_read_b128 a[208:211], v8 offset:256\n"
    "v_mul_f32 v120, s65, v120\n"
    "v_mfma_f32_16x16x32_bf16 v[108:111], a[224:227], v[72:75], 0\n"
    "v_mul_f32 v121, s65, v121\n"
    "v_mul_f32 v122, s65, v122\n"
    "v_mfma_f32_16x16x32_bf16 v[104:107], a[224:227], v[68:71], 0\n"
    "v_mul_f32 v123, s65, v123\n"
    "v_cvt_pk_bf16_f32 v120, v120, v121\n"
    "v_mfma_f32_16x16x32_bf16 v[100:103], a[224:227], v[64:67], 0\n"
    "s_waitcnt lgkmcnt(6)\n"
    "ds_read_b128 a[240:243], v8 offset:8448\n"
    "v_cvt_pk_bf16_f32 v121, v122, v123\n"
    "v_mfma_f32_16x16x32_bf16 v[88:91], a[196:199], v[76:79], v[88:91]\n"
    "v_mul_f32 v132, s65, v132\n"
    "v_mul_f32 v133, s65, v133\n"
    "v_mfma_f32_16x16x32_bf16 v[92:95], a[196:199], v[80:83], v[92:95]\n"
    "v_mul_f32 v134, s65, v134\n"
    "v_mul_f32 v135, s65, v135\n"
    "v_mfma_f32_16x16x32_bf16 v[96:99], a[196:199], v[84:87], v[96:99]\n"
    "s_waitcnt lgkmcnt(6)\n"
    "ds_read_b128 a[212:215], v9 offset:256\n"
    "v_cvt_pk_bf16_f32 v122, v132, v133\n"
    "v_mfma_f32_16x16x32_bf16 v[108:111], a[228:231], v[84:87], v[108:111]\n"
    "v_cvt_pk_bf16_f32 v123, v134, v135\n"
    "v_mul_f32 v136, s65, v136\n"
    "v_mfma_f32_16x16x32_bf16 v[104:107], a[228:231], v[80:83], v[104:107]\n"
    "v_mul_f32 v137, s65, v137\n"
    "v_mul_f32 v138, s65, v138\n"
    "v_mfma_f32_16x16x32_bf16 v[100:103], a[228:231], v[76:79], v[100:103]\n"
    "s_waitcnt lgkmcnt(6)\n"
    "ds_read_b128 a[244:247], v9 offset:8448\n"
    "v_mul_f32 v139, s65, v139\n"
    "v_mfma_f32_16x16x32_bf16 v[88:91], a[200:203], v[112:115], v[88:91]\n"
    "v_cvt_pk_bf16_f32 v124, v136, v137\n"
    "v_cvt_pk_bf16_f32 v125, v138, v139\n"
    "v_mfma_f32_16x16x32_bf16 v[92:95], a[200:203], v[116:119], v[92:95]\n"
    "v_mul_f32 v148, s65, v148\n"
    "v_mul_f32 v149, s65, v149\n"
    "v_mfma_f32_16x16x32_bf16 v[96:99], a[200:203], v[120:123], v[96:99]\n"
    "s_waitcnt lgkmcnt(6)\n"
    "ds_read_b128 a[216:219], v10 offset:256\n"
    "v_mul_f32 v150, s65, v150\n"
    "v_mfma_f32_16x16x32_bf16 v[108:111], a[232:235], v[120:123], v[108:111]\n"
    "v_mul_f32 v151, s65, v151\n"
    "v_cvt_pk_bf16_f32 v126, v148, v149\n"
    "v_mfma_f32_16x16x32_bf16 v[104:107], a[232:235], v[116:119], v[104:107]\n"
    "v_cvt_pk_bf16_f32 v127, v150, v151\n"
    "v_mul_f32 v140, s65, v140\n"
    "v_mfma_f32_16x16x32_bf16 v[100:103], a[232:235], v[112:115], v[100:103]\n"
    "s_waitcnt lgkmcnt(6)\n"
    "ds_read_b128 a[248:251], v10 offset:8448\n"
    "v_mul_f32 v141, s65, v141\n"
    "v_mul_f32 v142, s65, v142\n"
    "v_mul_f32 v143, s65, v143\n"
    "v_cvt_pk_bf16_f32 v128, v140, v141\n"
    "v_cvt_pk_bf16_f32 v129, v142, v143\n"
    "v_mul_f32 v152, s65, v152\n"
    "v_mul_f32 v153, s65, v153\n"
    "v_mul_f32 v154, s65, v154\n"
    "v_mul_f32 v155, s65, v155\n"
    "v_cvt_pk_bf16_f32 v130, v152, v153\n"
    "v_cvt_pk_bf16_f32 v131, v154, v155\n"
    "v_mul_f32 v144, s65, v144\n"
    "v_mul_f32 v145, s65, v145\n"
    "v_mul_f32 v146, s65, v146\n"
    "v_mul_f32 v147, s65, v147\n"
    "v_cvt_pk_bf16_f32 v132, v144, v145\n"
    "v_cvt_pk_bf16_f32 v133, v146, v147\n"
    "v_mul_f32 v156, s65, v156\n"
    "v_mul_f32 v157, s65, v157\n"
    "v_mul_f32 v158, s65, v158\n"
    "v_mul_f32 v159, s65, v159\n"
    "v_cvt_pk_bf16_f32 v134, v156, v157\n"
    "v_cvt_pk_bf16_f32 v135, v158, v159\n"
    "v_mfma_f32_16x16x32_bf16 v[88:91], a[204:207], v[124:127], v[88:91]\n"
    "v_mul_f32 v160, s65, v160\n"
    "v_mul_f32 v161, s65, v161\n"
    "v_mfma_f32_16x16x32_bf16 v[92:95], a[204:207], v[128:131], v[92:95]\n"
    "v_mul_f32 v162, s65, v162\n"
    "v_mul_f32 v163, s65, v163\n"
    "v_mfma_f32_16x16x32_bf16 v[96:99], a[204:207], v[132:135], v[96:99]\n"
    "s_waitcnt lgkmcnt(6)\n"
    "ds_read_b128 a[220:223], v11 offset:256\n"
    "v_cvt_pk_bf16_f32 v160, v160, v161\n"
    "v_mfma_f32_16x16x32_bf16 v[108:111], a[236:239], v[132:135], v[108:111]\n"
    "v_cvt_pk_bf16_f32 v161, v162, v163\n"
    "v_mul_f32 v172, s65, v172\n"
    "v_mfma_f32_16x16x32_bf16 v[104:107], a[236:239], v[128:131], v[104:107]\n"
    "v_mul_f32 v173, s65, v173\n"
    "v_mul_f32 v174, s65, v174\n"
    "v_mfma_f32_16x16x32_bf16 v[100:103], a[236:239], v[124:127], v[100:103]\n"
    "s_waitcnt lgkmcnt(6)\n"
    "ds_read_b128 a[252:255], v11 offset:8448\n"
    "v_mul_f32 v175, s65, v175\n"
    "v_cvt_pk_bf16_f32 v162, v172, v173\n"
    "v_cvt_pk_bf16_f32 v163, v174, v175\n"
    "v_mul_f32 v164, s65, v164\n"
    "v_mul_f32 v165, s65, v165\n"
    "v_mul_f32 v166, s65, v166\n"
    "v_mul_f32 v167, s65, v167\n"
    "v_cvt_pk_bf16_f32 v164, v164, v165\n"
    "v_cvt_pk_bf16_f32 v165, v166, v167\n"
    "v_mul_f32 v176, s65, v176\n"
    "v_mul_f32 v177, s65, v177\n"
    "v_mul_f32 v178, s65, v178\n"
    "v_mul_f32 v179, s65, v179\n"
    "v_cvt_pk_bf16_f32 v166, v176, v177\n"
    "v_cvt_pk_bf16_f32 v167, v178, v179\n"
    "v_mul_f32 v168, s65, v168\n"
    "v_mul_f32 v169, s65, v169\n"
    "v_mul_f32 v170, s65, v170\n"
    "v_mul_f32 v171, s65, v171\n"
    "v_cvt_pk_bf16_f32 v168, v168, v169\n"
    "v_cvt_pk_bf16_f32 v169, v170, v171\n"
    "v_mul_f32 v180, s65, v180\n"
    "v_mul_f32 v181, s65, v181\n"
    "v_mul_f32 v182, s65, v182\n"
    "v_mul_f32 v183, s65, v183\n"
    "v_cvt_pk_bf16_f32 v170, v180, v181\n"
    "v_cvt_pk_bf16_f32 v171, v182, v183\n"
    "v_mfma_f32_16x16x32_bf16 v[88:91], a[208:211], v[160:163], v[88:91]\n"
    "v_mul_f32 v184, s65, v184\n"
    "v_mul_f32 v185, s65, v185\n"
    "v_mfma_f32_16x16x32_bf16 v[92:95], a[208:211], v[164:167], v[92:95]\n"
    "v_mul_f32 v186, s65, v186\n"
    "v_mul_f32 v187, s65, v187\n"
    "v_mfma_f32_16x16x32_bf16 v[96:99], a[208:211], v[168:171], v[96:99]\n"
    "s_waitcnt lgkmcnt(6)\n"
    "v_cvt_pk_bf16_f32 v172, v184, v185\n"
    "v_cvt_pk_bf16_f32 v173, v186, v187\n"
    "v_mfma_f32_16x16x32_bf16 v[108:111], a[240:243], v[168:171], v[108:111]\n"
    "v_mul_f32 v196, s65, v196\n"
    "v_mul_f32 v197, s65, v197\n"
    "v_mfma_f32_16x16x32_bf16 v[104:107], a[240:243], v[164:167], v[104:107]\n"
    "v_mul_f32 v198, s65, v198\n"
    "v_mul_f32 v199, s65, v199\n"
    "v_mfma_f32_16x16x32_bf16 v[100:103], a[240:243], v[160:163], v[100:103]\n"
    "s_waitcnt lgkmcnt(5)\n"
    "v_cvt_pk_bf16_f32 v174, v196, v197\n"
    "v_cvt_pk_bf16_f32 v175, v198, v199\n"
    "v_mul_f32 v188, s65, v188\n"
    "v_mul_f32 v189, s65, v189\n"
    "v_mul_f32 v190, s65, v190\n"
    "v_mul_f32 v191, s65, v191\n"
    "v_cvt_pk_bf16_f32 v176, v188, v189\n"
    "v_cvt_pk_bf16_f32 v177, v190, v191\n"
    "v_mul_f32 v200, s65, v200\n"
    "v_mul_f32 v201, s65, v201\n"
    "v_mul_f32 v202, s65, v202\n"
    "v_mul_f32 v203, s65, v203\n"
    "v_cvt_pk_bf16_f32 v178, v200, v201\n"
    "v_cvt_pk_bf16_f32 v179, v202, v203\n"
    "v_mul_f32 v192, s65, v192\n"
    "v_mul_f32 v193, s65, v193\n"
    "v_mul_f32 v194, s65, v194\n"
    "v_mul_f32 v195, s65, v195\n"
    "v_cvt_pk_bf16_f32 v180, v192, v193\n"
    "v_cvt_pk_bf16_f32 v181, v194, v195\n"
    "v_mul_f32 v204, s65, v204\n"
    "v_mul_f32 v205, s65, v205\n"
    "v_mul_f32 v206, s65, v206\n"
    "v_mul_f32 v207, s65, v207\n"
    "v_cvt_pk_bf16_f32 v182, v204, v205\n"
    "v_cvt_pk_bf16_f32 v183, v206, v207\n"
    "v_mfma_f32_16x16x32_bf16 v[88:91], a[212:215], v[172:175], v[88:91]\n"
    "v_mul_f32 v208, s65, v208\n"
    "v_mul_f32 v209, s65, v209\n"
    "v_mfma_f32_16x16x32_bf16 v[92:95], a[212:215], v[176:179], v[92:95]\n"
    "v_mul_f32 v210, s65, v210\n"
    "v_mul_f32 v211, s65, v211\n"
    "v_mfma_f32_16x16x32_bf16 v[96:99], a[212:215], v[180:183], v[96:99]\n"
    "s_waitcnt lgkmcnt(4)\n"
    "v_cvt_pk_bf16_f32 v208, v208, v209\n"
    "v_cvt_pk_bf16_f32 v209, v210, v211\n"
    "v_mfma_f32_16x16x32_bf16 v[108:111], a[244:247], v[180:183], v[108:111]\n"
    "v_mul_f32 v220, s65, v220\n"
    "v_mul_f32 v221, s65, v221\n"
    "v_mfma_f32_16x16x32_bf16 v[104:107], a[244:247], v[176:179], v[104:107]\n"
    "v_mul_f32 v222, s65, v222\n"
    "v_mul_f32 v223, s65, v223\n"
    "v_mfma_f32_16x16x32_bf16 v[100:103], a[244:247], v[172:175], v[100:103]\n"
    "s_waitcnt lgkmcnt(0)\n"
    "s_barrier\n"
    "v_cvt_pk_bf16_f32 v210, v220, v221\n"
    "v_cvt_pk_bf16_f32 v211, v222, v223\n"
    "v_mul_f32 v212, s65, v212\n"
    "v_mul_f32 v213, s65, v213\n"
    "v_mul_f32 v214, s65, v214\n"
    "v_mul_f32 v215, s65, v215\n"
    "v_cvt_pk_bf16_f32 v212, v212, v213\n"
    "v_cvt_pk_bf16_f32 v213, v214, v215\n"
    "v_mul_f32 v224, s65, v224\n"
    "v_mul_f32 v225, s65, v225\n"
    "v_mul_f32 v226, s65, v226\n"
    "v_mul_f32 v227, s65, v227\n"
    "v_cvt_pk_bf16_f32 v214, v224, v225\n"
    "v_cvt_pk_bf16_f32 v215, v226, v227\n"
    "v_mul_f32 v216, s65, v216\n"
    "v_mul_f32 v217, s65, v217\n"
    "v_mul_f32 v218, s65, v218\n"
    "v_mul_f32 v219, s65, v219\n"
    "v_cvt_pk_bf16_f32 v216, v216, v217\n"
    "v_cvt_pk_bf16_f32 v217, v218, v219\n"
    "v_mul_f32 v228, s65, v228\n"
    "v_mul_f32 v229, s65, v229\n"
    "v_mul_f32 v230, s65, v230\n"
    "v_mul_f32 v231, s65, v231\n"
    "v_cvt_pk_bf16_f32 v218, v228, v229\n"
    "v_cvt_pk_bf16_f32 v219, v230, v231\n"
    "v_mfma_f32_16x16x32_bf16 v[88:91], a[216:219], v[208:211], v[88:91]\n"
    "ds_read_b128 a[192:195], v8 offset:16384\n"
    "v_mul_f32 v232, s65, v232\n"
    "v_mfma_f32_16x16x32_bf16 v[92:95], a[216:219], v[212:215], v[92:95]\n"
    "ds_read_b128 a[196:199], v9 offset:16384\n"
    "v_mul_f32 v233, s65, v233\n"
    "v_mfma_f32_16x16x32_bf16 v[96:99], a[216:219], v[216:219], v[96:99]\n"
    "ds_read_b128 a[200:203], v10 offset:16384\n"
    "v_mul_f32 v234, s65, v234\n"
    "v_mfma_f32_16x16x32_bf16 v[108:111], a[248:251], v[216:219], v[108:111]\n"
    "ds_read_b128 a[204:207], v11 offset:16384\n"
    "v_mul_f32 v235, s65, v235\n"
    "v_mfma_f32_16x16x32_bf16 v[104:107], a[248:251], v[212:215], v[104:107]\n"
    "ds_read_b128 a[208:211], v8 offset:16640\n"
    "v_cvt_pk_bf16_f32 v220, v232, v233\n"
    "v_mfma_f32_16x16x32_bf16 v[100:103], a[248:251], v[208:211], v[100:103]\n"
    "ds_read_b128 a[212:215], v9 offset:16640\n"
    "v_cvt_pk_bf16_f32 v221, v234, v235\n"
    "v_mul_f32 v244, s65, v244\n"
    "v_mul_f32 v245, s65, v245\n"
    "v_mul_f32 v246, s65, v246\n"
    "v_mul_f32 v247, s65, v247\n"
    "v_cvt_pk_bf16_f32 v222, v244, v245\n"
    "v_cvt_pk_bf16_f32 v223, v246, v247\n"
    "v_mul_f32 v236, s65, v236\n"
    "v_mul_f32 v237, s65, v237\n"
    "v_mul_f32 v238, s65, v238\n"
    "v_mul_f32 v239, s65, v239\n"
    "v_cvt_pk_bf16_f32 v224, v236, v237\n"
    "v_cvt_pk_bf16_f32 v225, v238, v239\n"
    "v_mul_f32 v248, s65, v248\n"
    "v_mul_f32 v249, s65, v249\n"
    "v_mul_f32 v250, s65, v250\n"
    "v_mul_f32 v251, s65, v251\n"
    "v_cvt_pk_bf16_f32 v226, v248, v249\n"
    "v_cvt_pk_bf16_f32 v227, v250, v251\n"
    "v_mul_f32 v240, s65, v240\n"
    "v_mul_f32 v241, s65, v241\n"
    "v_mul_f32 v242, s65, v242\n"
    "v_mul_f32 v243, s65, v243\n"
    "v_cvt_pk_bf16_f32 v228, v240, v241\n"
    "v_cvt_pk_bf16_f32 v229, v242, v243\n"
    "v_mul_f32 v252, s65, v252\n"
    "v_mul_f32 v253, s65, v253\n"
    "v_mul_f32 v254, s65, v254\n"
    "v_mul_f32 v255, s65, v255\n"
    "v_cvt_pk_bf16_f32 v230, v252, v253\n"
    "v_cvt_pk_bf16_f32 v231, v254, v255\n"
    "v_mfma_f32_16x16x32_bf16 v[88:91], a[220:223], v[220:223], v[88:91]\n"
    "ds_read_b128 a[216:219], v10 offset:16640\n"
    "v_mfma_f32_16x16x32_bf16 v[92:95], a[220:223], v[224:227], v[92:95]\n"
    "v_mfma_f32_16x16x32_bf16 v[96:99], a[220:223], v[228:231], v[96:99]\n"
    "ds_read_b128 a[220:223], v11 offset:16640\n"
    "v_mfma_f32_16x16x32_bf16 v[108:111], a[252:255], v[228:231], v[108:111]\n"
    "v_mfma_f32_16x16x32_bf16 v[104:107], a[252:255], v[224:227], v[104:107]\n"
    "v_mfma_f32_16x16x32_bf16 v[100:103], a[252:255], v[220:223], v[100:103]\n"
    "s_waitcnt lgkmcnt(6)\n"
    "v_max3_f32 v244, v88, v89, v90\n"
    "s_nop 5\n"
    "v_max3_f32 v244, v244, v91, v100\n"
    "v_max3_f32 v244, v244, v101, v102\n"
    "v_max_f32 v244, v244, v103\n"
    "v_max3_f32 v245, v92, v93, v94\n"
    "v_max3_f32 v245, v245, v95, v104\n"
    "v_max3_f32 v245, v245, v105, v106\n"
    "v_max_f32 v245, v245, v107\n"
    "v_max3_f32 v246, v96, v97, v98\n"
    "v_max3_f32 v246, v246, v99, v108\n"
    "v_max3_f32 v246, v246, v109, v110\n"
    "v_max_f32 v246, v246, v111\n"
    "v_mov_b32 v247, v244\n"
    "v_mov_b32 v248, v245\n"
    "v_mov_b32 v249, v246\n"
    "v_permlane32_swap_b32 v247, v244\n"
    "v_permlane32_swap_b32 v248, v245\n"
    "v_permlane32_swap_b32 v249, v246\n"
    "v_max_f32 v244, v247, v244\n"
    "v_max_f32 v245, v248, v245\n"
    "v_max_f32 v246, v249, v246\n"
    "v_mov_b32 v247, v244\n"
    "v_mov_b32 v248, v245\n"
    "v_mov_b32 v249, v246\n"
    "v_permlane16_swap_b32 v247, v244\n"
    "v_permlane16_swap_b32 v248, v245\n"
    "v_permlane16_swap_b32 v249, v246\n"
    "v_max_f32 v244, v247, v244\n"
    "v_max_f32 v245, v248, v245\n"
    "v_max_f32 v246, v249, v246\n"
    "v_sub_f32 v232, 0, v244\n"
    "v_sub_f32 v233, 0, v244\n"
    "v_sub_f32 v234, 0, v244\n"
    "v_sub_f32 v235, 0, v244\n"
    "v_sub_f32 v236, 0, v245\n"
    "v_sub_f32 v237, 0, v245\n"
    "v_sub_f32 v238, 0, v245\n"
    "v_sub_f32 v239, 0, v245\n"
    "v_sub_f32 v240, 0, v246\n"
    "v_sub_f32 v241, 0, v246\n"
    "v_sub_f32 v242, 0, v246\n"
    "v_sub_f32 v243, 0, v246\n"
    "v_mfma_f32_16x16x32_bf16 v[136:139], a[192:195], v[64:67], v[232:235]\n"
    "s_waitcnt lgkmcnt(5)\n"
    "v_add_f32 v88, v88, v232\n"
    "v_add_f32 v89, v89, v232\n"
    "v_mfma_f32_16x16x32_bf16 v[136:139], a[196:199], v[76:79], v[136:139]\n"
    "v_exp_f32 v244, v88\n"
    "v_mfma_f32_16x16x32_bf16 v[136:139], a[200:203], v[112:115], v[136:139]\n"
    "s_waitcnt lgkmcnt(3)\n"
    "ds_read_b128 a[252:255], v11 offset:24832\n"
    "v_add_f32 v90, v90, v232\n"
    "v_mfma_f32_16x16x32_bf16 v[136:139], a[204:207], v[124:127], v[136:139]\n"
    "ds_read_b128 a[248:251], v10 offset:24832\n"
    "v_add_f32 v91, v91, v232\n"
    "v_mfma_f32_16x16x32_bf16 v[136:139], a[208:211], v[160:163], v[136:139]\n"
    "s_waitcnt lgkmcnt(3)\n"
    "ds_read_b128 a[244:247], v9 offset:24832\n"
    "v_add_f32 v92, v92, v236\n"
    "v_mfma_f32_16x16x32_bf16 v[136:139], a[212:215], v[172:175], v[136:139]\n"
    "ds_read_b128 a[240:243], v8 offset:24832\n"
    "v_add_f32 v93, v93, v236\n"
    "v_mfma_f32_16x16x32_bf16 v[136:139], a[216:219], v[208:211], v[136:139]\n"
    "s_waitcnt lgkmcnt(4)\n"
    "ds_read_b128 a[236:239], v11 offset:24576\n"
    "v_add_f32 v94, v94, v236\n"
    "v_mfma_f32_16x16x32_bf16 v[136:139], a[220:223], v[220:223], v[136:139]\n"
    "ds_read_b128 a[232:235], v10 offset:24576\n"
    "v_add_f32 v95, v95, v236\n"
    "v_mfma_f32_16x16x32_bf16 v[140:143], a[220:223], v[224:227], v[236:239]\n"
    "ds_read_b128 a[228:231], v9 offset:24576\n"
    "v_add_f32 v96, v96, v240\n"
    "v_mfma_f32_16x16x32_bf16 v[140:143], a[216:219], v[212:215], v[140:143]\n"
    "ds_read_b128 a[224:227], v8 offset:24576\n"
    "v_add_f32 v97, v97, v240\n"
    "v_mfma_f32_16x16x32_bf16 v[140:143], a[212:215], v[176:179], v[140:143]\n"
    "v_exp_f32 v245, v89\n"
    "v_mfma_f32_16x16x32_bf16 v[140:143], a[208:211], v[164:167], v[140:143]\n"
    "v_exp_f32 v246, v90\n"
    "v_mfma_f32_16x16x32_bf16 v[140:143], a[204:207], v[128:131], v[140:143]\n"
    "v_exp_f32 v247, v91\n"
    "v_mfma_f32_16x16x32_bf16 v[140:143], a[200:203], v[116:119], v[140:143]\n"
    "v_exp_f32 v248, v92\n"
    "v_mfma_f32_16x16x32_bf16 v[140:143], a[196:199], v[80:83], v[140:143]\n"
    "v_exp_f32 v249, v93\n"
    "v_mfma_f32_16x16x32_bf16 v[140:143], a[192:195], v[68:71], v[140:143]\n"
    "v_exp_f32 v250, v94\n"
    "v_mfma_f32_16x16x32_bf16 v[144:147], a[192:195], v[72:75], v[240:243]\n"
    "v_exp_f32 v251, v95\n"
    "v_mfma_f32_16x16x32_bf16 v[144:147], a[196:199], v[84:87], v[144:147]\n"
    "v_add_f32 v98, v98, v240\n"
    "v_add_f32 v99, v99, v240\n"
    "v_mfma_f32_16x16x32_bf16 v[144:147], a[200:203], v[120:123], v[144:147]\n"
    "v_exp_f32 v252, v96\n"
    "v_mfma_f32_16x16x32_bf16 v[144:147], a[204:207], v[132:135], v[144:147]\n"
    "v_exp_f32 v253, v97\n"
    "v_mfma_f32_16x16x32_bf16 v[144:147], a[208:211], v[168:171], v[144:147]\n"
    "v_add_f32 v100, v100, v232\n"
    "v_add_f32 v101, v101, v232\n"
    "v_mfma_f32_16x16x32_bf16 v[144:147], a[212:215], v[180:183], v[144:147]\n"
    "v_add_f32 v102, v102, v232\n"
    "v_add_f32 v103, v103, v232\n"
    "v_mfma_f32_16x16x32_bf16 v[144:147], a[216:219], v[216:219], v[144:147]\n"
    "v_add_f32 v104, v104, v236\n"
    "v_add_f32 v105, v105, v236\n"
    "v_mfma_f32_16x16x32_bf16 v[144:147], a[220:223], v[228:231], v[144:147]\n"
    "s_waitcnt lgkmcnt(6)\n"
    "v_add_f32 v106, v106, v236\n"
    "v_add_f32 v107, v107, v236\n"
    "v_mfma_f32_16x16x32_bf16 v[148:151], a[252:255], v[220:223], v[232:235]\n"
    "v_add_f32 v108, v108, v240\n"
    "v_add_f32 v109, v109, v240\n"
    "v_mfma_f32_16x16x32_bf16 v[148:151], a[248:251], v[208:211], v[148:151]\n"
    "s_waitcnt lgkmcnt(4)\n"
    "v_add_f32 v110, v110, v240\n"
    "v_add_f32 v111, v111, v240\n"
    "v_mfma_f32_16x16x32_bf16 v[148:151], a[244:247], v[172:175], v[148:151]\n"
    "v_add_f32 v30, v244, v245\n"
    "v_cvt_pk_bf16_f32 v184, v244, v245\n"
    "v_mfma_f32_16x16x32_bf16 v[148:151], a[240:243], v[160:163], v[148:151]\n"
    "s_waitcnt lgkmcnt(2)\n"
    "v_exp_f32 v244, v98\n"
    "v_mfma_f32_16x16x32_bf16 v[148:151], a[236:239], v[124:127], v[148:151]\n"
    "v_exp_f32 v245, v99\n"
    "v_mfma_f32_16x16x32_bf16 v[148:151], a[232:235], v[112:115], v[148:151]\n"
    "s_waitcnt lgkmcnt(0)\n"
    "v_add_f32 v30, v30, v246\n"
    "v_cvt_pk_bf16_f32 v185, v246, v247\n"
    "v_mfma_f32_16x16x32_bf16 v[148:151], a[228:231], v[76:79], v[148:151]\n"
    "v_exp_f32 v246, v100\n"
    "v_mfma_f32_16x16x32_bf16 v[148:151], a[224:227], v[64:67], v[148:151]\n"
    "s_waitcnt lgkmcnt(0)\n"
    "s_barrier\n"
    "v_add_f32 v30, v30, v247\n"
    "v_mfma_f32_16x16x32_bf16 v[152:155], a[224:227], v[68:71], v[236:239]\n"
    "ds_read_b128 a[192:195], v8 offset:32768\n"
    "v_add_f32 v31, v248, v249\n"
    "v_mfma_f32_16x16x32_bf16 v[152:155], a[228:231], v[80:83], v[152:155]\n"
    "ds_read_b128 a[196:199], v9 offset:32768\n"
    "v_cvt_pk_bf16_f32 v188, v248, v249\n"
    "v_mfma_f32_16x16x32_bf16 v[152:155], a[232:235], v[116:119], v[152:155]\n"
    "ds_read_b128 a[200:203], v10 offset:32768\n"
    "v_add_f32 v31, v31, v250\n"
    "v_mfma_f32_16x16x32_bf16 v[152:155], a[236:239], v[128:131], v[152:155]\n"
    "ds_read_b128 a[204:207], v11 offset:32768\n"
    "v_cvt_pk_bf16_f32 v189, v250, v251\n"
    "v_mfma_f32_16x16x32_bf16 v[152:155], a[240:243], v[164:167], v[152:155]\n"
    "ds_read_b128 a[208:211], v8 offset:33024\n"
    "v_add_f32 v31, v31, v251\n"
    "v_mfma_f32_16x16x32_bf16 v[152:155], a[244:247], v[176:179], v[152:155]\n"
    "ds_read_b128 a[212:215], v9 offset:33024\n"
    "v_add_f32 v32, v252, v253\n"
    "v_mfma_f32_16x16x32_bf16 v[152:155], a[248:251], v[212:215], v[152:155]\n"
    "ds_read_b128 a[216:219], v10 offset:33024\n"
    "v_cvt_pk_bf16_f32 v192, v252, v253\n"
    "v_mfma_f32_16x16x32_bf16 v[152:155], a[252:255], v[224:227], v[152:155]\n"
    "ds_read_b128 a[220:223], v11 offset:33024\n"
    "v_add_f32 v32, v32, v244\n"
    "v_mfma_f32_16x16x32_bf16 v[156:159], a[252:255], v[228:231], v[240:243]\n"
    "v_exp_f32 v247, v101\n"
    "v_mfma_f32_16x16x32_bf16 v[156:159], a[248:251], v[216:219], v[156:159]\n"
    "v_exp_f32 v248, v102\n"
    "v_mfma_f32_16x16x32_bf16 v[156:159], a[244:247], v[180:183], v[156:159]\n"
    "v_exp_f32 v249, v103\n"
    "v_mfma_f32_16x16x32_bf16 v[156:159], a[240:243], v[168:171], v[156:159]\n"
    "v_exp_f32 v250, v104\n"
    "v_mfma_f32_16x16x32_bf16 v[156:159], a[236:239], v[132:135], v[156:159]\n"
    "v_exp_f32 v251, v105\n"
    "v_mfma_f32_16x16x32_bf16 v[156:159], a[232:235], v[120:123], v[156:159]\n"
    "v_exp_f32 v252, v106\n"
    "v_mfma_f32_16x16x32_bf16 v[156:159], a[228:231], v[84:87], v[156:159]\n"
    "v_exp_f32 v253, v107\n"
    "v_mfma_f32_16x16x32_bf16 v[156:159], a[224:227], v[72:75], v[156:159]\n"
    "s_waitcnt lgkmcnt(6)\n"
    "v_cvt_pk_bf16_f32 v193, v244, v245\n"
    "v_add_f32 v32, v32, v245\n"
    "v_add_f32 v30, v30, v246\n"
    "v_cvt_pk_bf16_f32 v186, v246, v247\n"
    "v_add_f32 v30, v30, v247\n"
    "v_add_f32 v30, v30, v248\n"
    "v_cvt_pk_bf16_f32 v187, v248, v249\n"
    "v_add_f32 v30, v30, v249\n"
    "v_add_f32 v31, v31, v250\n"
    "v_cvt_pk_bf16_f32 v190, v250, v251\n"
    "v_add_f32 v31, v31, v251\n"
    "v_add_f32 v31, v31, v252\n"
    "v_cvt_pk_bf16_f32 v191, v252, v253\n"
    "v_add_f32 v31, v31, v253\n"
    "v_exp_f32 v244, v108\n"
    "v_exp_f32 v245, v109\n"
    "v_exp_f32 v246, v110\n"
    "v_exp_f32 v247, v111\n"
    "v_add_f32 v32, v32, v244\n"
    "v_cvt_pk_bf16_f32 v194, v244, v245\n"
    "v_add_f32 v32, v32, v245\n"
    "v_add_f32 v32, v32, v246\n"
    "v_cvt_pk_bf16_f32 v195, v246, v247\n"
    "v_add_f32 v32, v32, v247\n"
    "v_exp_f32 v248, v136\n"
    "v_exp_f32 v249, v137\n"
    "v_exp_f32 v250, v138\n"
    "v_exp_f32 v251, v139\n"
    "v_add_f32 v30, v30, v248\n"
    "v_cvt_pk_bf16_f32 v196, v248, v249\n"
    "v_add_f32 v30, v30, v249\n"
    "v_add_f32 v30, v30, v250\n"
    "v_cvt_pk_bf16_f32 v197, v250, v251\n"
    "v_add_f32 v30, v30, v251\n"
    "v_exp_f32 v252, v140\n"
    "v_exp_f32 v253, v141\n"
    "v_exp_f32 v244, v142\n"
    "v_exp_f32 v245, v143\n"
    "v_add_f32 v31, v31, v252\n"
    "v_cvt_pk_bf16_f32 v200, v252, v253\n"
    "v_add_f32 v31, v31, v253\n"
    "v_add_f32 v31, v31, v244\n"
    "v_cvt_pk_bf16_f32 v201, v244, v245\n"
    "v_add_f32 v31, v31, v245\n"
    "v_exp_f32 v246, v144\n"
    "v_exp_f32 v247, v145\n"
    "v_exp_f32 v248, v146\n"
    "v_exp_f32 v249, v147\n"
    "v_add_f32 v32, v32, v246\n"
    "v_cvt_pk_bf16_f32 v204, v246, v247\n"
    "v_add_f32 v32, v32, v247\n"
    "v_add_f32 v32, v32, v248\n"
    "v_cvt_pk_bf16_f32 v205, v248, v249\n"
    "v_add_f32 v32, v32, v249\n"
    "v_exp_f32 v250, v148\n"
    "v_exp_f32 v251, v149\n"
    "v_exp_f32 v252, v150\n"
    "v_exp_f32 v253, v151\n"
    "v_add_f32 v30, v30, v250\n"
    "v_cvt_pk_bf16_f32 v198, v250, v251\n"
    "v_add_f32 v30, v30, v251\n"
    "v_add_f32 v30, v30, v252\n"
    "v_cvt_pk_bf16_f32 v199, v252, v253\n"
    "v_add_f32 v30, v30, v253\n"
    "v_exp_f32 v244, v152\n"
    "v_exp_f32 v245, v153\n"
    "v_exp_f32 v246, v154\n"
    "v_exp_f32 v247, v155\n"
    "v_add_f32 v31, v31, v244\n"
    "v_cvt_pk_bf16_f32 v202, v244, v245\n"
    "v_add_f32 v31, v31, v245\n"
    "v_add_f32 v31, v31, v246\n"
    "v_cvt_pk_bf16_f32 v203, v246, v247\n"
    "v_add_f32 v31, v31, v247\n"
    "v_exp_f32 v248, v156\n"
    "v_exp_f32 v249, v157\n"
    "v_exp_f32 v250, v158\n"
    "v_exp_f32 v251, v159\n"
    "v_add_f32 v32, v32, v248\n"
    "v_cvt_pk_bf16_f32 v206, v248, v249\n"
    "v_add_f32 v32, v32, v249\n"
    "v_add_f32 v32, v32, v250\n"
    "v_cvt_pk_bf16_f32 v207, v250, v251\n"
    "v_add_f32 v32, v32, v251\n"
    "v_max3_f32 v254, v30, v31, v32\n"
    "v_cmp_lt_f32 vcc, s58, v254\n"
    "s_cbranch_vccnz Lstub1_%=\n"
    "Lok1_%=:\n"
    "v_mfma_f32_16x16x32_bf16 v[88:91], a[192:195], v[64:67], v[232:235]\n"
    "s_waitcnt lgkmcnt(5)\n"
    "s_mov_b32 s64, 6\n"
    "v_mov_b32 v27, v30\n"
    "v_mfma_f32_16x16x32_bf16 v[88:91], a[196:199], v[76:79], v[88:91]\n"
    "v_mov_b32 v28, v31\n"
    "v_mov_b32 v29, v32\n"
    "v_mfma_f32_16x16x32_bf16 v[88:91], a[200:203], v[112:115], v[88:91]\n"
    "s_waitcnt lgkmcnt(3)\n"
    "ds_read_b128 a[252:255], v11 offset:41216\n"
    "v_lshl_add_u32 v254, v3, 2, v4\n"
    "v_mfma_f32_16x16x32_bf16 v[88:91], a[204:207], v[124:127], v[88:91]\n"
    "ds_read_b128 a[248:251], v10 offset:41216\n"
    "v_lshlrev_b32 v255, 7, v254\n"
    "v_mfma_f32_16x16x32_bf16 v[88:91], a[208:211], v[160:163], v[88:91]\n"
    "s_waitcnt lgkmcnt(3)\n"
    "ds_read_b128 a[244:247], v9 offset:41216\n"
    "v_lshl_add_u32 v255, v5, 3, v255\n"
    "v_mfma_f32_16x16x32_bf16 v[88:91], a[212:215], v[172:175], v[88:91]\n"
    "ds_read_b128 a[240:243], v8 offset:41216\n"
    "v_add_u32 v255, s99, v255\n"
    "v_mfma_f32_16x16x32_bf16 v[88:91], a[216:219], v[208:211], v[88:91]\n"
    "s_waitcnt lgkmcnt(4)\n"
    "ds_read_b128 a[236:239], v11 offset:40960\n"
    "v_add_u32 v255, 131072, v255\n"
    "v_mfma_f32_16x16x32_bf16 v[88:91], a[220:223], v[220:223], v[88:91]\n"
    "ds_read_b128 a[232:235], v10 offset:40960\n"
    "v_bfe_u32 v254, v254, 1, 2\n"
    "v_mfma_f32_16x16x32_bf16 v[92:95], a[220:223], v[224:227], v[236:239]\n"
    "ds_read_b128 a[228:231], v9 offset:40960\n"
    "v_xor_b32 v33, 0, v254\n"
    "v_mfma_f32_16x16x32_bf16 v[92:95], a[216:219], v[212:215], v[92:95]\n"
    "ds_read_b128 a[224:227], v8 offset:40960\n"
    "v_lshl_add_u32 v20, v33, 5, v255\n"
    "v_mfma_f32_16x16x32_bf16 v[92:95], a[212:215], v[176:179], v[92:95]\n"
    "v_exp_f32 v244, v88\n"
    "v_mfma_f32_16x16x32_bf16 v[92:95], a[208:211], v[164:167], v[92:95]\n"
    "v_exp_f32 v245, v89\n"
    "v_mfma_f32_16x16x32_bf16 v[92:95], a[204:207], v[128:131], v[92:95]\n"
    "v_exp_f32 v246, v90\n"
    "v_mfma_f32_16x16x32_bf16 v[92:95], a[200:203], v[116:119], v[92:95]\n"
    "v_exp_f32 v247, v91\n"
    "v_mfma_f32_16x16x32_bf16 v[92:95], a[196:199], v[80:83], v[92:95]\n"
    "v_xor_b32 v33, 1, v254\n"
    "v_lshl_add_u32 v21, v33, 5, v255\n"
    "v_mfma_f32_16x16x32_bf16 v[92:95], a[192:195], v[68:71], v[92:95]\n"
    "v_xor_b32 v33, 2, v254\n"
    "v_lshl_add_u32 v22, v33, 5, v255\n"
    "v_mfma_f32_16x16x32_bf16 v[96:99], a[192:195], v[72:75], v[240:243]\n"
    "v_xor_b32 v33, 3, v254\n"
    "v_lshl_add_u32 v23, v33, 5, v255\n"
    "v_mfma_f32_16x16x32_bf16 v[96:99], a[196:199], v[84:87], v[96:99]\n"
    "v_add_f32 v30, v244, v245\n"
    "v_cvt_pk_bf16_f32 v40, v244, v245\n"
    "v_mfma_f32_16x16x32_bf16 v[96:99], a[200:203], v[120:123], v[96:99]\n"
    "v_exp_f32 v248, v92\n"
    "v_mfma_f32_16x16x32_bf16 v[96:99], a[204:207], v[132:135], v[96:99]\n"
    "v_exp_f32 v249, v93\n"
    "v_mfma_f32_16x16x32_bf16 v[96:99], a[208:211], v[168:171], v[96:99]\n"
    "v_exp_f32 v250, v94\n"
    "v_mfma_f32_16x16x32_bf16 v[96:99], a[212:215], v[180:183], v[96:99]\n"
    "v_exp_f32 v251, v95\n"
    "v_mfma_f32_16x16x32_bf16 v[96:99], a[216:219], v[216:219], v[96:99]\n"
    "v_add_f32 v30, v30, v246\n"
    "v_cvt_pk_bf16_f32 v41, v246, v247\n"
    "v_mfma_f32_16x16x32_bf16 v[96:99], a[220:223], v[228:231], v[96:99]\n"
    "s_waitcnt lgkmcnt(6)\n"
    "v_add_f32 v30, v30, v247\n"
    "v_add_f32 v31, v248, v249\n"
    "v_mfma_f32_16x16x32_bf16 v[100:103], a[252:255], v[220:223], v[232:235]\n"
    "v_cvt_pk_bf16_f32 v44, v248, v249\n"
    "v_add_f32 v31, v31, v250\n"
    "v_mfma_f32_16x16x32_bf16 v[100:103], a[248:251], v[208:211], v[100:103]\n"
    "s_waitcnt lgkmcnt(4)\n"
    "v_cvt_pk_bf16_f32 v45, v250, v251\n"
    "v_add_f32 v31, v31, v251\n"
    "v_mfma_f32_16x16x32_bf16 v[100:103], a[244:247], v[172:175], v[100:103]\n"
    "ds_read_b128 a[192:195], v8 offset:49152\n"
    "v_mfma_f32_16x16x32_bf16 v[100:103], a[240:243], v[160:163], v[100:103]\n"
    "s_waitcnt lgkmcnt(3)\n"
    "ds_read_b128 a[196:199], v9 offset:49152\n"
    "v_mfma_f32_16x16x32_bf16 v[100:103], a[236:239], v[124:127], v[100:103]\n"
    "ds_read_b128 a[200:203], v10 offset:49152\n"
    "v_mfma_f32_16x16x32_bf16 v[100:103], a[232:235], v[112:115], v[100:103]\n"
    "s_waitcnt lgkmcnt(3)\n"
    "ds_read_b128 a[204:207], v11 offset:49152\n"
    "v_mfma_f32_16x16x32_bf16 v[100:103], a[228:231], v[76:79], v[100:103]\n"
    "ds_read_b128 a[208:211], v8 offset:49408\n"
    "v_mfma_f32_16x16x32_bf16 v[100:103], a[224:227], v[64:67], v[100:103]\n"
    "ds_read_b128 a[212:215], v9 offset:49408\n"
    "v_mfma_f32_16x16x32_bf16 v[104:107], a[224:227], v[68:71], v[236:239]\n"
    "ds_read_b128 a[216:219], v10 offset:49408\n"
    "v_mfma_f32_16x16x32_bf16 v[104:107], a[228:231], v[80:83], v[104:107]\n"
    "ds_read_b128 a[220:223], v11 offset:49408\n"
    "v_mfma_f32_16x16x32_bf16 v[104:107], a[232:235], v[116:119], v[104:107]\n"
    "v_exp_f32 v252, v96\n"
    "v_mfma_f32_16x16x32_bf16 v[104:107], a[236:239], v[128:131], v[104:107]\n"
    "v_exp_f32 v253, v97\n"
    "v_mfma_f32_16x16x32_bf16 v[104:107], a[240:243], v[164:167], v[104:107]\n"
    "v_exp_f32 v244, v98\n"
    "v_mfma_f32_16x16x32_bf16 v[104:107], a[244:247], v[176:179], v[104:107]\n"
    "v_exp_f32 v245, v99\n"
    "v_mfma_f32_16x16x32_bf16 v[104:107], a[248:251], v[212:215], v[104:107]\n"
    "v_exp_f32 v246, v100\n"
    "v_mfma_f32_16x16x32_bf16 v[104:107], a[252:255], v[224:227], v[104:107]\n"
    "v_exp_f32 v247, v101\n"
    "v_mfma_f32_16x16x32_bf16 v[108:111], a[252:255], v[228:231], v[240:243]\n"
    "v_exp_f32 v248, v102\n"
    "v_mfma_f32_16x16x32_bf16 v[108:111], a[248:251], v[216:219], v[108:111]\n"
    "v_exp_f32 v249, v103\n"
    "v_mfma_f32_16x16x32_bf16 v[108:111], a[244:247], v[180:183], v[108:111]\n"
    "v_add_f32 v32, v252, v253\n"
    "v_cvt_pk_bf16_f32 v48, v252, v253\n"
    "v_mfma_f32_16x16x32_bf16 v[108:111], a[240:243], v[168:171], v[108:111]\n"
    "v_exp_f32 v250, v104\n"
    "v_mfma_f32_16x16x32_bf16 v[108:111], a[236:239], v[132:135], v[108:111]\n"
    "v_exp_f32 v251, v105\n"
    "v_mfma_f32_16x16x32_bf16 v[108:111], a[232:235], v[120:123], v[108:111]\n"
    "v_exp_f32 v252, v106\n"
    "v_mfma_f32_16x16x32_bf16 v[108:111], a[228:231], v[84:87], v[108:111]\n"
    "v_exp_f32 v253, v107\n"
    "v_mfma_f32_16x16x32_bf16 v[108:111], a[224:227], v[72:75], v[108:111]\n"
    "s_waitcnt lgkmcnt(6)\n"
    "v_add_f32 v32, v32, v244\n"
    "v_cvt_pk_bf16_f32 v49, v244, v245\n"
    "v_mfma_f32_16x16x32_bf16 v[136:139], a[192:195], v[64:67], v[232:235]\n"
    "v_add_f32 v32, v32, v245\n"
    "v_add_f32 v30, v30, v246\n"
    "v_mfma_f32_16x16x32_bf16 v[136:139], a[196:199], v[76:79], v[136:139]\n"
    "s_waitcnt lgkmcnt(4)\n"
    "v_cvt_pk_bf16_f32 v42, v246, v247\n"
    "v_add_f32 v30, v30, v247\n"
    "v_mfma_f32_16x16x32_bf16 v[136:139], a[200:203], v[112:115], v[136:139]\n"
    "ds_read_b128 a[252:255], v11 offset:57600\n"
    "v_add_f32 v30, v30, v248\n"
    "v_mfma_f32_16x16x32_bf16 v[136:139], a[204:207], v[124:127], v[136:139]\n"
    "s_waitcnt lgkmcnt(3)\n"
    "ds_read_b128 a[248:251], v10 offset:57600\n"
    "v_cvt_pk_bf16_f32 v43, v248, v249\n"
    "v_mfma_f32_16x16x32_bf16 v[136:139], a[208:211], v[160:163], v[136:139]\n"
    "ds_read_b128 a[244:247], v9 offset:57600\n"
    "v_add_f32 v30, v30, v249\n"
    "v_mfma_f32_16x16x32_bf16 v[136:139], a[212:215], v[172:175], v[136:139]\n"
    "s_waitcnt lgkmcnt(3)\n"
    "ds_read_b128 a[240:243], v8 offset:57600\n"
    "v_add_f32 v31, v31, v250\n"
    "v_mfma_f32_16x16x32_bf16 v[136:139], a[216:219], v[208:211], v[136:139]\n"
    "ds_read_b128 a[236:239], v11 offset:57344\n"
    "v_cvt_pk_bf16_f32 v46, v250, v251\n"
    "v_mfma_f32_16x16x32_bf16 v[136:139], a[220:223], v[220:223], v[136:139]\n"
    "ds_read_b128 a[232:235], v10 offset:57344\n"
    "v_add_f32 v31, v31, v251\n"
    "v_mfma_f32_16x16x32_bf16 v[140:143], a[220:223], v[224:227], v[236:239]\n"
    "ds_read_b128 a[228:231], v9 offset:57344\n"
    "v_add_f32 v31, v31, v252\n"
    "v_mfma_f32_16x16x32_bf16 v[140:143], a[216:219], v[212:215], v[140:143]\n"
    "ds_read_b128 a[224:227], v8 offset:57344\n"
    "v_cvt_pk_bf16_f32 v47, v252, v253\n"
    "v_mfma_f32_16x16x32_bf16 v[140:143], a[212:215], v[176:179], v[140:143]\n"
    "v_exp_f32 v244, v108\n"
    "v_mfma_f32_16x16x32_bf16 v[140:143], a[208:211], v[164:167], v[140:143]\n"
    "v_exp_f32 v245, v109\n"
    "v_mfma_f32_16x16x32_bf16 v[140:143], a[204:207], v[128:131], v[140:143]\n"
    "v_exp_f32 v246, v110\n"
    "v_mfma_f32_16x16x32_bf16 v[140:143], a[200:203], v[116:119], v[140:143]\n"
    "v_exp_f32 v247, v111\n"
    "v_mfma_f32_16x16x32_bf16 v[140:143], a[196:199], v[80:83], v[140:143]\n"
    "v_exp_f32 v248, v136\n"
    "v_mfma_f32_16x16x32_bf16 v[140:143], a[192:195], v[68:71], v[140:143]\n"
    "v_exp_f32 v249, v137\n"
    "v_mfma_f32_16x16x32_bf16 v[144:147], a[192:195], v[72:75], v[240:243]\n"
    "v_exp_f32 v250, v138\n"
    "v_mfma_f32_16x16x32_bf16 v[144:147], a[196:199], v[84:87], v[144:147]\n"
    "v_exp_f32 v251, v139\n"
    "v_mfma_f32_16x16x32_bf16 v[144:147], a[200:203], v[120:123], v[144:147]\n"
    "v_add_f32 v31, v31, v253\n"
    "v_add_f32 v32, v32, v244\n"
    "v_mfma_f32_16x16x32_bf16 v[144:147], a[204:207], v[132:135], v[144:147]\n"
    "v_exp_f32 v252, v140\n"
    "v_mfma_f32_16x16x32_bf16 v[144:147], a[208:211], v[168:171], v[144:147]\n"
    "v_exp_f32 v253, v141\n"
    "v_mfma_f32_16x16x32_bf16 v[144:147], a[212:215], v[180:183], v[144:147]\n"
    "v_cvt_pk_bf16_f32 v50, v244, v245\n"
    "v_add_f32 v32, v32, v245\n"
    "v_mfma_f32_16x16x32_bf16 v[144:147], a[216:219], v[216:219], v[144:147]\n"
    "v_exp_f32 v244, v142\n"
    "v_mfma_f32_16x16x32_bf16 v[144:147], a[220:223], v[228:231], v[144:147]\n"
    "s_waitcnt lgkmcnt(6)\n"
    "v_exp_f32 v245, v143\n"
    "v_mfma_f32_16x16x32_bf16 v[148:151], a[252:255], v[220:223], v[232:235]\n"
    "v_add_f32 v32, v32, v246\n"
    "v_cvt_pk_bf16_f32 v51, v246, v247\n"
    "v_mfma_f32_16x16x32_bf16 v[148:151], a[248:251], v[208:211], v[148:151]\n"
    "s_waitcnt lgkmcnt(4)\n"
    "v_add_f32 v32, v32, v247\n"
    "v_add_f32 v30, v30, v248\n"
    "v_mfma_f32_16x16x32_bf16 v[148:151], a[244:247], v[172:175], v[148:151]\n"
    "v_exp_f32 v246, v144\n"
    "v_mfma_f32_16x16x32_bf16 v[148:151], a[240:243], v[160:163], v[148:151]\n"
    "s_waitcnt lgkmcnt(2)\n"
    "v_exp_f32 v247, v145\n"
    "v_mfma_f32_16x16x32_bf16 v[148:151], a[236:239], v[124:127], v[148:151]\n"
    "v_cvt_pk_bf16_f32 v52, v248, v249\n"
    "v_add_f32 v30, v30, v249\n"
    "v_mfma_f32_16x16x32_bf16 v[148:151], a[232:235], v[112:115], v[148:151]\n"
    "s_waitcnt lgkmcnt(0)\n"
    "v_exp_f32 v248, v146\n"
    "v_mfma_f32_16x16x32_bf16 v[148:151], a[228:231], v[76:79], v[148:151]\n"
    "v_exp_f32 v249, v147\n"
    "v_mfma_f32_16x16x32_bf16 v[148:151], a[224:227], v[64:67], v[148:151]\n"
    "s_waitcnt lgkmcnt(0)\n"
    "s_barrier\n"
    "v_add_f32 v30, v30, v250\n"
    "v_mfma_f32_16x16x32_bf16 v[152:155], a[224:227], v[68:71], v[236:239]\n"
    "ds_read_b64_tr_b16 a[192:193], v12 offset:0\n"
    "v_cvt_pk_bf16_f32 v53, v250, v251\n"
    "v_mfma_f32_16x16x32_bf16 v[152:155], a[228:231], v[80:83], v[152:155]\n"
    "ds_read_b64_tr_b16 a[194:195], v12 offset:8192\n"
    "v_add_f32 v30, v30, v251\n"
    "v_mfma_f32_16x16x32_bf16 v[152:155], a[232:235], v[116:119], v[152:155]\n"
    "ds_read_b64_tr_b16 a[196:197], v12 offset:16384\n"
    "v_add_f32 v31, v31, v252\n"
    "v_mfma_f32_16x16x32_bf16 v[152:155], a[236:239], v[128:131], v[152:155]\n"
    "ds_read_b64_tr_b16 a[198:199], v12 offset:24576\n"
    "v_cvt_pk_bf16_f32 v56, v252, v253\n"
    "v_mfma_f32_16x16x32_bf16 v[152:155], a[240:243], v[164:167], v[152:155]\n"
    "ds_read_b64_tr_b16 a[200:201], v13 offset:0\n"
    "v_add_f32 v31, v31, v253\n"
    "v_mfma_f32_16x16x32_bf16 v[152:155], a[244:247], v[176:179], v[152:155]\n"
    "ds_read_b64_tr_b16 a[202:203], v13 offset:8192\n"
    "v_add_f32 v31, v31, v244\n"
    "v_mfma_f32_16x16x32_bf16 v[152:155], a[248:251], v[212:215], v[152:155]\n"
    "ds_read_b64_tr_b16 a[204:205], v13 offset:16384\n"
    "v_cvt_pk_bf16_f32 v57, v244, v245\n"
    "v_mfma_f32_16x16x32_bf16 v[152:155], a[252:255], v[224:227], v[152:155]\n"
    "ds_read_b64_tr_b16 a[206:207], v13 offset:24576\n"
    "v_add_f32 v31, v31, v245\n"
    "v_mfma_f32_16x16x32_bf16 v[156:159], a[252:255], v[228:231], v[240:243]\n"
    "ds_read_b64_tr_b16 a[208:209], v14 offset:0\n"
    "v_add_f32 v32, v32, v246\n"
    "v_mfma_f32_16x16x32_bf16 v[156:159], a[248:251], v[216:219], v[156:159]\n"
    "ds_read_b64_tr_b16 a[210:211], v14 offset:8192\n"
    "v_cvt_pk_bf16_f32 v60, v246, v247\n"
    "v_mfma_f32_16x16x32_bf16 v[156:159], a[244:247], v[180:183], v[156:159]\n"
    "ds_read_b64_tr_b16 a[212:213], v14 offset:16384\n"
    "v_add_f32 v32, v32, v247\n"
    "v_mfma_f32_16x16x32_bf16 v[156:159], a[240:243], v[168:171], v[156:159]\n"
    "ds_read_b64_tr_b16 a[214:215], v14 offset:24576\n"
    "v_add_f32 v32, v32, v248\n"
    "v_mfma_f32_16x16x32_bf16 v[156:159], a[236:239], v[132:135], v[156:159]\n"
    "ds_read_b64_tr_b16 a[216:217], v15 offset:0\n"
    "v_cvt_pk_bf16_f32 v61, v248, v249\n"
    "v_mfma_f32_16x16x32_bf16 v[156:159], a[232:235], v[120:123], v[156:159]\n"
    "ds_read_b64_tr_b16 a[218:219], v15 offset:8192\n"
    "v_add_f32 v32, v32, v249\n"
    "v_mfma_f32_16x16x32_bf16 v[156:159], a[228:231], v[84:87], v[156:159]\n"
    "ds_read_b64_tr_b16 a[220:221], v15 offset:16384\n"
    "v_mfma_f32_16x16x32_bf16 v[156:159], a[224:227], v[72:75], v[156:159]\n"
    "s_waitcnt lgkmcnt(11)\n"
    "ds_read_b64_tr_b16 a[222:223], v15 offset:24576\n"
    "v_mfma_f32_16x16x32_bf16 a[0:3], a[192:195], v[184:187], 0\n"
    "v_exp_f32 v250, v148\n"
    "v_mfma_f32_16x16x32_bf16 a[0:3], a[196:199], v[196:199], a[0:3]\n"
    "v_exp_f32 v251, v149\n"
    "v_mfma_f32_16x16x32_bf16 a[4:7], a[196:199], v[200:203], 0\n"
    "ds_read_b64_tr_b16 a[224:225], v16 offset:0\n"
    "v_add_f32 v30, v30, v250\n"
    "v_mfma_f32_16x16x32_bf16 a[4:7], a[192:195], v[188:191], a[4:7]\n"
    "ds_read_b64_tr_b16 a[226:227], v16 offset:8192\n"
    "v_cvt_pk_bf16_f32 v54, v250, v251\n"
    "v_mfma_f32_16x16x32_bf16 a[8:11], a[192:195], v[192:195], 0\n"
    "ds_read_b64_tr_b16 a[228:229], v16 offset:16384\n"
    "v_add_f32 v30, v30, v251\n"
    "v_mfma_f32_16x16x32_bf16 a[8:11], a[196:199], v[204:207], a[8:11]\n"
    "s_waitcnt lgkmcnt(11)\n"
    "ds_read_b64_tr_b16 a[230:231], v16 offset:24576\n"
    "v_mfma_f32_16x16x32_bf16 a[20:23], a[200:203], v[192:195], 0\n"
    "v_exp_f32 v252, v150\n"
    "v_mfma_f32_16x16x32_bf16 a[20:23], a[204:207], v[204:207], a[20:23]\n"
    "v_exp_f32 v253, v151\n"
    "v_mfma_f32_16x16x32_bf16 a[16:19], a[204:207], v[200:203], 0\n"
    "ds_read_b64_tr_b16 a[232:233], v17 offset:0\n"
    "v_add_f32 v30, v30, v252\n"
    "v_mfma_f32_16x16x32_bf16 a[16:19], a[200:203], v[188:191], a[16:19]\n"
    "ds_read_b64_tr_b16 a[234:235], v17 offset:8192\n"
    "v_cvt_pk_bf16_f32 v55, v252, v253\n"
    "v_mfma_f32_16x16x32_bf16 a[12:15], a[200:203], v[184:187], 0\n"
    "ds_read_b64_tr_b16 a[236:237], v17 offset:16384\n"
    "v_add_f32 v30, v30, v253\n"
    "v_mfma_f32_16x16x32_bf16 a[12:15], a[204:207], v[196:199], a[12:15]\n"
    "s_waitcnt lgkmcnt(11)\n"
    "ds_read_b64_tr_b16 a[238:239], v17 offset:24576\n"
    "v_mfma_f32_16x16x32_bf16 a[24:27], a[208:211], v[184:187], 0\n"
    "v_exp_f32 v244, v152\n"
    "v_mfma_f32_16x16x32_bf16 a[24:27], a[212:215], v[196:199], a[24:27]\n"
    "v_exp_f32 v245, v153\n"
    "v_mfma_f32_16x16x32_bf16 a[28:31], a[212:215], v[200:203], 0\n"
    "ds_read_b64_tr_b16 a[240:241], v18 offset:0\n"
    "v_add_f32 v31, v31, v244\n"
    "v_mfma_f32_16x16x32_bf16 a[28:31], a[208:211], v[188:191], a[28:31]\n"
    "ds_read_b64_tr_b16 a[242:243], v18 offset:8192\n"
    "v_cvt_pk_bf16_f32 v58, v244, v245\n"
    "v_mfma_f32_16x16x32_bf16 a[32:35], a[208:211], v[192:195], 0\n"
    "ds_read_b64_tr_b16 a[244:245], v18 offset:16384\n"
    "v_add_f32 v31, v31, v245\n"
    "v_mfma_f32_16x16x32_bf16 a[32:35], a[212:215], v[204:207], a[32:35]\n"
    "s_waitcnt lgkmcnt(11)\n"
    "ds_read_b64_tr_b16 a[246:247], v18 offset:24576\n"
    "v_mfma_f32_16x16x32_bf16 a[44:47], a[216:219], v[192:195], 0\n"
    "v_exp_f32 v246, v154\n"
    "v_mfma_f32_16x16x32_bf16 a[44:47], a[220:223], v[204:207], a[44:47]\n"
    "v_exp_f32 v247, v155\n"
    "v_mfma_f32_16x16x32_bf16 a[40:43], a[220:223], v[200:203], 0\n"
    "ds_read_b64_tr_b16 a[248:249], v19 offset:0\n"
    "v_add_f32 v31, v31, v246\n"
    "v_mfma_f32_16x16x32_bf16 a[40:43], a[216:219], v[188:191], a[40:43]\n"
    "ds_read_b64_tr_b16 a[250:251], v19 offset:8192\n"
    "v_cvt_pk_bf16_f32 v59, v246, v247\n"
    "v_mfma_f32_16x16x32_bf16 a[36:39], a[216:219], v[184:187], 0\n"
    "ds_read_b64_tr_b16 a[252:253], v19 offset:16384\n"
    "v_add_f32 v31, v31, v247\n"
    "v_mfma_f32_16x16x32_bf16 a[36:39], a[220:223], v[196:199], a[36:39]\n"
    "s_waitcnt lgkmcnt(11)\n"
    "ds_read_b64_tr_b16 a[254:255], v19 offset:24576\n"
    "v_mfma_f32_16x16x32_bf16 a[48:51], a[224:227], v[184:187], 0\n"
    "v_exp_f32 v248, v156\n"
    "v_mfma_f32_16x16x32_bf16 a[48:51], a[228:231], v[196:199], a[48:51]\n"
    "v_exp_f32 v249, v157\n"
    "v_mfma_f32_16x16x32_bf16 a[52:55], a[228:231], v[200:203], 0\n"
    "ds_read_b64_tr_b16 a[192:193], v12 offset:256\n"
    "v_add_f32 v32, v32, v248\n"
    "v_mfma_f32_16x16x32_bf16 a[52:55], a[224:227], v[188:191], a[52:55]\n"
    "ds_read_b64_tr_b16 a[194:195], v12 offset:8448\n"
    "v_cvt_pk_bf16_f32 v62, v248, v249\n"
    "v_mfma_f32_16x16x32_bf16 a[56:59], a[224:227], v[192:195], 0\n"
    "ds_read_b64_tr_b16 a[196:197], v12 offset:16640\n"
    "v_add_f32 v32, v32, v249\n"
    "v_mfma_f32_16x16x32_bf16 a[56:59], a[228:231], v[204:207], a[56:59]\n"
    "s_waitcnt lgkmcnt(11)\n"
    "ds_read_b64_tr_b16 a[198:199], v12 offset:24832\n"
    "v_mfma_f32_16x16x32_bf16 a[68:71], a[232:235], v[192:195], 0\n"
    "v_exp_f32 v250, v158\n"
    "v_mfma_f32_16x16x32_bf16 a[68:71], a[236:239], v[204:207], a[68:71]\n"
    "v_exp_f32 v251, v159\n"
    "v_mfma_f32_16x16x32_bf16 a[64:67], a[236:239], v[200:203], 0\n"
    "ds_read_b64_tr_b16 a[200:201], v13 offset:256\n"
    "v_add_f32 v32, v32, v250\n"
    "v_mfma_f32_16x16x32_bf16 a[64:67], a[232:235], v[188:191], a[64:67]\n"
    "ds_read_b64_tr_b16 a[202:203], v13 offset:8448\n"
    "v_cvt_pk_bf16_f32 v63, v250, v251\n"
    "v_mfma_f32_16x16x32_bf16 a[60:63], a[232:235], v[184:187], 0\n"
    "s_lshl_b32 s49, s60, 3\n"
    "ds_read_b64_tr_b16 a[204:205], v13 offset:16640\n"
    "v_add_f32 v32, v32, v251\n"
    "v_mfma_f32_16x16x32_bf16 a[60:63], a[236:239], v[196:199], a[60:63]\n"
    "s_waitcnt lgkmcnt(11)\n"
    "ds_read_b64_tr_b16 a[206:207], v13 offset:24832\n"
    "v_max3_f32 v254, v30, v31, v32\n"
    "v_mfma_f32_16x16x32_bf16 a[72:75], a[240:243], v[184:187], 0\n"
    "s_add_u32 s49, s49, s61\n"
    "v_cmp_lt_f32 vcc, s58, v254\n"
    "s_mul_i32 s48, s49, 5504\n"
    "v_mfma_f32_16x16x32_bf16 a[72:75], a[244:247], v[196:199], a[72:75]\n"
    "s_add_u32 s48, s48, 33554432\n"
    "s_add_u32 s36, s78, s48\n"
    "s_addc_u32 s37, s79, 0\n"
    "v_mfma_f32_16x16x32_bf16 a[76:79], a[244:247], v[200:203], 0\n"
    "s_and_b32 s37, s37, 0xffff\n"
    "ds_read_b64_tr_b16 a[208:209], v14 offset:256\n"
    "s_mov_b32 s38, 5504\n"
    "v_mfma_f32_16x16x32_bf16 a[76:79], a[240:243], v[188:191], a[76:79]\n"
    "s_mov_b32 s39, 0x20000\n"
    "ds_read_b64_tr_b16 a[210:211], v14 offset:8448\n"
    "s_mul_i32 s48, s49, 2064384\n"
    "v_mfma_f32_16x16x32_bf16 a[80:83], a[240:243], v[192:195], 0\n"
    "s_add_u32 s40, s78, s48\n"
    "ds_read_b64_tr_b16 a[212:213], v14 offset:16640\n"
    "s_addc_u32 s41, s79, 0\n"
    "v_mfma_f32_16x16x32_bf16 a[80:83], a[244:247], v[204:207], a[80:83]\n"
    "s_waitcnt lgkmcnt(11)\n"
    "ds_read_b64_tr_b16 a[214:215], v14 offset:24832\n"
    "s_and_b32 s41, s41, 0xffff\n"
    "v_mfma_f32_16x16x32_bf16 a[92:95], a[248:251], v[192:195], 0\n"
    "s_mov_b32 s42, 2064384\n"
    "s_mov_b32 s43, 0x20000\n"
    "s_mul_i32 s50, s62, 129024\n"
    "v_mfma_f32_16x16x32_bf16 a[92:95], a[252:255], v[204:207], a[92:95]\n"
    "s_add_u32 s48, s48, s50\n"
    "s_add_u32 s28, s78, s48\n"
    "s_addc_u32 s29, s79, 0\n"
    "v_mfma_f32_16x16x32_bf16 a[88:91], a[252:255], v[200:203], 0\n"
    "s_and_b32 s29, s29, 0xffff\n"
    "ds_read_b64_tr_b16 a[216:217], v15 offset:256\n"
    "s_mov_b32 s30, 129024\n"
    "v_mfma_f32_16x16x32_bf16 a[88:91], a[248:251], v[188:191], a[88:91]\n"
    "s_mov_b32 s31, 0x20000\n"
    "ds_read_b64_tr_b16 a[218:219], v15 offset:8448\n"
    "s_mul_i32 s48, s61, 258048\n"
    "v_mfma_f32_16x16x32_bf16 a[84:87], a[248:251], v[184:187], 0\n"
    "s_mul_i32 s49, s60, 896\n"
    "ds_read_b64_tr_b16 a[220:221], v15 offset:16640\n"
    "s_add_u32 s48, s48, s49\n"
    "v_mfma_f32_16x16x32_bf16 a[84:87], a[252:255], v[196:199], a[84:87]\n"
    "s_waitcnt lgkmcnt(11)\n"
    "ds_read_b64_tr_b16 a[222:223], v15 offset:24832\n"
    "s_add_u32 s44, s76, s48\n"
    "v_mfma_f32_16x16x32_bf16 a[96:99], a[192:195], v[184:187], 0\n"
    "s_addc_u32 s45, s77, 0\n"
    "s_and_b32 s45, s45, 0xffff\n"
    "s_mov_b32 s46, 258048\n"
    "v_mfma_f32_16x16x32_bf16 a[96:99], a[196:199], v[196:199], a[96:99]\n"
    "s_mov_b32 s47, 0x20000\n"
    "s_sub_u32 s46, s46, s49\n"
    "v_lshlrev_b32 v36, 4, v1\n"
    "v_mfma_f32_16x16x32_bf16 a[100:103], a[196:199], v[200:203], 0\n"
    "s_lshl_b32 s48, s59, 7\n"
    "ds_read_b64_tr_b16 a[224:225], v16 offset:256\n"
    "v_lshlrev_b32 v6, 3, v2\n"
    "v_mfma_f32_16x16x32_bf16 a[100:103], a[192:195], v[188:191], a[100:103]\n"
    "s_lshl_b32 s49, s62, 3\n"
    "ds_read_b64_tr_b16 a[226:227], v16 offset:8448\n"
    "v_mul_u32_u24 v7, 1792, v2\n"
    "v_mfma_f32_16x16x32_bf16 a[104:107], a[192:195], v[192:195], 0\n"
    "s_add_u32 s48, s48, s49\n"
    "ds_read_b64_tr_b16 a[228:229], v16 offset:16640\n"
    "v_lshl_add_u32 v7, v3, 4, v7\n"
    "v_mfma_f32_16x16x32_bf16 a[104:107], a[196:199], v[204:207], a[104:107]\n"
    "s_waitcnt lgkmcnt(11)\n"
    "ds_read_b64_tr_b16 a[230:231], v16 offset:24832\n"
    "v_min_u32 v26, 1, v1\n"
    "v_mfma_f32_16x16x32_bf16 a[116:119], a[200:203], v[192:195], 0\n"
    "v_lshlrev_b32 v26, 30, v26\n"
    "v_mul_u32_u24 v33, 384, v1\n"
    "v_mfma_f32_16x16x32_bf16 a[116:119], a[204:207], v[204:207], a[116:119]\n"
    "v_add_u32 v33, s48, v33\n"
    "v_sub_u32 v34, 13, v1\n"
    "v_mfma_f32_16x16x32_bf16 a[112:115], a[204:207], v[200:203], 0\n"
    "ds_read_b64_tr_b16 a[232:233], v17 offset:256\n"
    "v_ashrrev_i32 v34, 31, v34\n"
    "v_mfma_f32_16x16x32_bf16 a[112:115], a[200:203], v[188:191], a[112:115]\n"
    "ds_read_b64_tr_b16 a[234:235], v17 offset:8448\n"
    "v_or_b32 v33, v33, v34\n"
    "v_mfma_f32_16x16x32_bf16 a[108:111], a[200:203], v[184:187], 0\n"
    "ds_read_b64_tr_b16 a[236:237], v17 offset:16640\n"
    "v_mov_b32 v38, 0\n"
    "v_mfma_f32_16x16x32_bf16 a[108:111], a[204:207], v[196:199], a[108:111]\n"
    "s_waitcnt lgkmcnt(11)\n"
    "ds_read_b64_tr_b16 a[238:239], v17 offset:24832\n"
    "v_mov_b32 v39, 0\n"
    "v_mfma_f32_16x16x32_bf16 a[120:123], a[208:211], v[184:187], 0\n"
    "v_mfma_f32_16x16x32_bf16 a[120:123], a[212:215], v[196:199], a[120:123]\n"
    "buffer_store_dwordx2 v[38:39], v33, s[36:39], 0 offen sc1\n"
    "v_mfma_f32_16x16x32_bf16 a[124:127], a[212:215], v[200:203], 0\n"
    "ds_read_b64_tr_b16 a[240:241], v18 offset:256\n"
    "v_mfma_f32_16x16x32_bf16 a[124:127], a[208:211], v[188:191], a[124:127]\n"
    "ds_read_b64_tr_b16 a[242:243], v18 offset:8448\n"
    "v_mfma_f32_16x16x32_bf16 a[128:131], a[208:211], v[192:195], 0\n"
    "ds_read_b64_tr_b16 a[244:245], v18 offset:16640\n"
    "v_mfma_f32_16x16x32_bf16 a[128:131], a[212:215], v[204:207], a[128:131]\n"
    "s_waitcnt lgkmcnt(11)\n"
    "ds_read_b64_tr_b16 a[246:247], v18 offset:24832\n"
    "v_mfma_f32_16x16x32_bf16 a[140:143], a[216:219], v[192:195], 0\n"
    "v_mfma_f32_16x16x32_bf16 a[140:143], a[220:223], v[204:207], a[140:143]\n"
    "v_mfma_f32_16x16x32_bf16 a[136:139], a[220:223], v[200:203], 0\n"
    "ds_read_b64_tr_b16 a[248:249], v19 offset:256\n"
    "v_mfma_f32_16x16x32_bf16 a[136:139], a[216:219], v[188:191], a[136:139]\n"
    "ds_read_b64_tr_b16 a[250:251], v19 offset:8448\n"
    "v_mfma_f32_16x16x32_bf16 a[132:135], a[216:219], v[184:187], 0\n"
    "ds_read_b64_tr_b16 a[252:253], v19 offset:16640\n"
    "v_mfma_f32_16x16x32_bf16 a[132:135], a[220:223], v[196:199], a[132:135]\n"
    "s_waitcnt lgkmcnt(11)\n"
    "ds_read_b64_tr_b16 a[254:255], v19 offset:24832\n"
    "v_mfma_f32_16x16x32_bf16 a[144:147], a[224:227], v[184:187], 0\n"
    "v_mfma_f32_16x16x32_bf16 a[144:147], a[228:231], v[196:199], a[144:147]\n"
    "v_mfma_f32_16x16x32_bf16 a[148:151], a[228:231], v[200:203], 0\n"
    "v_mfma_f32_16x16x32_bf16 a[148:151], a[224:227], v[188:191], a[148:151]\n"
    "v_mfma_f32_16x16x32_bf16 a[152:155], a[224:227], v[192:195], 0\n"
    "v_mfma_f32_16x16x32_bf16 a[152:155], a[228:231], v[204:207], a[152:155]\n"
    "s_waitcnt lgkmcnt(8)\n"
    "v_mfma_f32_16x16x32_bf16 a[164:167], a[232:235], v[192:195], 0\n"
    "v_mfma_f32_16x16x32_bf16 a[164:167], a[236:239], v[204:207], a[164:167]\n"
    "v_mfma_f32_16x16x32_bf16 a[160:163], a[236:239], v[200:203], 0\n"
    "v_mfma_f32_16x16x32_bf16 a[160:163], a[232:235], v[188:191], a[160:163]\n"
    "v_mfma_f32_16x16x32_bf16 a[156:159], a[232:235], v[184:187], 0\n"
    "v_mfma_f32_16x16x32_bf16 a[156:159], a[236:239], v[196:199], a[156:159]\n"
    "s_waitcnt lgkmcnt(0)\n"
    "s_barrier\n"
    "v_mfma_f32_16x16x32_bf16 a[168:171], a[240:243], v[184:187], 0\n"
    "ds_read_b128 a[192:195], v8 offset:0\n"
    "v_mfma_f32_16x16x32_bf16 a[168:171], a[244:247], v[196:199], a[168:171]\n"
    "ds_read_b128 a[196:199], v9 offset:0\n"
    "v_mfma_f32_16x16x32_bf16 a[172:175], a[244:247], v[200:203], 0\n"
    "ds_read_b128 a[200:203], v10 offset:0\n"
    "v_mfma_f32_16x16x32_bf16 a[172:175], a[240:243], v[188:191], a[172:175]\n"
    "ds_read_b128 a[204:207], v11 offset:0\n"
    "v_mfma_f32_16x16x32_bf16 a[176:179], a[240:243], v[192:195], 0\n"
    "ds_read_b128 a[208:211], v8 offset:256\n"
    "v_mfma_f32_16x16x32_bf16 a[176:179], a[244:247], v[204:207], a[176:179]\n"
    "ds_read_b128 a[212:215], v9 offset:256\n"
    "v_mfma_f32_16x16x32_bf16 a[188:191], a[248:251], v[192:195], 0\n"
    "ds_read_b128 a[216:219], v10 offset:256\n"
    "v_mfma_f32_16x16x32_bf16 a[188:191], a[252:255], v[204:207], a[188:191]\n"
    "ds_read_b128 a[220:223], v11 offset:256\n"
    "v_mfma_f32_16x16x32_bf16 a[184:187], a[252:255], v[200:203], 0\n"
    "v_mfma_f32_16x16x32_bf16 a[184:187], a[248:251], v[188:191], a[184:187]\n"
    "v_mfma_f32_16x16x32_bf16 a[180:183], a[248:251], v[184:187], 0\n"
    "v_mfma_f32_16x16x32_bf16 a[180:183], a[252:255], v[196:199], a[180:183]\n"
    "s_waitcnt lgkmcnt(6)\n"
    "s_cbranch_vccnz Lstub2_%=\n"
    "Lok2_%=:\n"
    "Lattn_%=:\n"
    "v_mfma_f32_16x16x32_bf16 v[88:91], a[192:195], v[64:67], v[232:235]\n"
    "s_waitcnt lgkmcnt(5)\n"
    "v_add_f32 v27, v27, v30\n"
    "v_add_f32 v28, v28, v31\n"
    "v_mfma_f32_16x16x32_bf16 v[88:91], a[196:199], v[76:79], v[88:91]\n"
    "v_add_f32 v29, v29, v32\n"
    "v_mfma_f32_16x16x32_bf16 v[88:91], a[200:203], v[112:115], v[88:91]\n"
    "s_waitcnt lgkmcnt(3)\n"
    "ds_read_b128 a[252:255], v11 offset:8448\n"
    "v_mfma_f32_16x16x32_bf16 v[88:91], a[204:207], v[124:127], v[88:91]\n"
    "ds_read_b128 a[248:251], v10 offset:8448\n"
    "v_mfma_f32_16x16x32_bf16 v[88:91], a[208:211], v[160:163], v[88:91]\n"
    "s_waitcnt lgkmcnt(3)\n"
    "ds_read_b128 a[244:247], v9 offset:8448\n"
    "v_mfma_f32_16x16x32_bf16 v[88:91], a[212:215], v[172:175], v[88:91]\n"
    "ds_read_b128 a[240:243], v8 offset:8448\n"
    "v_mfma_f32_16x16x32_bf16 v[88:91], a[216:219], v[208:211], v[88:91]\n"
    "s_waitcnt lgkmcnt(4)\n"
    "ds_read_b128 a[236:239], v11 offset:8192\n"
    "v_mfma_f32_16x16x32_bf16 v[88:91], a[220:223], v[220:223], v[88:91]\n"
    "ds_read_b128 a[232:235], v10 offset:8192\n"
    "v_mfma_f32_16x16x32_bf16 v[92:95], a[220:223], v[224:227], v[236:239]\n"
    "ds_read_b128 a[228:231], v9 offset:8192\n"
    "v_mfma_f32_16x16x32_bf16 v[92:95], a[216:219], v[212:215], v[92:95]\n"
    "ds_read_b128 a[224:227], v8 offset:8192\n"
    "v_mfma_f32_16x16x32_bf16 v[92:95], a[212:215], v[176:179], v[92:95]\n"
    "v_mfma_f32_16x16x32_bf16 v[92:95], a[208:211], v[164:167], v[92:95]\n"
    "v_mfma_f32_16x16x32_bf16 v[92:95], a[204:207], v[128:131], v[92:95]\n"
    "v_exp_f32 v244, v88\n"
    "v_mfma_f32_16x16x32_bf16 v[92:95], a[200:203], v[116:119], v[92:95]\n"
    "v_exp_f32 v245, v89\n"
    "v_mfma_f32_16x16x32_bf16 v[92:95], a[196:199], v[80:83], v[92:95]\n"
    "v_exp_f32 v246, v90\n"
    "v_mfma_f32_16x16x32_bf16 v[92:95], a[192:195], v[68:71], v[92:95]\n"
    "v_exp_f32 v247, v91\n"
    "v_mfma_f32_16x16x32_bf16 v[96:99], a[192:195], v[72:75], v[240:243]\n"
    "v_add_f32 v30, v244, v245\n"
    "v_cvt_pk_bf16_f32 v184, v244, v245\n"
    "v_mfma_f32_16x16x32_bf16 v[96:99], a[196:199], v[84:87], v[96:99]\n"
    "v_add_f32 v30, v30, v246\n"
    "v_cvt_pk_bf16_f32 v185, v246, v247\n"
    "v_mfma_f32_16x16x32_bf16 v[96:99], a[200:203], v[120:123], v[96:99]\n"
    "v_exp_f32 v248, v92\n"
    "v_mfma_f32_16x16x32_bf16 v[96:99], a[204:207], v[132:135], v[96:99]\n"
    "v_exp_f32 v249, v93\n"
    "v_mfma_f32_16x16x32_bf16 v[96:99], a[208:211], v[168:171], v[96:99]\n"
    "v_exp_f32 v250, v94\n"
    "v_mfma_f32_16x16x32_bf16 v[96:99], a[212:215], v[180:183], v[96:99]\n"
    "v_exp_f32 v251, v95\n"
    "v_mfma_f32_16x16x32_bf16 v[96:99], a[216:219], v[216:219], v[96:99]\n"
    "v_add_f32 v30, v30, v247\n"
    "v_add_f32 v31, v248, v249\n"
    "v_mfma_f32_16x16x32_bf16 v[96:99], a[220:223], v[228:231], v[96:99]\n"
    "s_waitcnt lgkmcnt(6)\n"
    "v_cvt_pk_bf16_f32 v188, v248, v249\n"
    "v_add_f32 v31, v31, v250\n"
    "v_mfma_f32_16x16x32_bf16 v[100:103], a[252:255], v[220:223], v[232:235]\n"
    "v_cvt_pk_bf16_f32 v189, v250, v251\n"
    "v_add_f32 v31, v31, v251\n"
    "v_mfma_f32_16x16x32_bf16 v[100:103], a[248:251], v[208:211], v[100:103]\n"
    "s_waitcnt lgkmcnt(4)\n"
    "v_mfma_f32_16x16x32_bf16 v[100:103], a[244:247], v[172:175], v[100:103]\n"
    "ds_read_b128 a[192:195], v8 offset:16384\n"
    "v_mfma_f32_16x16x32_bf16 v[100:103], a[240:243], v[160:163], v[100:103]\n"
    "s_waitcnt lgkmcnt(3)\n"
    "ds_read_b128 a[196:199], v9 offset:16384\n"
    "v_mfma_f32_16x16x32_bf16 v[100:103], a[236:239], v[124:127], v[100:103]\n"
    "ds_read_b128 a[200:203], v10 offset:16384\n"
    "v_mfma_f32_16x16x32_bf16 v[100:103], a[232:235], v[112:115], v[100:103]\n"
    "s_waitcnt lgkmcnt(3)\n"
    "ds_read_b128 a[204:207], v11 offset:16384\n"
    "v_mfma_f32_16x16x32_bf16 v[100:103], a[228:231], v[76:79], v[100:103]\n"
    "ds_read_b128 a[208:211], v8 offset:16640\n"
    "v_mfma_f32_16x16x32_bf16 v[100:103], a[224:227], v[64:67], v[100:103]\n"
    "ds_read_b128 a[212:215], v9 offset:16640\n"
    "v_mfma_f32_16x16x32_bf16 v[104:107], a[224:227], v[68:71], v[236:239]\n"
    "ds_read_b128 a[216:219], v10 offset:16640\n"
    "v_mfma_f32_16x16x32_bf16 v[104:107], a[228:231], v[80:83], v[104:107]\n"
    "ds_read_b128 a[220:223], v11 offset:16640\n"
    "v_mfma_f32_16x16x32_bf16 v[104:107], a[232:235], v[116:119], v[104:107]\n"
    "v_exp_f32 v252, v96\n"
    "v_mfma_f32_16x16x32_bf16 v[104:107], a[236:239], v[128:131], v[104:107]\n"
    "v_exp_f32 v253, v97\n"
    "v_mfma_f32_16x16x32_bf16 v[104:107], a[240:243], v[164:167], v[104:107]\n"
    "v_exp_f32 v244, v98\n"
    "v_mfma_f32_16x16x32_bf16 v[104:107], a[244:247], v[176:179], v[104:107]\n"
    "v_exp_f32 v245, v99\n"
    "v_mfma_f32_16x16x32_bf16 v[104:107], a[248:251], v[212:215], v[104:107]\n"
    "v_exp_f32 v246, v100\n"
    "v_mfma_f32_16x16x32_bf16 v[104:107], a[252:255], v[224:227], v[104:107]\n"
    "v_exp_f32 v247, v101\n"
    "v_mfma_f32_16x16x32_bf16 v[108:111], a[252:255], v[228:231], v[240:243]\n"
    "v_exp_f32 v248, v102\n"
    "v_mfma_f32_16x16x32_bf16 v[108:111], a[248:251], v[216:219], v[108:111]\n"
    "v_exp_f32 v249, v103\n"
    "v_mfma_f32_16x16x32_bf16 v[108:111], a[244:247], v[180:183], v[108:111]\n"
    "v_add_f32 v32, v252, v253\n"
    "v_cvt_pk_bf16_f32 v192, v252, v253\n"
    "v_mfma_f32_16x16x32_bf16 v[108:111], a[240:243], v[168:171], v[108:111]\n"
    "v_exp_f32 v250, v104\n"
    "v_mfma_f32_16x16x32_bf16 v[108:111], a[236:239], v[132:135], v[108:111]\n"
    "v_exp_f32 v251, v105\n"
    "v_mfma_f32_16x16x32_bf16 v[108:111], a[232:235], v[120:123], v[108:111]\n"
    "v_exp_f32 v252, v106\n"
    "v_mfma_f32_16x16x32_bf16 v[108:111], a[228:231], v[84:87], v[108:111]\n"
    "v_exp_f32 v253, v107\n"
    "v_mfma_f32_16x16x32_bf16 v[108:111], a[224:227], v[72:75], v[108:111]\n"
    "s_waitcnt lgkmcnt(6)\n"
    "v_add_f32 v32, v32, v244\n"
    "v_cvt_pk_bf16_f32 v193, v244, v245\n"
    "v_mfma_f32_16x16x32_bf16 v[136:139], a[192:195], v[64:67], v[232:235]\n"
    "v_add_f32 v32, v32, v245\n"
    "v_add_f32 v30, v30, v246\n"
    "v_mfma_f32_16x16x32_bf16 v[136:139], a[196:199], v[76:79], v[136:139]\n"
    "s_waitcnt lgkmcnt(4)\n"
    "v_cvt_pk_bf16_f32 v186, v246, v247\n"
    "v_add_f32 v30, v30, v247\n"
    "v_mfma_f32_16x16x32_bf16 v[136:139], a[200:203], v[112:115], v[136:139]\n"
    "ds_read_b128 a[252:255], v11 offset:24832\n"
    "v_add_f32 v30, v30, v248\n"
    "v_mfma_f32_16x16x32_bf16 v[136:139], a[204:207], v[124:127], v[136:139]\n"
    "s_waitcnt lgkmcnt(3)\n"
    "ds_read_b128 a[248:251], v10 offset:24832\n"
    "v_cvt_pk_bf16_f32 v187, v248, v249\n"
    "v_mfma_f32_16x16x32_bf16 v[136:139], a[208:211], v[160:163], v[136:139]\n"
    "ds_read_b128 a[244:247], v9 offset:24832\n"
    "v_add_f32 v30, v30, v249\n"
    "v_mfma_f32_16x16x32_bf16 v[136:139], a[212:215], v[172:175], v[136:139]\n"
    "s_waitcnt lgkmcnt(3)\n"
    "ds_read_b128 a[240:243], v8 offset:24832\n"
    "v_add_f32 v31, v31, v250\n"
    "v_mfma_f32_16x16x32_bf16 v[136:139], a[216:219], v[208:211], v[136:139]\n"
    "ds_read_b128 a[236:239], v11 offset:24576\n"
    "v_cvt_pk_bf16_f32 v190, v250, v251\n"
    "v_mfma_f32_16x16x32_bf16 v[136:139], a[220:223], v[220:223], v[136:139]\n"
    "ds_read_b128 a[232:235], v10 offset:24576\n"
    "v_add_f32 v31, v31, v251\n"
    "v_mfma_f32_16x16x32_bf16 v[140:143], a[220:223], v[224:227], v[236:239]\n"
    "ds_read_b128 a[228:231], v9 offset:24576\n"
    "v_add_f32 v31, v31, v252\n"
    "v_mfma_f32_16x16x32_bf16 v[140:143], a[216:219], v[212:215], v[140:143]\n"
    "ds_read_b128 a[224:227], v8 offset:24576\n"
    "v_cvt_pk_bf16_f32 v191, v252, v253\n"
    "v_mfma_f32_16x16x32_bf16 v[140:143], a[212:215], v[176:179], v[140:143]\n"
    "v_exp_f32 v244, v108\n"
    "v_mfma_f32_16x16x32_bf16 v[140:143], a[208:211], v[164:167], v[140:143]\n"
    "v_exp_f32 v245, v109\n"
    "v_mfma_f32_16x16x32_bf16 v[140:143], a[204:207], v[128:131], v[140:143]\n"
    "v_exp_f32 v246, v110\n"
    "v_mfma_f32_16x16x32_bf16 v[140:143], a[200:203], v[116:119], v[140:143]\n"
    "v_exp_f32 v247, v111\n"
    "v_mfma_f32_16x16x32_bf16 v[140:143], a[196:199], v[80:83], v[140:143]\n"
    "v_exp_f32 v248, v136\n"
    "v_mfma_f32_16x16x32_bf16 v[140:143], a[192:195], v[68:71], v[140:143]\n"
    "v_exp_f32 v249, v137\n"
    "v_mfma_f32_16x16x32_bf16 v[144:147], a[192:195], v[72:75], v[240:243]\n"
    "v_exp_f32 v250, v138\n"
    "v_mfma_f32_16x16x32_bf16 v[144:147], a[196:199], v[84:87], v[144:147]\n"
    "v_exp_f32 v251, v139\n"
    "v_mfma_f32_16x16x32_bf16 v[144:147], a[200:203], v[120:123], v[144:147]\n"
    "v_add_f32 v31, v31, v253\n"
    "v_add_f32 v32, v32, v244\n"
    "v_mfma_f32_16x16x32_bf16 v[144:147], a[204:207], v[132:135], v[144:147]\n"
    "v_exp_f32 v252, v140\n"
    "v_mfma_f32_16x16x32_bf16 v[144:147], a[208:211], v[168:171], v[144:147]\n"
    "v_exp_f32 v253, v141\n"
    "v_mfma_f32_16x16x32_bf16 v[144:147], a[212:215], v[180:183], v[144:147]\n"
    "v_cvt_pk_bf16_f32 v194, v244, v245\n"
    "v_add_f32 v32, v32, v245\n"
    "v_mfma_f32_16x16x32_bf16 v[144:147], a[216:219], v[216:219], v[144:147]\n"
    "v_exp_f32 v244, v142\n"
    "v_mfma_f32_16x16x32_bf16 v[144:147], a[220:223], v[228:231], v[144:147]\n"
    "s_waitcnt lgkmcnt(6)\n"
    "v_exp_f32 v245, v143\n"
    "v_mfma_f32_16x16x32_bf16 v[148:151], a[252:255], v[220:223], v[232:235]\n"
    "v_add_f32 v32, v32, v246\n"
    "v_cvt_pk_bf16_f32 v195, v246, v247\n"
    "v_mfma_f32_16x16x32_bf16 v[148:151], a[248:251], v[208:211], v[148:151]\n"
    "s_waitcnt lgkmcnt(4)\n"
    "v_add_f32 v32, v32, v247\n"
    "v_add_f32 v30, v30, v248\n"
    "v_mfma_f32_16x16x32_bf16 v[148:151], a[244:247], v[172:175], v[148:151]\n"
    "v_exp_f32 v246, v144\n"
    "v_mfma_f32_16x16x32_bf16 v[148:151], a[240:243], v[160:163], v[148:151]\n"
    "s_waitcnt lgkmcnt(2)\n"
    "v_exp_f32 v247, v145\n"
    "v_mfma_f32_16x16x32_bf16 v[148:151], a[236:239], v[124:127], v[148:151]\n"
    "v_cvt_pk_bf16_f32 v196, v248, v249\n"
    "v_add_f32 v30, v30, v249\n"
    "v_mfma_f32_16x16x32_bf16 v[148:151], a[232:235], v[112:115], v[148:151]\n"
    "s_waitcnt lgkmcnt(0)\n"
    "v_exp_f32 v248, v146\n"
    "v_mfma_f32_16x16x32_bf16 v[148:151], a[228:231], v[76:79], v[148:151]\n"
    "v_exp_f32 v249, v147\n"
    "v_mfma_f32_16x16x32_bf16 v[148:151], a[224:227], v[64:67], v[148:151]\n"
    "s_waitcnt lgkmcnt(0)\n"
    "s_barrier\n"
    "v_add_f32 v30, v30, v250\n"
    "v_mfma_f32_16x16x32_bf16 v[152:155], a[224:227], v[68:71], v[236:239]\n"
    "ds_read_b64_tr_b16 a[192:193], v12 offset:32768\n"
    "v_cvt_pk_bf16_f32 v197, v250, v251\n"
    "v_mfma_f32_16x16x32_bf16 v[152:155], a[228:231], v[80:83], v[152:155]\n"
    "ds_read_b64_tr_b16 a[194:195], v12 offset:40960\n"
    "v_add_f32 v30, v30, v251\n"
    "v_mfma_f32_16x16x32_bf16 v[152:155], a[232:235], v[116:119], v[152:155]\n"
    "ds_read_b64_tr_b16 a[196:197], v12 offset:49152\n"
    "v_add_f32 v31, v31, v252\n"
    "v_mfma_f32_16x16x32_bf16 v[152:155], a[236:239], v[128:131], v[152:155]\n"
    "ds_read_b64_tr_b16 a[198:199], v12 offset:57344\n"
    "v_cvt_pk_bf16_f32 v200, v252, v253\n"
    "v_mfma_f32_16x16x32_bf16 v[152:155], a[240:243], v[164:167], v[152:155]\n"
    "ds_read_b64_tr_b16 a[200:201], v13 offset:32768\n"
    "v_add_f32 v31, v31, v253\n"
    "v_mfma_f32_16x16x32_bf16 v[152:155], a[244:247], v[176:179], v[152:155]\n"
    "ds_read_b64_tr_b16 a[202:203], v13 offset:40960\n"
    "v_add_f32 v31, v31, v244\n"
    "v_mfma_f32_16x16x32_bf16 v[152:155], a[248:251], v[212:215], v[152:155]\n"
    "ds_read_b64_tr_b16 a[204:205], v13 offset:49152\n"
    "v_cvt_pk_bf16_f32 v201, v244, v245\n"
    "v_mfma_f32_16x16x32_bf16 v[152:155], a[252:255], v[224:227], v[152:155]\n"
    "ds_read_b64_tr_b16 a[206:207], v13 offset:57344\n"
    "v_add_f32 v31, v31, v245\n"
    "v_mfma_f32_16x16x32_bf16 v[156:159], a[252:255], v[228:231], v[240:243]\n"
    "ds_read_b64_tr_b16 a[208:209], v14 offset:32768\n"
    "v_add_f32 v32, v32, v246\n"
    "v_mfma_f32_16x16x32_bf16 v[156:159], a[248:251], v[216:219], v[156:159]\n"
    "ds_read_b64_tr_b16 a[210:211], v14 offset:40960\n"
    "v_cvt_pk_bf16_f32 v204, v246, v247\n"
    "v_mfma_f32_16x16x32_bf16 v[156:159], a[244:247], v[180:183], v[156:159]\n"
    "ds_read_b64_tr_b16 a[212:213], v14 offset:49152\n"
    "v_add_f32 v32, v32, v247\n"
    "v_mfma_f32_16x16x32_bf16 v[156:159], a[240:243], v[168:171], v[156:159]\n"
    "ds_read_b64_tr_b16 a[214:215], v14 offset:57344\n"
    "v_add_f32 v32, v32, v248\n"
    "v_mfma_f32_16x16x32_bf16 v[156:159], a[236:239], v[132:135], v[156:159]\n"
    "ds_read_b64_tr_b16 a[216:217], v15 offset:32768\n"
    "v_cvt_pk_bf16_f32 v205, v248, v249\n"
    "v_mfma_f32_16x16x32_bf16 v[156:159], a[232:235], v[120:123], v[156:159]\n"
    "ds_read_b64_tr_b16 a[218:219], v15 offset:40960\n"
    "v_add_f32 v32, v32, v249\n"
    "v_mfma_f32_16x16x32_bf16 v[156:159], a[228:231], v[84:87], v[156:159]\n"
    "ds_read_b64_tr_b16 a[220:221], v15 offset:49152\n"
    "v_mfma_f32_16x16x32_bf16 v[156:159], a[224:227], v[72:75], v[156:159]\n"
    "s_waitcnt lgkmcnt(11)\n"
    "ds_read_b64_tr_b16 a[222:223], v15 offset:57344\n"
    "v_mfma_f32_16x16x32_bf16 a[0:3], a[192:195], v[40:43], a[0:3]\n"
    "v_exp_f32 v250, v148\n"
    "v_mfma_f32_16x16x32_bf16 a[0:3], a[196:199], v[52:55], a[0:3]\n"
    "v_exp_f32 v251, v149\n"
    "v_mfma_f32_16x16x32_bf16 a[4:7], a[196:199], v[56:59], a[4:7]\n"
    "ds_read_b64_tr_b16 a[224:225], v16 offset:32768\n"
    "v_add_f32 v30, v30, v250\n"
    "v_mfma_f32_16x16x32_bf16 a[4:7], a[192:195], v[44:47], a[4:7]\n"
    "ds_read_b64_tr_b16 a[226:227], v16 offset:40960\n"
    "v_cvt_pk_bf16_f32 v198, v250, v251\n"
    "v_mfma_f32_16x16x32_bf16 a[8:11], a[192:195], v[48:51], a[8:11]\n"
    "ds_read_b64_tr_b16 a[228:229], v16 offset:49152\n"
    "v_add_f32 v30, v30, v251\n"
    "v_mfma_f32_16x16x32_bf16 a[8:11], a[196:199], v[60:63], a[8:11]\n"
    "s_waitcnt lgkmcnt(11)\n"
    "ds_read_b64_tr_b16 a[230:231], v16 offset:57344\n"
    "v_mfma_f32_16x16x32_bf16 a[20:23], a[200:203], v[48:51], a[20:23]\n"
    "v_exp_f32 v252, v150\n"
    "v_mfma_f32_16x16x32_bf16 a[20:23], a[204:207], v[60:63], a[20:23]\n"
    "v_exp_f32 v253, v151\n"
    "v_mfma_f32_16x16x32_bf16 a[16:19], a[204:207], v[56:59], a[16:19]\n"
    "ds_read_b64_tr_b16 a[232:233], v17 offset:32768\n"
    "v_add_f32 v30, v30, v252\n"
    "v_mfma_f32_16x16x32_bf16 a[16:19], a[200:203], v[44:47], a[16:19]\n"
    "ds_read_b64_tr_b16 a[234:235], v17 offset:40960\n"
    "v_cvt_pk_bf16_f32 v199, v252, v253\n"
    "v_mfma_f32_16x16x32_bf16 a[12:15], a[200:203], v[40:43], a[12:15]\n"
    "ds_read_b64_tr_b16 a[236:237], v17 offset:49152\n"
    "v_add_f32 v30, v30, v253\n"
    "v_mfma_f32_16x16x32_bf16 a[12:15], a[204:207], v[52:55], a[12:15]\n"
    "s_waitcnt lgkmcnt(11)\n"
    "ds_read_b64_tr_b16 a[238:239], v17 offset:57344\n"
    "v_mfma_f32_16x16x32_bf16 a[24:27], a[208:211], v[40:43], a[24:27]\n"
    "v_exp_f32 v244, v152\n"
    "v_mfma_f32_16x16x32_bf16 a[24:27], a[212:215], v[52:55], a[24:27]\n"
    "v_exp_f32 v245, v153\n"
    "v_mfma_f32_16x16x32_bf16 a[28:31], a[212:215], v[56:59], a[28:31]\n"
    "ds_read_b64_tr_b16 a[240:241], v18 offset:32768\n"
    "v_add_f32 v31, v31, v244\n"
    "v_mfma_f32_16x16x32_bf16 a[28:31], a[208:211], v[44:47], a[28:31]\n"
    "ds_read_b64_tr_b16 a[242:243], v18 offset:40960\n"
    "v_cvt_pk_bf16_f32 v202, v244, v245\n"
    "v_mfma_f32_16x16x32_bf16 a[32:35], a[208:211], v[48:51], a[32:35]\n"
    "ds_read_b64_tr_b16 a[244:245], v18 offset:49152\n"
    "v_add_f32 v31, v31, v245\n"
    "v_mfma_f32_16x16x32_bf16 a[32:35], a[212:215], v[60:63], a[32:35]\n"
    "s_waitcnt lgkmcnt(11)\n"
    "ds_read_b64_tr_b16 a[246:247], v18 offset:57344\n"
    "v_mfma_f32_16x16x32_bf16 a[44:47], a[216:219], v[48:51], a[44:47]\n"
    "v_exp_f32 v246, v154\n"
    "v_mfma_f32_16x16x32_bf16 a[44:47], a[220:223], v[60:63], a[44:47]\n"
    "v_exp_f32 v247, v155\n"
    "v_mfma_f32_16x16x32_bf16 a[40:43], a[220:223], v[56:59], a[40:43]\n"
    "ds_read_b64_tr_b16 a[248:249], v19 offset:32768\n"
    "v_add_f32 v31, v31, v246\n"
    "v_mfma_f32_16x16x32_bf16 a[40:43], a[216:219], v[44:47], a[40:43]\n"
    "ds_read_b64_tr_b16 a[250:251], v19 offset:40960\n"
    "v_cvt_pk_bf16_f32 v203, v246, v247\n"
    "v_mfma_f32_16x16x32_bf16 a[36:39], a[216:219], v[40:43], a[36:39]\n"
    "ds_read_b64_tr_b16 a[252:253], v19 offset:49152\n"
    "v_add_f32 v31, v31, v247\n"
    "v_mfma_f32_16x16x32_bf16 a[36:39], a[220:223], v[52:55], a[36:39]\n"
    "s_waitcnt lgkmcnt(11)\n"
    "ds_read_b64_tr_b16 a[254:255], v19 offset:57344\n"
    "v_mfma_f32_16x16x32_bf16 a[48:51], a[224:227], v[40:43], a[48:51]\n"
    "v_exp_f32 v248, v156\n"
    "v_mfma_f32_16x16x32_bf16 a[48:51], a[228:231], v[52:55], a[48:51]\n"
    "v_exp_f32 v249, v157\n"
    "v_mfma_f32_16x16x32_bf16 a[52:55], a[228:231], v[56:59], a[52:55]\n"
    "ds_read_b64_tr_b16 a[192:193], v12 offset:33024\n"
    "v_add_f32 v32, v32, v248\n"
    "v_mfma_f32_16x16x32_bf16 a[52:55], a[224:227], v[44:47], a[52:55]\n"
    "ds_read_b64_tr_b16 a[194:195], v12 offset:41216\n"
    "v_cvt_pk_bf16_f32 v206, v248, v249\n"
    "v_mfma_f32_16x16x32_bf16 a[56:59], a[224:227], v[48:51], a[56:59]\n"
    "ds_read_b64_tr_b16 a[196:197], v12 offset:49408\n"
    "v_add_f32 v32, v32, v249\n"
    "v_mfma_f32_16x16x32_bf16 a[56:59], a[228:231], v[60:63], a[56:59]\n"
    "s_waitcnt lgkmcnt(11)\n"
    "ds_read_b64_tr_b16 a[198:199], v12 offset:57600\n"
    "v_mfma_f32_16x16x32_bf16 a[68:71], a[232:235], v[48:51], a[68:71]\n"
    "v_exp_f32 v250, v158\n"
    "v_mfma_f32_16x16x32_bf16 a[68:71], a[236:239], v[60:63], a[68:71]\n"
    "v_exp_f32 v251, v159\n"
    "v_mfma_f32_16x16x32_bf16 a[64:67], a[236:239], v[56:59], a[64:67]\n"
    "ds_read_b64_tr_b16 a[200:201], v13 offset:33024\n"
    "v_add_f32 v32, v32, v250\n"
    "v_mfma_f32_16x16x32_bf16 a[64:67], a[232:235], v[44:47], a[64:67]\n"
    "ds_read_b64_tr_b16 a[202:203], v13 offset:41216\n"
    "v_cvt_pk_bf16_f32 v207, v250, v251\n"
    "v_mfma_f32_16x16x32_bf16 a[60:63], a[232:235], v[40:43], a[60:63]\n"
    "ds_read_b64_tr_b16 a[204:205], v13 offset:49408\n"
    "v_add_f32 v32, v32, v251\n"
    "v_mfma_f32_16x16x32_bf16 a[60:63], a[236:239], v[52:55], a[60:63]\n"
    "s_waitcnt lgkmcnt(11)\n"
    "ds_read_b64_tr_b16 a[206:207], v13 offset:57600\n"
    "v_max3_f32 v254, v30, v31, v32\n"
    "v_mfma_f32_16x16x32_bf16 a[72:75], a[240:243], v[40:43], a[72:75]\n"
    "v_cmp_lt_f32 vcc, s58, v254\n"
    "v_mfma_f32_16x16x32_bf16 a[72:75], a[244:247], v[52:55], a[72:75]\n"
    "v_mfma_f32_16x16x32_bf16 a[76:79], a[244:247], v[56:59], a[76:79]\n"
    "ds_read_b64_tr_b16 a[208:209], v14 offset:33024\n"
    "v_mfma_f32_16x16x32_bf16 a[76:79], a[240:243], v[44:47], a[76:79]\n"
    "ds_read_b64_tr_b16 a[210:211], v14 offset:41216\n"
    "v_mfma_f32_16x16x32_bf16 a[80:83], a[240:243], v[48:51], a[80:83]\n"
    "ds_read_b64_tr_b16 a[212:213], v14 offset:49408\n"
    "v_mfma_f32_16x16x32_bf16 a[80:83], a[244:247], v[60:63], a[80:83]\n"
    "s_waitcnt lgkmcnt(11)\n"
    "ds_read_b64_tr_b16 a[214:215], v14 offset:57600\n"
    "v_mfma_f32_16x16x32_bf16 a[92:95], a[248:251], v[48:51], a[92:95]\n"
    "v_mfma_f32_16x16x32_bf16 a[92:95], a[252:255], v[60:63], a[92:95]\n"
    "v_mfma_f32_16x16x32_bf16 a[88:91], a[252:255], v[56:59], a[88:91]\n"
    "ds_read_b64_tr_b16 a[216:217], v15 offset:33024\n"
    "v_mfma_f32_16x16x32_bf16 a[88:91], a[248:251], v[44:47], a[88:91]\n"
    "ds_read_b64_tr_b16 a[218:219], v15 offset:41216\n"
    "v_mfma_f32_16x16x32_bf16 a[84:87], a[248:251], v[40:43], a[84:87]\n"
    "ds_read_b64_tr_b16 a[220:221], v15 offset:49408\n"
    "v_mfma_f32_16x16x32_bf16 a[84:87], a[252:255], v[52:55], a[84:87]\n"
    "s_waitcnt lgkmcnt(11)\n"
    "ds_read_b64_tr_b16 a[222:223], v15 offset:57600\n"
    "v_mfma_f32_16x16x32_bf16 a[96:99], a[192:195], v[40:43], a[96:99]\n"
    "v_mfma_f32_16x16x32_bf16 a[96:99], a[196:199], v[52:55], a[96:99]\n"
    "v_mfma_f32_16x16x32_bf16 a[100:103], a[196:199], v[56:59], a[100:103]\n"
    "ds_read_b64_tr_b16 a[224:225], v16 offset:33024\n"
    "v_mfma_f32_16x16x32_bf16 a[100:103], a[192:195], v[44:47], a[100:103]\n"
    "ds_read_b64_tr_b16 a[226:227], v16 offset:41216\n"
    "v_mfma_f32_16x16x32_bf16 a[104:107], a[192:195], v[48:51], a[104:107]\n"
    "ds_read_b64_tr_b16 a[228:229], v16 offset:49408\n"
    "v_mfma_f32_16x16x32_bf16 a[104:107], a[196:199], v[60:63], a[104:107]\n"
    "s_waitcnt lgkmcnt(11)\n"
    "ds_read_b64_tr_b16 a[230:231], v16 offset:57600\n"
    "v_mfma_f32_16x16x32_bf16 a[116:119], a[200:203], v[48:51], a[116:119]\n"
    "v_mfma_f32_16x16x32_bf16 a[116:119], a[204:207], v[60:63], a[116:119]\n"
    "v_mfma_f32_16x16x32_bf16 a[112:115], a[204:207], v[56:59], a[112:115]\n"
    "ds_read_b64_tr_b16 a[232:233], v17 offset:33024\n"
    "v_mfma_f32_16x16x32_bf16 a[112:115], a[200:203], v[44:47], a[112:115]\n"
    "ds_read_b64_tr_b16 a[234:235], v17 offset:41216\n"
    "v_mfma_f32_16x16x32_bf16 a[108:111], a[200:203], v[40:43], a[108:111]\n"
    "ds_read_b64_tr_b16 a[236:237], v17 offset:49408\n"
    "v_mfma_f32_16x16x32_bf16 a[108:111], a[204:207], v[52:55], a[108:111]\n"
    "s_waitcnt lgkmcnt(11)\n"
    "ds_read_b64_tr_b16 a[238:239], v17 offset:57600\n"
    "v_mfma_f32_16x16x32_bf16 a[120:123], a[208:211], v[40:43], a[120:123]\n"
    "v_mfma_f32_16x16x32_bf16 a[120:123], a[212:215], v[52:55], a[120:123]\n"
    "v_mfma_f32_16x16x32_bf16 a[124:127], a[212:215], v[56:59], a[124:127]\n"
    "ds_read_b64_tr_b16 a[240:241], v18 offset:33024\n"
    "v_mfma_f32_16x16x32_bf16 a[124:127], a[208:211], v[44:47], a[124:127]\n"
    "ds_read_b64_tr_b16 a[242:243], v18 offset:41216\n"
    "v_mfma_f32_16x16x32_bf16 a[128:131], a[208:211], v[48:51], a[128:131]\n"
    "ds_read_b64_tr_b16 a[244:245], v18 offset:49408\n"
    "v_mfma_f32_16x16x32_bf16 a[128:131], a[212:215], v[60:63], a[128:131]\n"
    "s_waitcnt lgkmcnt(11)\n"
    "ds_read_b64_tr_b16 a[246:247], v18 offset:57600\n"
    "v_mfma_f32_16x16x32_bf16 a[140:143], a[216:219], v[48:51], a[140:143]\n"
    "v_mfma_f32_16x16x32_bf16 a[140:143], a[220:223], v[60:63], a[140:143]\n"
    "v_mfma_f32_16x16x32_bf16 a[136:139], a[220:223], v[56:59], a[136:139]\n"
    "ds_read_b64_tr_b16 a[248:249], v19 offset:33024\n"
    "v_mfma_f32_16x16x32_bf16 a[136:139], a[216:219], v[44:47], a[136:139]\n"
    "ds_read_b64_tr_b16 a[250:251], v19 offset:41216\n"
    "v_mfma_f32_16x16x32_bf16 a[132:135], a[216:219], v[40:43], a[132:135]\n"
    "ds_read_b64_tr_b16 a[252:253], v19 offset:49408\n"
    "v_mfma_f32_16x16x32_bf16 a[132:135], a[220:223], v[52:55], a[132:135]\n"
    "s_waitcnt lgkmcnt(11)\n"
    "ds_read_b64_tr_b16 a[254:255], v19 offset:57600\n"
    "v_mfma_f32_16x16x32_bf16 a[144:147], a[224:227], v[40:43], a[144:147]\n"
    "v_mfma_f32_16x16x32_bf16 a[144:147], a[228:231], v[52:55], a[144:147]\n"
    "v_mfma_f32_16x16x32_bf16 a[148:151], a[228:231], v[56:59], a[148:151]\n"
    "v_mfma_f32_16x16x32_bf16 a[148:151], a[224:227], v[44:47], a[148:151]\n"
    "v_mfma_f32_16x16x32_bf16 a[152:155], a[224:227], v[48:51], a[152:155]\n"
    "v_mfma_f32_16x16x32_bf16 a[152:155], a[228:231], v[60:63], a[152:155]\n"
    "s_waitcnt lgkmcnt(8)\n"
    "v_mfma_f32_16x16x32_bf16 a[164:167], a[232:235], v[48:51], a[164:167]\n"
    "v_mfma_f32_16x16x32_bf16 a[164:167], a[236:239], v[60:63], a[164:167]\n"
    "v_mfma_f32_16x16x32_bf16 a[160:163], a[236:239], v[56:59], a[160:163]\n"
    "v_mfma_f32_16x16x32_bf16 a[160:163], a[232:235], v[44:47], a[160:163]\n"
    "v_mfma_f32_16x16x32_bf16 a[156:159], a[232:235], v[40:43], a[156:159]\n"
    "v_mfma_f32_16x16x32_bf16 a[156:159], a[236:239], v[52:55], a[156:159]\n"
    "s_waitcnt lgkmcnt(0)\n"
    "s_barrier\n"
    "v_mfma_f32_16x16x32_bf16 a[168:171], a[240:243], v[40:43], a[168:171]\n"
    "ds_read_b128 a[192:195], v8 offset:32768\n"
    "v_mfma_f32_16x16x32_bf16 a[168:171], a[244:247], v[52:55], a[168:171]\n"
    "ds_read_b128 a[196:199], v9 offset:32768\n"
    "v_mfma_f32_16x16x32_bf16 a[172:175], a[244:247], v[56:59], a[172:175]\n"
    "ds_read_b128 a[200:203], v10 offset:32768\n"
    "v_mfma_f32_16x16x32_bf16 a[172:175], a[240:243], v[44:47], a[172:175]\n"
    "ds_read_b128 a[204:207], v11 offset:32768\n"
    "v_mfma_f32_16x16x32_bf16 a[176:179], a[240:243], v[48:51], a[176:179]\n"
    "ds_read_b128 a[208:211], v8 offset:33024\n"
    "v_mfma_f32_16x16x32_bf16 a[176:179], a[244:247], v[60:63], a[176:179]\n"
    "ds_read_b128 a[212:215], v9 offset:33024\n"
    "v_mfma_f32_16x16x32_bf16 a[188:191], a[248:251], v[48:51], a[188:191]\n"
    "ds_read_b128 a[216:219], v10 offset:33024\n"
    "v_mfma_f32_16x16x32_bf16 a[188:191], a[252:255], v[60:63], a[188:191]\n"
    "ds_read_b128 a[220:223], v11 offset:33024\n"
    "v_mfma_f32_16x16x32_bf16 a[184:187], a[252:255], v[56:59], a[184:187]\n"
    "v_mfma_f32_16x16x32_bf16 a[184:187], a[248:251], v[44:47], a[184:187]\n"
    "v_mfma_f32_16x16x32_bf16 a[180:183], a[248:251], v[40:43], a[180:183]\n"
    "v_mfma_f32_16x16x32_bf16 a[180:183], a[252:255], v[52:55], a[180:183]\n"
    "s_waitcnt lgkmcnt(6)\n"
    "s_cbranch_vccnz Lstub3_%=\n"
    "Lok3_%=:\n"
    "v_mfma_f32_16x16x32_bf16 v[88:91], a[192:195], v[64:67], v[232:235]\n"
    "s_waitcnt lgkmcnt(5)\n"
    "v_add_f32 v27, v27, v30\n"
    "v_add_f32 v28, v28, v31\n"
    "v_mfma_f32_16x16x32_bf16 v[88:91], a[196:199], v[76:79], v[88:91]\n"
    "v_add_f32 v29, v29, v32\n"
    "v_mfma_f32_16x16x32_bf16 v[88:91], a[200:203], v[112:115], v[88:91]\n"
    "s_waitcnt lgkmcnt(3)\n"
    "ds_read_b128 a[252:255], v11 offset:41216\n"
    "v_mfma_f32_16x16x32_bf16 v[88:91], a[204:207], v[124:127], v[88:91]\n"
    "ds_read_b128 a[248:251], v10 offset:41216\n"
    "v_mfma_f32_16x16x32_bf16 v[88:91], a[208:211], v[160:163], v[88:91]\n"
    "s_waitcnt lgkmcnt(3)\n"
    "ds_read_b128 a[244:247], v9 offset:41216\n"
    "v_mfma_f32_16x16x32_bf16 v[88:91], a[212:215], v[172:175], v[88:91]\n"
    "ds_read_b128 a[240:243], v8 offset:41216\n"
    "v_mfma_f32_16x16x32_bf16 v[88:91], a[216:219], v[208:211], v[88:91]\n"
    "s_waitcnt lgkmcnt(4)\n"
    "ds_read_b128 a[236:239], v11 offset:40960\n"
    "v_mfma_f32_16x16x32_bf16 v[88:91], a[220:223], v[220:223], v[88:91]\n"
    "ds_read_b128 a[232:235], v10 offset:40960\n"
    "v_mfma_f32_16x16x32_bf16 v[92:95], a[220:223], v[224:227], v[236:239]\n"
    "ds_read_b128 a[228:231], v9 offset:40960\n"
    "v_mfma_f32_16x16x32_bf16 v[92:95], a[216:219], v[212:215], v[92:95]\n"
    "ds_read_b128 a[224:227], v8 offset:40960\n"
    "v_mfma_f32_16x16x32_bf16 v[92:95], a[212:215], v[176:179], v[92:95]\n"
    "v_mfma_f32_16x16x32_bf16 v[92:95], a[208:211], v[164:167], v[92:95]\n"
    "v_mfma_f32_16x16x32_bf16 v[92:95], a[204:207], v[128:131], v[92:95]\n"
    "v_exp_f32 v244, v88\n"
    "v_mfma_f32_16x16x32_bf16 v[92:95], a[200:203], v[116:119], v[92:95]\n"
    "v_exp_f32 v245, v89\n"
    "v_mfma_f32_16x16x32_bf16 v[92:95], a[196:199], v[80:83], v[92:95]\n"
    "v_exp_f32 v246, v90\n"
    "v_mfma_f32_16x16x32_bf16 v[92:95], a[192:195], v[68:71], v[92:95]\n"
    "v_exp_f32 v247, v91\n"
    "v_mfma_f32_16x16x32_bf16 v[96:99], a[192:195], v[72:75], v[240:243]\n"
    "v_add_f32 v30, v244, v245\n"
    "v_cvt_pk_bf16_f32 v40, v244, v245\n"
    "v_mfma_f32_16x16x32_bf16 v[96:99], a[196:199], v[84:87], v[96:99]\n"
    "v_add_f32 v30, v30, v246\n"
    "v_cvt_pk_bf16_f32 v41, v246, v247\n"
    "v_mfma_f32_16x16x32_bf16 v[96:99], a[200:203], v[120:123], v[96:99]\n"
    "v_exp_f32 v248, v92\n"
    "v_mfma_f32_16x16x32_bf16 v[96:99], a[204:207], v[132:135], v[96:99]\n"
    "v_exp_f32 v249, v93\n"
    "v_mfma_f32_16x16x32_bf16 v[96:99], a[208:211], v[168:171], v[96:99]\n"
    "v_exp_f32 v250, v94\n"
    "v_mfma_f32_16x16x32_bf16 v[96:99], a[212:215], v[180:183], v[96:99]\n"
    "v_exp_f32 v251, v95\n"
    "v_mfma_f32_16x16x32_bf16 v[96:99], a[216:219], v[216:219], v[96:99]\n"
    "v_add_f32 v30, v30, v247\n"
    "v_add_f32 v31, v248, v249\n"
    "v_mfma_f32_16x16x32_bf16 v[96:99], a[220:223], v[228:231], v[96:99]\n"
    "s_waitcnt lgkmcnt(6)\n"
    "v_cvt_pk_bf16_f32 v44, v248, v249\n"
    "v_add_f32 v31, v31, v250\n"
    "v_mfma_f32_16x16x32_bf16 v[100:103], a[252:255], v[220:223], v[232:235]\n"
    "v_cvt_pk_bf16_f32 v45, v250, v251\n"
    "v_add_f32 v31, v31, v251\n"
    "v_mfma_f32_16x16x32_bf16 v[100:103], a[248:251], v[208:211], v[100:103]\n"
    "s_waitcnt lgkmcnt(4)\n"
    "v_mfma_f32_16x16x32_bf16 v[100:103], a[244:247], v[172:175], v[100:103]\n"
    "ds_read_b128 a[192:195], v8 offset:49152\n"
    "v_mfma_f32_16x16x32_bf16 v[100:103], a[240:243], v[160:163], v[100:103]\n"
    "s_waitcnt lgkmcnt(3)\n"
    "ds_read_b128 a[196:199], v9 offset:49152\n"
    "v_mfma_f32_16x16x32_bf16 v[100:103], a[236:239], v[124:127], v[100:103]\n"
    "ds_read_b128 a[200:203], v10 offset:49152\n"
    "v_mfma_f32_16x16x32_bf16 v[100:103], a[232:235], v[112:115], v[100:103]\n"
    "s_waitcnt lgkmcnt(3)\n"
    "ds_read_b128 a[204:207], v11 offset:49152\n"
    "v_mfma_f32_16x16x32_bf16 v[100:103], a[228:231], v[76:79], v[100:103]\n"
    "ds_read_b128 a[208:211], v8 offset:49408\n"
    "v_mfma_f32_16x16x32_bf16 v[100:103], a[224:227], v[64:67], v[100:103]\n"
    "ds_read_b128 a[212:215], v9 offset:49408\n"
    "v_mfma_f32_16x16x32_bf16 v[104:107], a[224:227], v[68:71], v[236:239]\n"
    "ds_read_b128 a[216:219], v10 offset:49408\n"
    "v_mfma_f32_16x16x32_bf16 v[104:107], a[228:231], v[80:83], v[104:107]\n"
    "ds_read_b128 a[220:223], v11 offset:49408\n"
    "v_mfma_f32_16x16x32_bf16 v[104:107], a[232:235], v[116:119], v[104:107]\n"
    "v_exp_f32 v252, v96\n"
    "v_mfma_f32_16x16x32_bf16 v[104:107], a[236:239], v[128:131], v[104:107]\n"
    "v_exp_f32 v253, v97\n"
    "v_mfma_f32_16x16x32_bf16 v[104:107], a[240:243], v[164:167], v[104:107]\n"
    "v_exp_f32 v244, v98\n"
    "v_mfma_f32_16x16x32_bf16 v[104:107], a[244:247], v[176:179], v[104:107]\n"
    "v_exp_f32 v245, v99\n"
    "v_mfma_f32_16x16x32_bf16 v[104:107], a[248:251], v[212:215], v[104:107]\n"
    "v_exp_f32 v246, v100\n"
    "v_mfma_f32_16x16x32_bf16 v[104:107], a[252:255], v[224:227], v[104:107]\n"
    "v_exp_f32 v247, v101\n"
    "v_mfma_f32_16x16x32_bf16 v[108:111], a[252:255], v[228:231], v[240:243]\n"
    "v_exp_f32 v248, v102\n"
    "v_mfma_f32_16x16x32_bf16 v[108:111], a[248:251], v[216:219], v[108:111]\n"
    "v_exp_f32 v249, v103\n"
    "v_mfma_f32_16x16x32_bf16 v[108:111], a[244:247], v[180:183], v[108:111]\n"
    "v_add_f32 v32, v252, v253\n"
    "v_cvt_pk_bf16_f32 v48, v252, v253\n"
    "v_mfma_f32_16x16x32_bf16 v[108:111], a[240:243], v[168:171], v[108:111]\n"
    "v_exp_f32 v250, v104\n"
    "v_mfma_f32_16x16x32_bf16 v[108:111], a[236:239], v[132:135], v[108:111]\n"
    "v_exp_f32 v251, v105\n"
    "v_mfma_f32_16x16x32_bf16 v[108:111], a[232:235], v[120:123], v[108:111]\n"
    "v_exp_f32 v252, v106\n"
    "v_mfma_f32_16x16x32_bf16 v[108:111], a[228:231], v[84:87], v[108:111]\n"
    "v_exp_f32 v253, v107\n"
    "v_mfma_f32_16x16x32_bf16 v[108:111], a[224:227], v[72:75], v[108:111]\n"
    "s_waitcnt lgkmcnt(6)\n"
    "v_add_f32 v32, v32, v244\n"
    "v_cvt_pk_bf16_f32 v49, v244, v245\n"
    "v_mfma_f32_16x16x32_bf16 v[136:139], a[192:195], v[64:67], v[232:235]\n"
    "v_add_f32 v32, v32, v245\n"
    "v_add_f32 v30, v30, v246\n"
    "v_mfma_f32_16x16x32_bf16 v[136:139], a[196:199], v[76:79], v[136:139]\n"
    "s_waitcnt lgkmcnt(4)\n"
    "v_cvt_pk_bf16_f32 v42, v246, v247\n"
    "v_add_f32 v30, v30, v247\n"
    "v_mfma_f32_16x16x32_bf16 v[136:139], a[200:203], v[112:115], v[136:139]\n"
    "ds_read_b128 a[252:255], v11 offset:57600\n"
    "v_add_f32 v30, v30, v248\n"
    "v_mfma_f32_16x16x32_bf16 v[136:139], a[204:207], v[124:127], v[136:139]\n"
    "s_waitcnt lgkmcnt(3)\n"
    "ds_read_b128 a[248:251], v10 offset:57600\n"
    "v_cvt_pk_bf16_f32 v43, v248, v249\n"
    "v_mfma_f32_16x16x32_bf16 v[136:139], a[208:211], v[160:163], v[136:139]\n"
    "ds_read_b128 a[244:247], v9 offset:57600\n"
    "v_add_f32 v30, v30, v249\n"
    "v_mfma_f32_16x16x32_bf16 v[136:139], a[212:215], v[172:175], v[136:139]\n"
    "s_waitcnt lgkmcnt(3)\n"
    "ds_read_b128 a[240:243], v8 offset:57600\n"
    "v_add_f32 v31, v31, v250\n"
    "v_mfma_f32_16x16x32_bf16 v[136:139], a[216:219], v[208:211], v[136:139]\n"
    "ds_read_b128 a[236:239], v11 offset:57344\n"
    "v_cvt_pk_bf16_f32 v46, v250, v251\n"
    "v_mfma_f32_16x16x32_bf16 v[136:139], a[220:223], v[220:223], v[136:139]\n"
    "ds_read_b128 a[232:235], v10 offset:57344\n"
    "v_add_f32 v31, v31, v251\n"
    "v_mfma_f32_16x16x32_bf16 v[140:143], a[220:223], v[224:227], v[236:239]\n"
    "ds_read_b128 a[228:231], v9 offset:57344\n"
    "v_add_f32 v31, v31, v252\n"
    "v_mfma_f32_16x16x32_bf16 v[140:143], a[216:219], v[212:215], v[140:143]\n"
    "ds_read_b128 a[224:227], v8 offset:57344\n"
    "v_cvt_pk_bf16_f32 v47, v252, v253\n"
    "v_mfma_f32_16x16x32_bf16 v[140:143], a[212:215], v[176:179], v[140:143]\n"
    "v_exp_f32 v244, v108\n"
    "v_mfma_f32_16x16x32_bf16 v[140:143], a[208:211], v[164:167], v[140:143]\n"
    "v_exp_f32 v245, v109\n"
    "v_mfma_f32_16x16x32_bf16 v[140:143], a[204:207], v[128:131], v[140:143]\n"
    "v_exp_f32 v246, v110\n"
    "v_mfma_f32_16x16x32_bf16 v[140:143], a[200:203], v[116:119], v[140:143]\n"
    "v_exp_f32 v247, v111\n"
    "v_mfma_f32_16x16x32_bf16 v[140:143], a[196:199], v[80:83], v[140:143]\n"
    "v_exp_f32 v248, v136\n"
    "v_mfma_f32_16x16x32_bf16 v[140:143], a[192:195], v[68:71], v[140:143]\n"
    "v_exp_f32 v249, v137\n"
    "v_mfma_f32_16x16x32_bf16 v[144:147], a[192:195], v[72:75], v[240:243]\n"
    "v_exp_f32 v250, v138\n"
    "v_mfma_f32_16x16x32_bf16 v[144:147], a[196:199], v[84:87], v[144:147]\n"
    "v_exp_f32 v251, v139\n"
    "v_mfma_f32_16x16x32_bf16 v[144:147], a[200:203], v[120:123], v[144:147]\n"
    "v_add_f32 v31, v31, v253\n"
    "v_add_f32 v32, v32, v244\n"
    "v_mfma_f32_16x16x32_bf16 v[144:147], a[204:207], v[132:135], v[144:147]\n"
    "v_exp_f32 v252, v140\n"
    "v_mfma_f32_16x16x32_bf16 v[144:147], a[208:211], v[168:171], v[144:147]\n"
    "v_exp_f32 v253, v141\n"
    "v_mfma_f32_16x16x32_bf16 v[144:147], a[212:215], v[180:183], v[144:147]\n"
    "v_cvt_pk_bf16_f32 v50, v244, v245\n"
    "v_add_f32 v32, v32, v245\n"
    "v_mfma_f32_16x16x32_bf16 v[144:147], a[216:219], v[216:219], v[144:147]\n"
    "v_exp_f32 v244, v142\n"
    "v_mfma_f32_16x16x32_bf16 v[144:147], a[220:223], v[228:231], v[144:147]\n"
    "s_waitcnt lgkmcnt(6)\n"
    "v_exp_f32 v245, v143\n"
    "v_mfma_f32_16x16x32_bf16 v[148:151], a[252:255], v[220:223], v[232:235]\n"
    "v_add_f32 v32, v32, v246\n"
    "v_cvt_pk_bf16_f32 v51, v246, v247\n"
    "v_mfma_f32_16x16x32_bf16 v[148:151], a[248:251], v[208:211], v[148:151]\n"
    "s_waitcnt lgkmcnt(4)\n"
    "v_add_f32 v32, v32, v247\n"
    "v_add_f32 v30, v30, v248\n"
    "v_mfma_f32_16x16x32_bf16 v[148:151], a[244:247], v[172:175], v[148:151]\n"
    "v_exp_f32 v246, v144\n"
    "v_mfma_f32_16x16x32_bf16 v[148:151], a[240:243], v[160:163], v[148:151]\n"
    "s_waitcnt lgkmcnt(2)\n"
    "v_exp_f32 v247, v145\n"
    "v_mfma_f32_16x16x32_bf16 v[148:151], a[236:239], v[124:127], v[148:151]\n"
    "v_cvt_pk_bf16_f32 v52, v248, v249\n"
    "v_add_f32 v30, v30, v249\n"
    "v_mfma_f32_16x16x32_bf16 v[148:151], a[232:235], v[112:115], v[148:151]\n"
    "s_waitcnt lgkmcnt(0)\n"
    "v_exp_f32 v248, v146\n"
    "v_mfma_f32_16x16x32_bf16 v[148:151], a[228:231], v[76:79], v[148:151]\n"
    "v_exp_f32 v249, v147\n"
    "v_mfma_f32_16x16x32_bf16 v[148:151], a[224:227], v[64:67], v[148:151]\n"
    "s_waitcnt lgkmcnt(0)\n"
    "s_barrier\n"
    "v_add_f32 v30, v30, v250\n"
    "v_mfma_f32_16x16x32_bf16 v[152:155], a[224:227], v[68:71], v[236:239]\n"
    "ds_read_b64_tr_b16 a[192:193], v12 offset:0\n"
    "v_cvt_pk_bf16_f32 v53, v250, v251\n"
    "v_mfma_f32_16x16x32_bf16 v[152:155], a[228:231], v[80:83], v[152:155]\n"
    "ds_read_b64_tr_b16 a[194:195], v12 offset:8192\n"
    "v_add_f32 v30, v30, v251\n"
    "v_mfma_f32_16x16x32_bf16 v[152:155], a[232:235], v[116:119], v[152:155]\n"
    "ds_read_b64_tr_b16 a[196:197], v12 offset:16384\n"
    "v_add_f32 v31, v31, v252\n"
    "v_mfma_f32_16x16x32_bf16 v[152:155], a[236:239], v[128:131], v[152:155]\n"
    "ds_read_b64_tr_b16 a[198:199], v12 offset:24576\n"
    "v_cvt_pk_bf16_f32 v56, v252, v253\n"
    "v_mfma_f32_16x16x32_bf16 v[152:155], a[240:243], v[164:167], v[152:155]\n"
    "ds_read_b64_tr_b16 a[200:201], v13 offset:0\n"
    "v_add_f32 v31, v31, v253\n"
    "v_mfma_f32_16x16x32_bf16 v[152:155], a[244:247], v[176:179], v[152:155]\n"
    "ds_read_b64_tr_b16 a[202:203], v13 offset:8192\n"
    "v_add_f32 v31, v31, v244\n"
    "v_mfma_f32_16x16x32_bf16 v[152:155], a[248:251], v[212:215], v[152:155]\n"
    "ds_read_b64_tr_b16 a[204:205], v13 offset:16384\n"
    "v_cvt_pk_bf16_f32 v57, v244, v245\n"
    "v_mfma_f32_16x16x32_bf16 v[152:155], a[252:255], v[224:227], v[152:155]\n"
    "ds_read_b64_tr_b16 a[206:207], v13 offset:24576\n"
    "v_add_f32 v31, v31, v245\n"
    "v_mfma_f32_16x16x32_bf16 v[156:159], a[252:255], v[228:231], v[240:243]\n"
    "ds_read_b64_tr_b16 a[208:209], v14 offset:0\n"
    "v_add_f32 v32, v32, v246\n"
    "v_mfma_f32_16x16x32_bf16 v[156:159], a[248:251], v[216:219], v[156:159]\n"
    "ds_read_b64_tr_b16 a[210:211], v14 offset:8192\n"
    "v_cvt_pk_bf16_f32 v60, v246, v247\n"
    "v_mfma_f32_16x16x32_bf16 v[156:159], a[244:247], v[180:183], v[156:159]\n"
    "ds_read_b64_tr_b16 a[212:213], v14 offset:16384\n"
    "v_add_f32 v32, v32, v247\n"
    "v_mfma_f32_16x16x32_bf16 v[156:159], a[240:243], v[168:171], v[156:159]\n"
    "ds_read_b64_tr_b16 a[214:215], v14 offset:24576\n"
    "v_add_f32 v32, v32, v248\n"
    "v_mfma_f32_16x16x32_bf16 v[156:159], a[236:239], v[132:135], v[156:159]\n"
    "ds_read_b64_tr_b16 a[216:217], v15 offset:0\n"
    "v_cvt_pk_bf16_f32 v61, v248, v249\n"
    "v_mfma_f32_16x16x32_bf16 v[156:159], a[232:235], v[120:123], v[156:159]\n"
    "ds_read_b64_tr_b16 a[218:219], v15 offset:8192\n"
    "v_add_f32 v32, v32, v249\n"
    "v_mfma_f32_16x16x32_bf16 v[156:159], a[228:231], v[84:87], v[156:159]\n"
    "ds_read_b64_tr_b16 a[220:221], v15 offset:16384\n"
    "v_mfma_f32_16x16x32_bf16 v[156:159], a[224:227], v[72:75], v[156:159]\n"
    "s_waitcnt lgkmcnt(11)\n"
    "ds_read_b64_tr_b16 a[222:223], v15 offset:24576\n"
    "v_mfma_f32_16x16x32_bf16 a[0:3], a[192:195], v[184:187], a[0:3]\n"
    "v_exp_f32 v250, v148\n"
    "v_mfma_f32_16x16x32_bf16 a[0:3], a[196:199], v[196:199], a[0:3]\n"
    "v_exp_f32 v251, v149\n"
    "v_mfma_f32_16x16x32_bf16 a[4:7], a[196:199], v[200:203], a[4:7]\n"
    "ds_read_b64_tr_b16 a[224:225], v16 offset:0\n"
    "v_add_f32 v30, v30, v250\n"
    "v_mfma_f32_16x16x32_bf16 a[4:7], a[192:195], v[188:191], a[4:7]\n"
    "ds_read_b64_tr_b16 a[226:227], v16 offset:8192\n"
    "v_cvt_pk_bf16_f32 v54, v250, v251\n"
    "v_mfma_f32_16x16x32_bf16 a[8:11], a[192:195], v[192:195], a[8:11]\n"
    "ds_read_b64_tr_b16 a[228:229], v16 offset:16384\n"
    "v_add_f32 v30, v30, v251\n"
    "v_mfma_f32_16x16x32_bf16 a[8:11], a[196:199], v[204:207], a[8:11]\n"
    "s_waitcnt lgkmcnt(11)\n"
    "ds_read_b64_tr_b16 a[230:231], v16 offset:24576\n"
    "v_mfma_f32_16x16x32_bf16 a[20:23], a[200:203], v[192:195], a[20:23]\n"
    "v_exp_f32 v252, v150\n"
    "v_mfma_f32_16x16x32_bf16 a[20:23], a[204:207], v[204:207], a[20:23]\n"
    "v_exp_f32 v253, v151\n"
    "v_mfma_f32_16x16x32_bf16 a[16:19], a[204:207], v[200:203], a[16:19]\n"
    "ds_read_b64_tr_b16 a[232:233], v17 offset:0\n"
    "v_add_f32 v30, v30, v252\n"
    "v_mfma_f32_16x16x32_bf16 a[16:19], a[200:203], v[188:191], a[16:19]\n"
    "ds_read_b64_tr_b16 a[234:235], v17 offset:8192\n"
    "v_cvt_pk_bf16_f32 v55, v252, v253\n"
    "v_mfma_f32_16x16x32_bf16 a[12:15], a[200:203], v[184:187], a[12:15]\n"
    "ds_read_b64_tr_b16 a[236:237], v17 offset:16384\n"
    "v_add_f32 v30, v30, v253\n"
    "v_mfma_f32_16x16x32_bf16 a[12:15], a[204:207], v[196:199], a[12:15]\n"
    "s_waitcnt lgkmcnt(11)\n"
    "ds_read_b64_tr_b16 a[238:239], v17 offset:24576\n"
    "v_mfma_f32_16x16x32_bf16 a[24:27], a[208:211], v[184:187], a[24:27]\n"
    "v_exp_f32 v244, v152\n"
    "v_mfma_f32_16x16x32_bf16 a[24:27], a[212:215], v[196:199], a[24:27]\n"
    "v_exp_f32 v245, v153\n"
    "v_mfma_f32_16x16x32_bf16 a[28:31], a[212:215], v[200:203], a[28:31]\n"
    "ds_read_b64_tr_b16 a[240:241], v18 offset:0\n"
    "v_add_f32 v31, v31, v244\n"
    "v_mfma_f32_16x16x32_bf16 a[28:31], a[208:211], v[188:191], a[28:31]\n"
    "ds_read_b64_tr_b16 a[242:243], v18 offset:8192\n"
    "v_cvt_pk_bf16_f32 v58, v244, v245\n"
    "v_mfma_f32_16x16x32_bf16 a[32:35], a[208:211], v[192:195], a[32:35]\n"
    "ds_read_b64_tr_b16 a[244:245], v18 offset:16384\n"
    "v_add_f32 v31, v31, v245\n"
    "v_mfma_f32_16x16x32_bf16 a[32:35], a[212:215], v[204:207], a[32:35]\n"
    "s_waitcnt lgkmcnt(11)\n"
    "ds_read_b64_tr_b16 a[246:247], v18 offset:24576\n"
    "v_mfma_f32_16x16x32_bf16 a[44:47], a[216:219], v[192:195], a[44:47]\n"
    "v_exp_f32 v246, v154\n"
    "v_mfma_f32_16x16x32_bf16 a[44:47], a[220:223], v[204:207], a[44:47]\n"
    "v_exp_f32 v247, v155\n"
    "v_mfma_f32_16x16x32_bf16 a[40:43], a[220:223], v[200:203], a[40:43]\n"
    "ds_read_b64_tr_b16 a[248:249], v19 offset:0\n"
    "v_add_f32 v31, v31, v246\n"
    "v_mfma_f32_16x16x32_bf16 a[40:43], a[216:219], v[188:191], a[40:43]\n"
    "ds_read_b64_tr_b16 a[250:251], v19 offset:8192\n"
    "v_cvt_pk_bf16_f32 v59, v246, v247\n"
    "v_mfma_f32_16x16x32_bf16 a[36:39], a[216:219], v[184:187], a[36:39]\n"
    "ds_read_b64_tr_b16 a[252:253], v19 offset:16384\n"
    "v_add_f32 v31, v31, v247\n"
    "v_mfma_f32_16x16x32_bf16 a[36:39], a[220:223], v[196:199], a[36:39]\n"
    "s_waitcnt lgkmcnt(11)\n"
    "ds_read_b64_tr_b16 a[254:255], v19 offset:24576\n"
    "v_mfma_f32_16x16x32_bf16 a[48:51], a[224:227], v[184:187], a[48:51]\n"
    "s_sub_u32 s64, s64, 1\n"
    "v_exp_f32 v248, v156\n"
    "v_mfma_f32_16x16x32_bf16 a[48:51], a[228:231], v[196:199], a[48:51]\n"
    "s_cmp_lg_u32 s64, 0\n"
    "v_exp_f32 v249, v157\n"
    "v_mfma_f32_16x16x32_bf16 a[52:55], a[228:231], v[200:203], a[52:55]\n"
    "ds_read_b64_tr_b16 a[192:193], v12 offset:256\n"
    "v_add_f32 v32, v32, v248\n"
    "v_mfma_f32_16x16x32_bf16 a[52:55], a[224:227], v[188:191], a[52:55]\n"
    "ds_read_b64_tr_b16 a[194:195], v12 offset:8448\n"
    "v_cvt_pk_bf16_f32 v62, v248, v249\n"
    "v_mfma_f32_16x16x32_bf16 a[56:59], a[224:227], v[192:195], a[56:59]\n"
    "ds_read_b64_tr_b16 a[196:197], v12 offset:16640\n"
    "v_add_f32 v32, v32, v249\n"
    "v_mfma_f32_16x16x32_bf16 a[56:59], a[228:231], v[204:207], a[56:59]\n"
    "s_waitcnt lgkmcnt(11)\n"
    "ds_read_b64_tr_b16 a[198:199], v12 offset:24832\n"
    "v_mfma_f32_16x16x32_bf16 a[68:71], a[232:235], v[192:195], a[68:71]\n"
    "v_exp_f32 v250, v158\n"
    "v_mfma_f32_16x16x32_bf16 a[68:71], a[236:239], v[204:207], a[68:71]\n"
    "v_exp_f32 v251, v159\n"
    "v_mfma_f32_16x16x32_bf16 a[64:67], a[236:239], v[200:203], a[64:67]\n"
    "ds_read_b64_tr_b16 a[200:201], v13 offset:256\n"
    "v_add_f32 v32, v32, v250\n"
    "v_mfma_f32_16x16x32_bf16 a[64:67], a[232:235], v[188:191], a[64:67]\n"
    "ds_read_b64_tr_b16 a[202:203], v13 offset:8448\n"
    "v_cvt_pk_bf16_f32 v63, v250, v251\n"
    "v_mfma_f32_16x16x32_bf16 a[60:63], a[232:235], v[184:187], a[60:63]\n"
    "ds_read_b64_tr_b16 a[204:205], v13 offset:16640\n"
    "v_add_f32 v32, v32, v251\n"
    "v_mfma_f32_16x16x32_bf16 a[60:63], a[236:239], v[196:199], a[60:63]\n"
    "s_waitcnt lgkmcnt(11)\n"
    "ds_read_b64_tr_b16 a[206:207], v13 offset:24832\n"
    "v_max3_f32 v254, v30, v31, v32\n"
    "v_mfma_f32_16x16x32_bf16 a[72:75], a[240:243], v[184:187], a[72:75]\n"
    "v_cmp_lt_f32 vcc, s58, v254\n"
    "v_mfma_f32_16x16x32_bf16 a[72:75], a[244:247], v[196:199], a[72:75]\n"
    "v_mfma_f32_16x16x32_bf16 a[76:79], a[244:247], v[200:203], a[76:79]\n"
    "ds_read_b64_tr_b16 a[208:209], v14 offset:256\n"
    "v_mfma_f32_16x16x32_bf16 a[76:79], a[240:243], v[188:191], a[76:79]\n"
    "ds_read_b64_tr_b16 a[210:211], v14 offset:8448\n"
    "v_mfma_f32_16x16x32_bf16 a[80:83], a[240:243], v[192:195], a[80:83]\n"
    "ds_read_b64_tr_b16 a[212:213], v14 offset:16640\n"
    "v_mfma_f32_16x16x32_bf16 a[80:83], a[244:247], v[204:207], a[80:83]\n"
    "s_waitcnt lgkmcnt(11)\n"
    "ds_read_b64_tr_b16 a[214:215], v14 offset:24832\n"
    "v_mfma_f32_16x16x32_bf16 a[92:95], a[248:251], v[192:195], a[92:95]\n"
    "v_mfma_f32_16x16x32_bf16 a[92:95], a[252:255], v[204:207], a[92:95]\n"
    "v_mfma_f32_16x16x32_bf16 a[88:91], a[252:255], v[200:203], a[88:91]\n"
    "ds_read_b64_tr_b16 a[216:217], v15 offset:256\n"
    "v_mfma_f32_16x16x32_bf16 a[88:91], a[248:251], v[188:191], a[88:91]\n"
    "ds_read_b64_tr_b16 a[218:219], v15 offset:8448\n"
    "v_mfma_f32_16x16x32_bf16 a[84:87], a[248:251], v[184:187], a[84:87]\n"
    "ds_read_b64_tr_b16 a[220:221], v15 offset:16640\n"
    "v_mfma_f32_16x16x32_bf16 a[84:87], a[252:255], v[196:199], a[84:87]\n"
    "s_waitcnt lgkmcnt(11)\n"
    "ds_read_b64_tr_b16 a[222:223], v15 offset:24832\n"
    "v_mfma_f32_16x16x32_bf16 a[96:99], a[192:195], v[184:187], a[96:99]\n"
    "v_mfma_f32_16x16x32_bf16 a[96:99], a[196:199], v[196:199], a[96:99]\n"
    "v_mfma_f32_16x16x32_bf16 a[100:103], a[196:199], v[200:203], a[100:103]\n"
    "ds_read_b64_tr_b16 a[224:225], v16 offset:256\n"
    "v_mfma_f32_16x16x32_bf16 a[100:103], a[192:195], v[188:191], a[100:103]\n"
    "ds_read_b64_tr_b16 a[226:227], v16 offset:8448\n"
    "v_mfma_f32_16x16x32_bf16 a[104:107], a[192:195], v[192:195], a[104:107]\n"
    "ds_read_b64_tr_b16 a[228:229], v16 offset:16640\n"
    "v_mfma_f32_16x16x32_bf16 a[104:107], a[196:199], v[204:207], a[104:107]\n"
    "s_waitcnt lgkmcnt(11)\n"
    "ds_read_b64_tr_b16 a[230:231], v16 offset:24832\n"
    "v_mfma_f32_16x16x32_bf16 a[116:119], a[200:203], v[192:195], a[116:119]\n"
    "v_mfma_f32_16x16x32_bf16 a[116:119], a[204:207], v[204:207], a[116:119]\n"
    "v_mfma_f32_16x16x32_bf16 a[112:115], a[204:207], v[200:203], a[112:115]\n"
    "ds_read_b64_tr_b16 a[232:233], v17 offset:256\n"
    "v_mfma_f32_16x16x32_bf16 a[112:115], a[200:203], v[188:191], a[112:115]\n"
    "ds_read_b64_tr_b16 a[234:235], v17 offset:8448\n"
    "v_mfma_f32_16x16x32_bf16 a[108:111], a[200:203], v[184:187], a[108:111]\n"
    "ds_read_b64_tr_b16 a[236:237], v17 offset:16640\n"
    "v_mfma_f32_16x16x32_bf16 a[108:111], a[204:207], v[196:199], a[108:111]\n"
    "s_waitcnt lgkmcnt(11)\n"
    "ds_read_b64_tr_b16 a[238:239], v17 offset:24832\n"
    "v_mfma_f32_16x16x32_bf16 a[120:123], a[208:211], v[184:187], a[120:123]\n"
    "v_mfma_f32_16x16x32_bf16 a[120:123], a[212:215], v[196:199], a[120:123]\n"
    "v_mfma_f32_16x16x32_bf16 a[124:127], a[212:215], v[200:203], a[124:127]\n"
    "ds_read_b64_tr_b16 a[240:241], v18 offset:256\n"
    "v_mfma_f32_16x16x32_bf16 a[124:127], a[208:211], v[188:191], a[124:127]\n"
    "ds_read_b64_tr_b16 a[242:243], v18 offset:8448\n"
    "v_mfma_f32_16x16x32_bf16 a[128:131], a[208:211], v[192:195], a[128:131]\n"
    "ds_read_b64_tr_b16 a[244:245], v18 offset:16640\n"
    "v_mfma_f32_16x16x32_bf16 a[128:131], a[212:215], v[204:207], a[128:131]\n"
    "s_waitcnt lgkmcnt(11)\n"
    "ds_read_b64_tr_b16 a[246:247], v18 offset:24832\n"
    "v_mfma_f32_16x16x32_bf16 a[140:143], a[216:219], v[192:195], a[140:143]\n"
    "v_mfma_f32_16x16x32_bf16 a[140:143], a[220:223], v[204:207], a[140:143]\n"
    "v_mfma_f32_16x16x32_bf16 a[136:139], a[220:223], v[200:203], a[136:139]\n"
    "ds_read_b64_tr_b16 a[248:249], v19 offset:256\n"
    "v_mfma_f32_16x16x32_bf16 a[136:139], a[216:219], v[188:191], a[136:139]\n"
    "ds_read_b64_tr_b16 a[250:251], v19 offset:8448\n"
    "v_mfma_f32_16x16x32_bf16 a[132:135], a[216:219], v[184:187], a[132:135]\n"
    "ds_read_b64_tr_b16 a[252:253], v19 offset:16640\n"
    "v_mfma_f32_16x16x32_bf16 a[132:135], a[220:223], v[196:199], a[132:135]\n"
    "s_waitcnt lgkmcnt(11)\n"
    "ds_read_b64_tr_b16 a[254:255], v19 offset:24832\n"
    "v_mfma_f32_16x16x32_bf16 a[144:147], a[224:227], v[184:187], a[144:147]\n"
    "v_mfma_f32_16x16x32_bf16 a[144:147], a[228:231], v[196:199], a[144:147]\n"
    "v_mfma_f32_16x16x32_bf16 a[148:151], a[228:231], v[200:203], a[148:151]\n"
    "v_mfma_f32_16x16x32_bf16 a[148:151], a[224:227], v[188:191], a[148:151]\n"
    "v_mfma_f32_16x16x32_bf16 a[152:155], a[224:227], v[192:195], a[152:155]\n"
    "v_mfma_f32_16x16x32_bf16 a[152:155], a[228:231], v[204:207], a[152:155]\n"
    "s_waitcnt lgkmcnt(8)\n"
    "v_mfma_f32_16x16x32_bf16 a[164:167], a[232:235], v[192:195], a[164:167]\n"
    "v_mfma_f32_16x16x32_bf16 a[164:167], a[236:239], v[204:207], a[164:167]\n"
    "v_mfma_f32_16x16x32_bf16 a[160:163], a[236:239], v[200:203], a[160:163]\n"
    "v_mfma_f32_16x16x32_bf16 a[160:163], a[232:235], v[188:191], a[160:163]\n"
    "v_mfma_f32_16x16x32_bf16 a[156:159], a[232:235], v[184:187], a[156:159]\n"
    "v_mfma_f32_16x16x32_bf16 a[156:159], a[236:239], v[196:199], a[156:159]\n"
    "s_waitcnt lgkmcnt(0)\n"
    "s_barrier\n"
    "v_mfma_f32_16x16x32_bf16 a[168:171], a[240:243], v[184:187], a[168:171]\n"
    "ds_read_b128 a[192:195], v8 offset:0\n"
    "v_mfma_f32_16x16x32_bf16 a[168:171], a[244:247], v[196:199], a[168:171]\n"
    "ds_read_b128 a[196:199], v9 offset:0\n"
    "v_mfma_f32_16x16x32_bf16 a[172:175], a[244:247], v[200:203], a[172:175]\n"
    "ds_read_b128 a[200:203], v10 offset:0\n"
    "v_mfma_f32_16x16x32_bf16 a[172:175], a[240:243], v[188:191], a[172:175]\n"
    "ds_read_b128 a[204:207], v11 offset:0\n"
    "v_mfma_f32_16x16x32_bf16 a[176:179], a[240:243], v[192:195], a[176:179]\n"
    "ds_read_b128 a[208:211], v8 offset:256\n"
    "v_mfma_f32_16x16x32_bf16 a[176:179], a[244:247], v[204:207], a[176:179]\n"
    "ds_read_b128 a[212:215], v9 offset:256\n"
    "v_mfma_f32_16x16x32_bf16 a[188:191], a[248:251], v[192:195], a[188:191]\n"
    "ds_read_b128 a[216:219], v10 offset:256\n"
    "v_mfma_f32_16x16x32_bf16 a[188:191], a[252:255], v[204:207], a[188:191]\n"
    "ds_read_b128 a[220:223], v11 offset:256\n"
    "v_mfma_f32_16x16x32_bf16 a[184:187], a[252:255], v[200:203], a[184:187]\n"
    "v_mfma_f32_16x16x32_bf16 a[184:187], a[248:251], v[188:191], a[184:187]\n"
    "v_mfma_f32_16x16x32_bf16 a[180:183], a[248:251], v[184:187], a[180:183]\n"
    "v_mfma_f32_16x16x32_bf16 a[180:183], a[252:255], v[196:199], a[180:183]\n"
    "s_waitcnt lgkmcnt(6)\n"
    "s_cbranch_vccnz Lstub4_%=\n"
    "Lok4_%=:\n"
    "s_cbranch_scc1 Lattn_%=\n"
    "v_mfma_f32_16x16x32_bf16 v[88:91], a[192:195], v[64:67], v[232:235]\n"
    "s_waitcnt lgkmcnt(5)\n"
    "v_add_f32 v27, v27, v30\n"
    "v_add_f32 v28, v28, v31\n"
    "v_mfma_f32_16x16x32_bf16 v[88:91], a[196:199], v[76:79], v[88:91]\n"
    "v_add_f32 v29, v29, v32\n"
    "v_mov_b32 v38, 5376\n"
    "v_mfma_f32_16x16x32_bf16 v[88:91], a[200:203], v[112:115], v[88:91]\n"
    "s_waitcnt lgkmcnt(3)\n"
    "ds_read_b128 a[252:255], v11 offset:8448\n"
    "v_mfma_f32_16x16x32_bf16 v[88:91], a[204:207], v[124:127], v[88:91]\n"
    "ds_read_b128 a[248:251], v10 offset:8448\n"
    "v_mfma_f32_16x16x32_bf16 v[88:91], a[208:211], v[160:163], v[88:91]\n"
    "s_waitcnt lgkmcnt(3)\n"
    "ds_read_b128 a[244:247], v9 offset:8448\n"
    "v_mfma_f32_16x16x32_bf16 v[88:91], a[212:215], v[172:175], v[88:91]\n"
    "ds_read_b128 a[240:243], v8 offset:8448\n"
    "v_mfma_f32_16x16x32_bf16 v[88:91], a[216:219], v[208:211], v[88:91]\n"
    "s_waitcnt lgkmcnt(4)\n"
    "ds_read_b128 a[236:239], v11 offset:8192\n"
    "v_mfma_f32_16x16x32_bf16 v[88:91], a[220:223], v[220:223], v[88:91]\n"
    "buffer_load_dwordx2 v[38:39], v38, s[36:39], 0 offen sc1\n"
    "v_mfma_f32_16x16x32_bf16 v[92:95], a[220:223], v[224:227], v[236:239]\n"
    "ds_read_b128 a[232:235], v10 offset:8192\n"
    "v_mfma_f32_16x16x32_bf16 v[92:95], a[216:219], v[212:215], v[92:95]\n"
    "ds_read_b128 a[228:231], v9 offset:8192\n"
    "v_mfma_f32_16x16x32_bf16 v[92:95], a[212:215], v[176:179], v[92:95]\n"
    "ds_read_b128 a[224:227], v8 offset:8192\n"
    "v_mfma_f32_16x16x32_bf16 v[92:95], a[208:211], v[164:167], v[92:95]\n"
    "v_exp_f32 v244, v88\n"
    "v_mfma_f32_16x16x32_bf16 v[92:95], a[204:207], v[128:131], v[92:95]\n"
    "v_exp_f32 v245, v89\n"
    "v_mfma_f32_16x16x32_bf16 v[92:95], a[200:203], v[116:119], v[92:95]\n"
    "v_exp_f32 v246, v90\n"
    "v_mfma_f32_16x16x32_bf16 v[92:95], a[196:199], v[80:83], v[92:95]\n"
    "v_exp_f32 v247, v91\n"
    "v_mfma_f32_16x16x32_bf16 v[92:95], a[192:195], v[68:71], v[92:95]\n"
    "v_add_f32 v30, v244, v245\n"
    "v_cvt_pk_bf16_f32 v184, v244, v245\n"
    "v_mfma_f32_16x16x32_bf16 v[96:99], a[192:195], v[72:75], v[240:243]\n"
    "v_add_f32 v30, v30, v246\n"
    "v_cvt_pk_bf16_f32 v185, v246, v247\n"
    "v_mfma_f32_16x16x32_bf16 v[96:99], a[196:199], v[84:87], v[96:99]\n"
    "v_add_f32 v30, v30, v247\n"
    "v_mfma_f32_16x16x32_bf16 v[96:99], a[200:203], v[120:123], v[96:99]\n"
    "v_exp_f32 v248, v92\n"
    "v_mfma_f32_16x16x32_bf16 v[96:99], a[204:207], v[132:135], v[96:99]\n"
    "v_exp_f32 v249, v93\n"
    "v_mfma_f32_16x16x32_bf16 v[96:99], a[208:211], v[168:171], v[96:99]\n"
    "v_exp_f32 v250, v94\n"
    "v_mfma_f32_16x16x32_bf16 v[96:99], a[212:215], v[180:183], v[96:99]\n"
    "v_exp_f32 v251, v95\n"
    "v_mfma_f32_16x16x32_bf16 v[96:99], a[216:219], v[216:219], v[96:99]\n"
    "v_add_f32 v31, v248, v249\n"
    "v_cvt_pk_bf16_f32 v188, v248, v249\n"
    "v_mfma_f32_16x16x32_bf16 v[96:99], a[220:223], v[228:231], v[96:99]\n"
    "s_waitcnt lgkmcnt(6)\n"
    "v_add_f32 v31, v31, v250\n"
    "v_cvt_pk_bf16_f32 v189, v250, v251\n"
    "v_mfma_f32_16x16x32_bf16 v[100:103], a[252:255], v[220:223], v[232:235]\n"
    "v_add_f32 v31, v31, v251\n"
    "v_mfma_f32_16x16x32_bf16 v[100:103], a[248:251], v[208:211], v[100:103]\n"
    "s_waitcnt lgkmcnt(4)\n"
    "v_mfma_f32_16x16x32_bf16 v[100:103], a[244:247], v[172:175], v[100:103]\n"
    "ds_read_b128 a[192:195], v8 offset:16384\n"
    "v_mfma_f32_16x16x32_bf16 v[100:103], a[240:243], v[160:163], v[100:103]\n"
    "s_waitcnt lgkmcnt(3)\n"
    "ds_read_b128 a[196:199], v9 offset:16384\n"
    "v_mfma_f32_16x16x32_bf16 v[100:103], a[236:239], v[124:127], v[100:103]\n"
    "ds_read_b128 a[200:203], v10 offset:16384\n"
    "v_mfma_f32_16x16x32_bf16 v[100:103], a[232:235], v[112:115], v[100:103]\n"
    "s_waitcnt lgkmcnt(3)\n"
    "ds_read_b128 a[204:207], v11 offset:16384\n"
    "v_mfma_f32_16x16x32_bf16 v[100:103], a[228:231], v[76:79], v[100:103]\n"
    "ds_read_b128 a[208:211], v8 offset:16640\n"
    "v_mfma_f32_16x16x32_bf16 v[100:103], a[224:227], v[64:67], v[100:103]\n"
    "ds_read_b128 a[212:215], v9 offset:16640\n"
    "v_mfma_f32_16x16x32_bf16 v[104:107], a[224:227], v[68:71], v[236:239]\n"
    "ds_read_b128 a[216:219], v10 offset:16640\n"
    "v_mfma_f32_16x16x32_bf16 v[104:107], a[228:231], v[80:83], v[104:107]\n"
    "ds_read_b128 a[220:223], v11 offset:16640\n"
    "v_mfma_f32_16x16x32_bf16 v[104:107], a[232:235], v[116:119], v[104:107]\n"
    "v_exp_f32 v252, v96\n"
    "v_mfma_f32_16x16x32_bf16 v[104:107], a[236:239], v[128:131], v[104:107]\n"
    "v_exp_f32 v253, v97\n"
    "v_mfma_f32_16x16x32_bf16 v[104:107], a[240:243], v[164:167], v[104:107]\n"
    "v_exp_f32 v244, v98\n"
    "v_mfma_f32_16x16x32_bf16 v[104:107], a[244:247], v[176:179], v[104:107]\n"
    "v_exp_f32 v245, v99\n"
    "v_mfma_f32_16x16x32_bf16 v[104:107], a[248:251], v[212:215], v[104:107]\n"
    "v_exp_f32 v246, v100\n"
    "v_mfma_f32_16x16x32_bf16 v[104:107], a[252:255], v[224:227], v[104:107]\n"
    "v_exp_f32 v247, v101\n"
    "v_mfma_f32_16x16x32_bf16 v[108:111], a[252:255], v[228:231], v[240:243]\n"
    "v_exp_f32 v248, v102\n"
    "v_mfma_f32_16x16x32_bf16 v[108:111], a[248:251], v[216:219], v[108:111]\n"
    "v_exp_f32 v249, v103\n"
    "v_mfma_f32_16x16x32_bf16 v[108:111], a[244:247], v[180:183], v[108:111]\n"
    "v_add_f32 v32, v252, v253\n"
    "v_cvt_pk_bf16_f32 v192, v252, v253\n"
    "v_mfma_f32_16x16x32_bf16 v[108:111], a[240:243], v[168:171], v[108:111]\n"
    "v_exp_f32 v250, v104\n"
    "v_mfma_f32_16x16x32_bf16 v[108:111], a[236:239], v[132:135], v[108:111]\n"
    "v_exp_f32 v251, v105\n"
    "v_mfma_f32_16x16x32_bf16 v[108:111], a[232:235], v[120:123], v[108:111]\n"
    "v_exp_f32 v252, v106\n"
    "v_mfma_f32_16x16x32_bf16 v[108:111], a[228:231], v[84:87], v[108:111]\n"
    "v_exp_f32 v253, v107\n"
    "v_mfma_f32_16x16x32_bf16 v[108:111], a[224:227], v[72:75], v[108:111]\n"
    "s_waitcnt lgkmcnt(6)\n"
    "v_add_f32 v32, v32, v244\n"
    "v_cvt_pk_bf16_f32 v193, v244, v245\n"
    "v_mfma_f32_16x16x32_bf16 v[136:139], a[192:195], v[64:67], v[232:235]\n"
    "v_add_f32 v32, v32, v245\n"
    "v_add_f32 v30, v30, v246\n"
    "v_mfma_f32_16x16x32_bf16 v[136:139], a[196:199], v[76:79], v[136:139]\n"
    "s_waitcnt lgkmcnt(4)\n"
    "v_cvt_pk_bf16_f32 v186, v246, v247\n"
    "v_add_f32 v30, v30, v247\n"
    "v_mfma_f32_16x16x32_bf16 v[136:139], a[200:203], v[112:115], v[136:139]\n"
    "ds_read_b128 a[252:255], v11 offset:24832\n"
    "v_add_f32 v30, v30, v248\n"
    "v_mfma_f32_16x16x32_bf16 v[136:139], a[204:207], v[124:127], v[136:139]\n"
    "s_waitcnt lgkmcnt(3)\n"
    "ds_read_b128 a[248:251], v10 offset:24832\n"
    "v_cvt_pk_bf16_f32 v187, v248, v249\n"
    "v_mfma_f32_16x16x32_bf16 v[136:139], a[208:211], v[160:163], v[136:139]\n"
    "ds_read_b128 a[244:247], v9 offset:24832\n"
    "v_add_f32 v30, v30, v249\n"
    "v_mfma_f32_16x16x32_bf16 v[136:139], a[212:215], v[172:175], v[136:139]\n"
    "s_waitcnt lgkmcnt(3)\n"
    "ds_read_b128 a[240:243], v8 offset:24832\n"
    "v_add_f32 v31, v31, v250\n"
    "v_mfma_f32_16x16x32_bf16 v[136:139], a[216:219], v[208:211], v[136:139]\n"
    "ds_read_b128 a[236:239], v11 offset:24576\n"
    "v_cvt_pk_bf16_f32 v190, v250, v251\n"
    "v_mfma_f32_16x16x32_bf16 v[136:139], a[220:223], v[220:223], v[136:139]\n"
    "ds_read_b128 a[232:235], v10 offset:24576\n"
    "v_add_f32 v31, v31, v251\n"
    "v_mfma_f32_16x16x32_bf16 v[140:143], a[220:223], v[224:227], v[236:239]\n"
    "ds_read_b128 a[228:231], v9 offset:24576\n"
    "v_add_f32 v31, v31, v252\n"
    "v_mfma_f32_16x16x32_bf16 v[140:143], a[216:219], v[212:215], v[140:143]\n"
    "ds_read_b128 a[224:227], v8 offset:24576\n"
    "v_cvt_pk_bf16_f32 v191, v252, v253\n"
    "v_mfma_f32_16x16x32_bf16 v[140:143], a[212:215], v[176:179], v[140:143]\n"
    "v_exp_f32 v244, v108\n"
    "v_mfma_f32_16x16x32_bf16 v[140:143], a[208:211], v[164:167], v[140:143]\n"
    "v_exp_f32 v245, v109\n"
    "v_mfma_f32_16x16x32_bf16 v[140:143], a[204:207], v[128:131], v[140:143]\n"
    "v_exp_f32 v246, v110\n"
    "v_mfma_f32_16x16x32_bf16 v[140:143], a[200:203], v[116:119], v[140:143]\n"
    "v_exp_f32 v247, v111\n"
    "v_mfma_f32_16x16x32_bf16 v[140:143], a[196:199], v[80:83], v[140:143]\n"
    "v_exp_f32 v248, v136\n"
    "v_mfma_f32_16x16x32_bf16 v[140:143], a[192:195], v[68:71], v[140:143]\n"
    "v_exp_f32 v249, v137\n"
    "v_mfma_f32_16x16x32_bf16 v[144:147], a[192:195], v[72:75], v[240:243]\n"
    "v_exp_f32 v250, v138\n"
    "v_mfma_f32_16x16x32_bf16 v[144:147], a[196:199], v[84:87], v[144:147]\n"
    "v_exp_f32 v251, v139\n"
    "v_mfma_f32_16x16x32_bf16 v[144:147], a[200:203], v[120:123], v[144:147]\n"
    "v_add_f32 v31, v31, v253\n"
    "v_add_f32 v32, v32, v244\n"
    "v_mfma_f32_16x16x32_bf16 v[144:147], a[204:207], v[132:135], v[144:147]\n"
    "v_exp_f32 v252, v140\n"
    "v_mfma_f32_16x16x32_bf16 v[144:147], a[208:211], v[168:171], v[144:147]\n"
    "v_exp_f32 v253, v141\n"
    "v_mfma_f32_16x16x32_bf16 v[144:147], a[212:215], v[180:183], v[144:147]\n"
    "v_cvt_pk_bf16_f32 v194, v244, v245\n"
    "v_add_f32 v32, v32, v245\n"
    "v_mfma_f32_16x16x32_bf16 v[144:147], a[216:219], v[216:219], v[144:147]\n"
    "v_exp_f32 v244, v142\n"
    "v_mfma_f32_16x16x32_bf16 v[144:147], a[220:223], v[228:231], v[144:147]\n"
    "s_waitcnt lgkmcnt(6)\n"
    "v_exp_f32 v245, v143\n"
    "v_mfma_f32_16x16x32_bf16 v[148:151], a[252:255], v[220:223], v[232:235]\n"
    "v_add_f32 v32, v32, v246\n"
    "v_cvt_pk_bf16_f32 v195, v246, v247\n"
    "v_mfma_f32_16x16x32_bf16 v[148:151], a[248:251], v[208:211], v[148:151]\n"
    "s_waitcnt lgkmcnt(4)\n"
    "v_add_f32 v32, v32, v247\n"
    "v_add_f32 v30, v30, v248\n"
    "v_mfma_f32_16x16x32_bf16 v[148:151], a[244:247], v[172:175], v[148:151]\n"
    "v_exp_f32 v246, v144\n"
    "v_mfma_f32_16x16x32_bf16 v[148:151], a[240:243], v[160:163], v[148:151]\n"
    "s_waitcnt lgkmcnt(2)\n"
    "v_exp_f32 v247, v145\n"
    "v_mfma_f32_16x16x32_bf16 v[148:151], a[236:239], v[124:127], v[148:151]\n"
    "v_cvt_pk_bf16_f32 v196, v248, v249\n"
    "v_add_f32 v30, v30, v249\n"
    "v_mfma_f32_16x16x32_bf16 v[148:151], a[232:235], v[112:115], v[148:151]\n"
    "s_waitcnt lgkmcnt(0)\n"
    "v_exp_f32 v248, v146\n"
    "v_mfma_f32_16x16x32_bf16 v[148:151], a[228:231], v[76:79], v[148:151]\n"
    "v_exp_f32 v249, v147\n"
    "v_mfma_f32_16x16x32_bf16 v[148:151], a[224:227], v[64:67], v[148:151]\n"
    "s_waitcnt lgkmcnt(0)\n"
    "s_barrier\n"
    "v_add_f32 v30, v30, v250\n"
    "v_mfma_f32_16x16x32_bf16 v[152:155], a[224:227], v[68:71], v[236:239]\n"
    "ds_read_b64_tr_b16 a[192:193], v12 offset:32768\n"
    "v_cvt_pk_bf16_f32 v197, v250, v251\n"
    "v_mfma_f32_16x16x32_bf16 v[152:155], a[228:231], v[80:83], v[152:155]\n"
    "ds_read_b64_tr_b16 a[194:195], v12 offset:40960\n"
    "v_add_f32 v30, v30, v251\n"
    "v_mfma_f32_16x16x32_bf16 v[152:155], a[232:235], v[116:119], v[152:155]\n"
    "ds_read_b64_tr_b16 a[196:197], v12 offset:49152\n"
    "v_add_f32 v31, v31, v252\n"
    "v_mfma_f32_16x16x32_bf16 v[152:155], a[236:239], v[128:131], v[152:155]\n"
    "ds_read_b64_tr_b16 a[198:199], v12 offset:57344\n"
    "v_cvt_pk_bf16_f32 v200, v252, v253\n"
    "v_mfma_f32_16x16x32_bf16 v[152:155], a[240:243], v[164:167], v[152:155]\n"
    "ds_read_b64_tr_b16 a[200:201], v13 offset:32768\n"
    "v_add_f32 v31, v31, v253\n"
    "v_mfma_f32_16x16x32_bf16 v[152:155], a[244:247], v[176:179], v[152:155]\n"
    "ds_read_b64_tr_b16 a[202:203], v13 offset:40960\n"
    "v_add_f32 v31, v31, v244\n"
    "v_mfma_f32_16x16x32_bf16 v[152:155], a[248:251], v[212:215], v[152:155]\n"
    "ds_read_b64_tr_b16 a[204:205], v13 offset:49152\n"
    "v_cvt_pk_bf16_f32 v201, v244, v245\n"
    "v_mfma_f32_16x16x32_bf16 v[152:155], a[252:255], v[224:227], v[152:155]\n"
    "ds_read_b64_tr_b16 a[206:207], v13 offset:57344\n"
    "v_add_f32 v31, v31, v245\n"
    "v_mfma_f32_16x16x32_bf16 v[156:159], a[252:255], v[228:231], v[240:243]\n"
    "ds_read_b64_tr_b16 a[208:209], v14 offset:32768\n"
    "v_add_f32 v32, v32, v246\n"
    "v_mfma_f32_16x16x32_bf16 v[156:159], a[248:251], v[216:219], v[156:159]\n"
    "ds_read_b64_tr_b16 a[210:211], v14 offset:40960\n"
    "v_cvt_pk_bf16_f32 v204, v246, v247\n"
    "v_mfma_f32_16x16x32_bf16 v[156:159], a[244:247], v[180:183], v[156:159]\n"
    "ds_read_b64_tr_b16 a[212:213], v14 offset:49152\n"
    "v_add_f32 v32, v32, v247\n"
    "v_mfma_f32_16x16x32_bf16 v[156:159], a[240:243], v[168:171], v[156:159]\n"
    "ds_read_b64_tr_b16 a[214:215], v14 offset:57344\n"
    "v_add_f32 v32, v32, v248\n"
    "v_mfma_f32_16x16x32_bf16 v[156:159], a[236:239], v[132:135], v[156:159]\n"
    "ds_read_b64_tr_b16 a[216:217], v15 offset:32768\n"
    "v_cvt_pk_bf16_f32 v205, v248, v249\n"
    "v_mfma_f32_16x16x32_bf16 v[156:159], a[232:235], v[120:123], v[156:159]\n"
    "ds_read_b64_tr_b16 a[218:219], v15 offset:40960\n"
    "v_add_f32 v32, v32, v249\n"
    "v_mfma_f32_16x16x32_bf16 v[156:159], a[228:231], v[84:87], v[156:159]\n"
    "ds_read_b64_tr_b16 a[220:221], v15 offset:49152\n"
    "v_mfma_f32_16x16x32_bf16 v[156:159], a[224:227], v[72:75], v[156:159]\n"
    "s_waitcnt lgkmcnt(11)\n"
    "ds_read_b64_tr_b16 a[222:223], v15 offset:57344\n"
    "v_mfma_f32_16x16x32_bf16 a[0:3], a[192:195], v[40:43], a[0:3]\n"
    "v_exp_f32 v250, v148\n"
    "v_mfma_f32_16x16x32_bf16 a[0:3], a[196:199], v[52:55], a[0:3]\n"
    "v_exp_f32 v251, v149\n"
    "v_mfma_f32_16x16x32_bf16 a[4:7], a[196:199], v[56:59], a[4:7]\n"
    "ds_read_b64_tr_b16 a[224:225], v16 offset:32768\n"
    "v_add_f32 v30, v30, v250\n"
    "v_mfma_f32_16x16x32_bf16 a[4:7], a[192:195], v[44:47], a[4:7]\n"
    "ds_read_b64_tr_b16 a[226:227], v16 offset:40960\n"
    "v_cvt_pk_bf16_f32 v198, v250, v251\n"
    "v_mfma_f32_16x16x32_bf16 a[8:11], a[192:195], v[48:51], a[8:11]\n"
    "ds_read_b64_tr_b16 a[228:229], v16 offset:49152\n"
    "v_add_f32 v30, v30, v251\n"
    "v_mfma_f32_16x16x32_bf16 a[8:11], a[196:199], v[60:63], a[8:11]\n"
    "s_waitcnt lgkmcnt(11)\n"
    "ds_read_b64_tr_b16 a[230:231], v16 offset:57344\n"
    "v_mfma_f32_16x16x32_bf16 a[20:23], a[200:203], v[48:51], a[20:23]\n"
    "v_exp_f32 v252, v150\n"
    "v_mfma_f32_16x16x32_bf16 a[20:23], a[204:207], v[60:63], a[20:23]\n"
    "v_exp_f32 v253, v151\n"
    "v_mfma_f32_16x16x32_bf16 a[16:19], a[204:207], v[56:59], a[16:19]\n"
    "ds_read_b64_tr_b16 a[232:233], v17 offset:32768\n"
    "v_add_f32 v30, v30, v252\n"
    "v_mfma_f32_16x16x32_bf16 a[16:19], a[200:203], v[44:47], a[16:19]\n"
    "ds_read_b64_tr_b16 a[234:235], v17 offset:40960\n"
    "v_cvt_pk_bf16_f32 v199, v252, v253\n"
    "v_mfma_f32_16x16x32_bf16 a[12:15], a[200:203], v[40:43], a[12:15]\n"
    "ds_read_b64_tr_b16 a[236:237], v17 offset:49152\n"
    "v_add_f32 v30, v30, v253\n"
    "v_mfma_f32_16x16x32_bf16 a[12:15], a[204:207], v[52:55], a[12:15]\n"
    "s_waitcnt lgkmcnt(11)\n"
    "ds_read_b64_tr_b16 a[238:239], v17 offset:57344\n"
    "v_mfma_f32_16x16x32_bf16 a[24:27], a[208:211], v[40:43], a[24:27]\n"
    "v_exp_f32 v244, v152\n"
    "v_mfma_f32_16x16x32_bf16 a[24:27], a[212:215], v[52:55], a[24:27]\n"
    "v_exp_f32 v245, v153\n"
    "v_mfma_f32_16x16x32_bf16 a[28:31], a[212:215], v[56:59], a[28:31]\n"
    "ds_read_b64_tr_b16 a[240:241], v18 offset:32768\n"
    "v_add_f32 v31, v31, v244\n"
    "v_mfma_f32_16x16x32_bf16 a[28:31], a[208:211], v[44:47], a[28:31]\n"
    "ds_read_b64_tr_b16 a[242:243], v18 offset:40960\n"
    "v_cvt_pk_bf16_f32 v202, v244, v245\n"
    "v_mfma_f32_16x16x32_bf16 a[32:35], a[208:211], v[48:51], a[32:35]\n"
    "ds_read_b64_tr_b16 a[244:245], v18 offset:49152\n"
    "v_add_f32 v31, v31, v245\n"
    "v_mfma_f32_16x16x32_bf16 a[32:35], a[212:215], v[60:63], a[32:35]\n"
    "s_waitcnt lgkmcnt(11)\n"
    "ds_read_b64_tr_b16 a[246:247], v18 offset:57344\n"
    "v_mfma_f32_16x16x32_bf16 a[44:47], a[216:219], v[48:51], a[44:47]\n"
    "v_exp_f32 v246, v154\n"
    "v_mfma_f32_16x16x32_bf16 a[44:47], a[220:223], v[60:63], a[44:47]\n"
    "v_exp_f32 v247, v155\n"
    "v_mfma_f32_16x16x32_bf16 a[40:43], a[220:223], v[56:59], a[40:43]\n"
    "ds_read_b64_tr_b16 a[248:249], v19 offset:32768\n"
    "v_add_f32 v31, v31, v246\n"
    "v_mfma_f32_16x16x32_bf16 a[40:43], a[216:219], v[44:47], a[40:43]\n"
    "ds_read_b64_tr_b16 a[250:251], v19 offset:40960\n"
    "v_cvt_pk_bf16_f32 v203, v246, v247\n"
    "v_mfma_f32_16x16x32_bf16 a[36:39], a[216:219], v[40:43], a[36:39]\n"
    "ds_read_b64_tr_b16 a[252:253], v19 offset:49152\n"
    "v_add_f32 v31, v31, v247\n"
    "v_mfma_f32_16x16x32_bf16 a[36:39], a[220:223], v[52:55], a[36:39]\n"
    "s_waitcnt lgkmcnt(11)\n"
    "ds_read_b64_tr_b16 a[254:255], v19 offset:57344\n"
    "v_mfma_f32_16x16x32_bf16 a[48:51], a[224:227], v[40:43], a[48:51]\n"
    "v_exp_f32 v248, v156\n"
    "v_mfma_f32_16x16x32_bf16 a[48:51], a[228:231], v[52:55], a[48:51]\n"
    "v_exp_f32 v249, v157\n"
    "v_mfma_f32_16x16x32_bf16 a[52:55], a[228:231], v[56:59], a[52:55]\n"
    "ds_read_b64_tr_b16 a[192:193], v12 offset:33024\n"
    "v_add_f32 v32, v32, v248\n"
    "v_mfma_f32_16x16x32_bf16 a[52:55], a[224:227], v[44:47], a[52:55]\n"
    "ds_read_b64_tr_b16 a[194:195], v12 offset:41216\n"
    "v_cvt_pk_bf16_f32 v206, v248, v249\n"
    "v_mfma_f32_16x16x32_bf16 a[56:59], a[224:227], v[48:51], a[56:59]\n"
    "ds_read_b64_tr_b16 a[196:197], v12 offset:49408\n"
    "v_add_f32 v32, v32, v249\n"
    "v_mfma_f32_16x16x32_bf16 a[56:59], a[228:231], v[60:63], a[56:59]\n"
    "s_waitcnt lgkmcnt(11)\n"
    "ds_read_b64_tr_b16 a[198:199], v12 offset:57600\n"
    "v_mfma_f32_16x16x32_bf16 a[68:71], a[232:235], v[48:51], a[68:71]\n"
    "v_exp_f32 v250, v158\n"
    "v_mfma_f32_16x16x32_bf16 a[68:71], a[236:239], v[60:63], a[68:71]\n"
    "v_exp_f32 v251, v159\n"
    "v_mfma_f32_16x16x32_bf16 a[64:67], a[236:239], v[56:59], a[64:67]\n"
    "ds_read_b64_tr_b16 a[200:201], v13 offset:33024\n"
    "v_add_f32 v32, v32, v250\n"
    "v_mfma_f32_16x16x32_bf16 a[64:67], a[232:235], v[44:47], a[64:67]\n"
    "ds_read_b64_tr_b16 a[202:203], v13 offset:41216\n"
    "v_cvt_pk_bf16_f32 v207, v250, v251\n"
    "v_mfma_f32_16x16x32_bf16 a[60:63], a[232:235], v[40:43], a[60:63]\n"
    "ds_read_b64_tr_b16 a[204:205], v13 offset:49408\n"
    "v_add_f32 v32, v32, v251\n"
    "v_mfma_f32_16x16x32_bf16 a[60:63], a[236:239], v[52:55], a[60:63]\n"
    "s_waitcnt lgkmcnt(11)\n"
    "ds_read_b64_tr_b16 a[206:207], v13 offset:57600\n"
    "v_max3_f32 v254, v30, v31, v32\n"
    "v_mfma_f32_16x16x32_bf16 a[72:75], a[240:243], v[40:43], a[72:75]\n"
    "v_cmp_lt_f32 vcc, s58, v254\n"
    "v_mfma_f32_16x16x32_bf16 a[72:75], a[244:247], v[52:55], a[72:75]\n"
    "v_mfma_f32_16x16x32_bf16 a[76:79], a[244:247], v[56:59], a[76:79]\n"
    "ds_read_b64_tr_b16 a[208:209], v14 offset:33024\n"
    "v_mfma_f32_16x16x32_bf16 a[76:79], a[240:243], v[44:47], a[76:79]\n"
    "ds_read_b64_tr_b16 a[210:211], v14 offset:41216\n"
    "v_mfma_f32_16x16x32_bf16 a[80:83], a[240:243], v[48:51], a[80:83]\n"
    "ds_read_b64_tr_b16 a[212:213], v14 offset:49408\n"
    "v_mfma_f32_16x16x32_bf16 a[80:83], a[244:247], v[60:63], a[80:83]\n"
    "s_waitcnt lgkmcnt(11)\n"
    "ds_read_b64_tr_b16 a[214:215], v14 offset:57600\n"
    "v_mfma_f32_16x16x32_bf16 a[92:95], a[248:251], v[48:51], a[92:95]\n"
    "v_mfma_f32_16x16x32_bf16 a[92:95], a[252:255], v[60:63], a[92:95]\n"
    "v_mfma_f32_16x16x32_bf16 a[88:91], a[252:255], v[56:59], a[88:91]\n"
    "ds_read_b64_tr_b16 a[216:217], v15 offset:33024\n"
    "v_mfma_f32_16x16x32_bf16 a[88:91], a[248:251], v[44:47], a[88:91]\n"
    "ds_read_b64_tr_b16 a[218:219], v15 offset:41216\n"
    "v_mfma_f32_16x16x32_bf16 a[84:87], a[248:251], v[40:43], a[84:87]\n"
    "ds_read_b64_tr_b16 a[220:221], v15 offset:49408\n"
    "v_mfma_f32_16x16x32_bf16 a[84:87], a[252:255], v[52:55], a[84:87]\n"
    "s_waitcnt lgkmcnt(11)\n"
    "ds_read_b64_tr_b16 a[222:223], v15 offset:57600\n"
    "v_mfma_f32_16x16x32_bf16 a[96:99], a[192:195], v[40:43], a[96:99]\n"
    "v_mfma_f32_16x16x32_bf16 a[96:99], a[196:199], v[52:55], a[96:99]\n"
    "v_mfma_f32_16x16x32_bf16 a[100:103], a[196:199], v[56:59], a[100:103]\n"
    "ds_read_b64_tr_b16 a[224:225], v16 offset:33024\n"
    "v_mfma_f32_16x16x32_bf16 a[100:103], a[192:195], v[44:47], a[100:103]\n"
    "ds_read_b64_tr_b16 a[226:227], v16 offset:41216\n"
    "v_mfma_f32_16x16x32_bf16 a[104:107], a[192:195], v[48:51], a[104:107]\n"
    "ds_read_b64_tr_b16 a[228:229], v16 offset:49408\n"
    "v_mfma_f32_16x16x32_bf16 a[104:107], a[196:199], v[60:63], a[104:107]\n"
    "s_waitcnt lgkmcnt(11)\n"
    "ds_read_b64_tr_b16 a[230:231], v16 offset:57600\n"
    "v_mfma_f32_16x16x32_bf16 a[116:119], a[200:203], v[48:51], a[116:119]\n"
    "v_mfma_f32_16x16x32_bf16 a[116:119], a[204:207], v[60:63], a[116:119]\n"
    "v_mfma_f32_16x16x32_bf16 a[112:115], a[204:207], v[56:59], a[112:115]\n"
    "ds_read_b64_tr_b16 a[232:233], v17 offset:33024\n"
    "v_mfma_f32_16x16x32_bf16 a[112:115], a[200:203], v[44:47], a[112:115]\n"
    "ds_read_b64_tr_b16 a[234:235], v17 offset:41216\n"
    "v_mfma_f32_16x16x32_bf16 a[108:111], a[200:203], v[40:43], a[108:111]\n"
    "ds_read_b64_tr_b16 a[236:237], v17 offset:49408\n"
    "v_mfma_f32_16x16x32_bf16 a[108:111], a[204:207], v[52:55], a[108:111]\n"
    "s_waitcnt lgkmcnt(11)\n"
    "ds_read_b64_tr_b16 a[238:239], v17 offset:57600\n"
    "v_mfma_f32_16x16x32_bf16 a[120:123], a[208:211], v[40:43], a[120:123]\n"
    "v_mfma_f32_16x16x32_bf16 a[120:123], a[212:215], v[52:55], a[120:123]\n"
    "v_mfma_f32_16x16x32_bf16 a[124:127], a[212:215], v[56:59], a[124:127]\n"
    "ds_read_b64_tr_b16 a[240:241], v18 offset:33024\n"
    "v_mfma_f32_16x16x32_bf16 a[124:127], a[208:211], v[44:47], a[124:127]\n"
    "ds_read_b64_tr_b16 a[242:243], v18 offset:41216\n"
    "v_mfma_f32_16x16x32_bf16 a[128:131], a[208:211], v[48:51], a[128:131]\n"
    "ds_read_b64_tr_b16 a[244:245], v18 offset:49408\n"
    "v_mfma_f32_16x16x32_bf16 a[128:131], a[212:215], v[60:63], a[128:131]\n"
    "s_waitcnt lgkmcnt(11)\n"
    "ds_read_b64_tr_b16 a[246:247], v18 offset:57600\n"
    "v_mfma_f32_16x16x32_bf16 a[140:143], a[216:219], v[48:51], a[140:143]\n"
    "v_mfma_f32_16x16x32_bf16 a[140:143], a[220:223], v[60:63], a[140:143]\n"
    "v_mfma_f32_16x16x32_bf16 a[136:139], a[220:223], v[56:59], a[136:139]\n"
    "ds_read_b64_tr_b16 a[248:249], v19 offset:33024\n"
    "v_mfma_f32_16x16x32_bf16 a[136:139], a[216:219], v[44:47], a[136:139]\n"
    "ds_read_b64_tr_b16 a[250:251], v19 offset:41216\n"
    "v_mfma_f32_16x16x32_bf16 a[132:135], a[216:219], v[40:43], a[132:135]\n"
    "ds_read_b64_tr_b16 a[252:253], v19 offset:49408\n"
    "v_mfma_f32_16x16x32_bf16 a[132:135], a[220:223], v[52:55], a[132:135]\n"
    "s_waitcnt lgkmcnt(11)\n"
    "ds_read_b64_tr_b16 a[254:255], v19 offset:57600\n"
    "v_mfma_f32_16x16x32_bf16 a[144:147], a[224:227], v[40:43], a[144:147]\n"
    "v_mfma_f32_16x16x32_bf16 a[144:147], a[228:231], v[52:55], a[144:147]\n"
    "v_mfma_f32_16x16x32_bf16 a[148:151], a[228:231], v[56:59], a[148:151]\n"
    "v_mfma_f32_16x16x32_bf16 a[148:151], a[224:227], v[44:47], a[148:151]\n"
    "v_mfma_f32_16x16x32_bf16 a[152:155], a[224:227], v[48:51], a[152:155]\n"
    "v_mfma_f32_16x16x32_bf16 a[152:155], a[228:231], v[60:63], a[152:155]\n"
    "s_waitcnt lgkmcnt(8)\n"
    "v_mfma_f32_16x16x32_bf16 a[164:167], a[232:235], v[48:51], a[164:167]\n"
    "v_mfma_f32_16x16x32_bf16 a[164:167], a[236:239], v[60:63], a[164:167]\n"
    "v_mfma_f32_16x16x32_bf16 a[160:163], a[236:239], v[56:59], a[160:163]\n"
    "v_mfma_f32_16x16x32_bf16 a[160:163], a[232:235], v[44:47], a[160:163]\n"
    "v_mfma_f32_16x16x32_bf16 a[156:159], a[232:235], v[40:43], a[156:159]\n"
    "v_mfma_f32_16x16x32_bf16 a[156:159], a[236:239], v[52:55], a[156:159]\n"
    "s_waitcnt lgkmcnt(0)\n"
    "s_barrier\n"
    "v_mfma_f32_16x16x32_bf16 a[168:171], a[240:243], v[40:43], a[168:171]\n"
    "ds_read_b128 a[192:195], v8 offset:32768\n"
    "v_mfma_f32_16x16x32_bf16 a[168:171], a[244:247], v[52:55], a[168:171]\n"
    "ds_read_b128 a[196:199], v9 offset:32768\n"
    "v_mfma_f32_16x16x32_bf16 a[172:175], a[244:247], v[56:59], a[172:175]\n"
    "ds_read_b128 a[200:203], v10 offset:32768\n"
    "v_mfma_f32_16x16x32_bf16 a[172:175], a[240:243], v[44:47], a[172:175]\n"
    "ds_read_b128 a[204:207], v11 offset:32768\n"
    "v_mfma_f32_16x16x32_bf16 a[176:179], a[240:243], v[48:51], a[176:179]\n"
    "ds_read_b128 a[208:211], v8 offset:33024\n"
    "v_mfma_f32_16x16x32_bf16 a[176:179], a[244:247], v[60:63], a[176:179]\n"
    "ds_read_b128 a[212:215], v9 offset:33024\n"
    "v_mfma_f32_16x16x32_bf16 a[188:191], a[248:251], v[48:51], a[188:191]\n"
    "ds_read_b128 a[216:219], v10 offset:33024\n"
    "v_mfma_f32_16x16x32_bf16 a[188:191], a[252:255], v[60:63], a[188:191]\n"
    "ds_read_b128 a[220:223], v11 offset:33024\n"
    "v_mfma_f32_16x16x32_bf16 a[184:187], a[252:255], v[56:59], a[184:187]\n"
    "v_mfma_f32_16x16x32_bf16 a[184:187], a[248:251], v[44:47], a[184:187]\n"
    "v_mfma_f32_16x16x32_bf16 a[180:183], a[248:251], v[40:43], a[180:183]\n"
    "v_mfma_f32_16x16x32_bf16 a[180:183], a[252:255], v[52:55], a[180:183]\n"
    "s_waitcnt lgkmcnt(6)\n"
    "s_cbranch_vccnz Lstub5_%=\n"
    "Lok5_%=:\n"
    "v_mfma_f32_16x16x32_bf16 v[88:91], a[192:195], v[64:67], v[232:235]\n"
    "s_waitcnt lgkmcnt(5)\n"
    "v_add_f32 v27, v27, v30\n"
    "v_add_f32 v28, v28, v31\n"
    "v_mfma_f32_16x16x32_bf16 v[88:91], a[196:199], v[76:79], v[88:91]\n"
    "v_add_f32 v29, v29, v32\n"
    "v_mfma_f32_16x16x32_bf16 v[88:91], a[200:203], v[112:115], v[88:91]\n"
    "s_waitcnt lgkmcnt(3)\n"
    "ds_read_b128 a[252:255], v11 offset:41216\n"
    "v_mfma_f32_16x16x32_bf16 v[88:91], a[204:207], v[124:127], v[88:91]\n"
    "ds_read_b128 a[248:251], v10 offset:41216\n"
    "v_mfma_f32_16x16x32_bf16 v[88:91], a[208:211], v[160:163], v[88:91]\n"
    "s_waitcnt lgkmcnt(3)\n"
    "ds_read_b128 a[244:247], v9 offset:41216\n"
    "v_mfma_f32_16x16x32_bf16 v[88:91], a[212:215], v[172:175], v[88:91]\n"
    "ds_read_b128 a[240:243], v8 offset:41216\n"
    "v_mfma_f32_16x16x32_bf16 v[88:91], a[216:219], v[208:211], v[88:91]\n"
    "s_waitcnt lgkmcnt(4)\n"
    "ds_read_b128 a[236:239], v11 offset:40960\n"
    "v_mfma_f32_16x16x32_bf16 v[88:91], a[220:223], v[220:223], v[88:91]\n"
    "ds_read_b128 a[232:235], v10 offset:40960\n"
    "v_mfma_f32_16x16x32_bf16 v[92:95], a[220:223], v[224:227], v[236:239]\n"
    "ds_read_b128 a[228:231], v9 offset:40960\n"
    "v_mfma_f32_16x16x32_bf16 v[92:95], a[216:219], v[212:215], v[92:95]\n"
    "ds_read_b128 a[224:227], v8 offset:40960\n"
    "v_mfma_f32_16x16x32_bf16 v[92:95], a[212:215], v[176:179], v[92:95]\n"
    "v_mfma_f32_16x16x32_bf16 v[92:95], a[208:211], v[164:167], v[92:95]\n"
    "v_mfma_f32_16x16x32_bf16 v[92:95], a[204:207], v[128:131], v[92:95]\n"
    "v_exp_f32 v244, v88\n"
    "v_mfma_f32_16x16x32_bf16 v[92:95], a[200:203], v[116:119], v[92:95]\n"
    "v_exp_f32 v245, v89\n"
    "v_mfma_f32_16x16x32_bf16 v[92:95], a[196:199], v[80:83], v[92:95]\n"
    "v_exp_f32 v246, v90\n"
    "v_mfma_f32_16x16x32_bf16 v[92:95], a[192:195], v[68:71], v[92:95]\n"
    "v_exp_f32 v247, v91\n"
    "v_mfma_f32_16x16x32_bf16 v[96:99], a[192:195], v[72:75], v[240:243]\n"
    "v_add_f32 v30, v244, v245\n"
    "v_cvt_pk_bf16_f32 v40, v244, v245\n"
    "v_mfma_f32_16x16x32_bf16 v[96:99], a[196:199], v[84:87], v[96:99]\n"
    "v_add_f32 v30, v30, v246\n"
    "v_cvt_pk_bf16_f32 v41, v246, v247\n"
    "v_mfma_f32_16x16x32_bf16 v[96:99], a[200:203], v[120:123], v[96:99]\n"
    "v_exp_f32 v248, v92\n"
    "v_mfma_f32_16x16x32_bf16 v[96:99], a[204:207], v[132:135], v[96:99]\n"
    "v_exp_f32 v249, v93\n"
    "v_mfma_f32_16x16x32_bf16 v[96:99], a[208:211], v[168:171], v[96:99]\n"
    "v_exp_f32 v250, v94\n"
    "v_mfma_f32_16x16x32_bf16 v[96:99], a[212:215], v[180:183], v[96:99]\n"
    "v_exp_f32 v251, v95\n"
    "v_mfma_f32_16x16x32_bf16 v[96:99], a[216:219], v[216:219], v[96:99]\n"
    "v_add_f32 v30, v30, v247\n"
    "v_add_f32 v31, v248, v249\n"
    "v_mfma_f32_16x16x32_bf16 v[96:99], a[220:223], v[228:231], v[96:99]\n"
    "s_waitcnt lgkmcnt(6)\n"
    "v_cvt_pk_bf16_f32 v44, v248, v249\n"
    "v_add_f32 v31, v31, v250\n"
    "v_mfma_f32_16x16x32_bf16 v[100:103], a[252:255], v[220:223], v[232:235]\n"
    "v_cvt_pk_bf16_f32 v45, v250, v251\n"
    "v_add_f32 v31, v31, v251\n"
    "v_mfma_f32_16x16x32_bf16 v[100:103], a[248:251], v[208:211], v[100:103]\n"
    "s_waitcnt lgkmcnt(4)\n"
    "v_mfma_f32_16x16x32_bf16 v[100:103], a[244:247], v[172:175], v[100:103]\n"
    "ds_read_b128 a[192:195], v8 offset:49152\n"
    "v_mfma_f32_16x16x32_bf16 v[100:103], a[240:243], v[160:163], v[100:103]\n"
    "s_waitcnt lgkmcnt(3)\n"
    "ds_read_b128 a[196:199], v9 offset:49152\n"
    "v_mfma_f32_16x16x32_bf16 v[100:103], a[236:239], v[124:127], v[100:103]\n"
    "ds_read_b128 a[200:203], v10 offset:49152\n"
    "v_mfma_f32_16x16x32_bf16 v[100:103], a[232:235], v[112:115], v[100:103]\n"
    "s_waitcnt lgkmcnt(3)\n"
    "ds_read_b128 a[204:207], v11 offset:49152\n"
    "v_mfma_f32_16x16x32_bf16 v[100:103], a[228:231], v[76:79], v[100:103]\n"
    "ds_read_b128 a[208:211], v8 offset:49408\n"
    "v_mfma_f32_16x16x32_bf16 v[100:103], a[224:227], v[64:67], v[100:103]\n"
    "ds_read_b128 a[212:215], v9 offset:49408\n"
    "v_mfma_f32_16x16x32_bf16 v[104:107], a[224:227], v[68:71], v[236:239]\n"
    "ds_read_b128 a[216:219], v10 offset:49408\n"
    "v_mfma_f32_16x16x32_bf16 v[104:107], a[228:231], v[80:83], v[104:107]\n"
    "ds_read_b128 a[220:223], v11 offset:49408\n"
    "v_mfma_f32_16x16x32_bf16 v[104:107], a[232:235], v[116:119], v[104:107]\n"
    "v_exp_f32 v252, v96\n"
    "v_mfma_f32_16x16x32_bf16 v[104:107], a[236:239], v[128:131], v[104:107]\n"
    "v_exp_f32 v253, v97\n"
    "v_mfma_f32_16x16x32_bf16 v[104:107], a[240:243], v[164:167], v[104:107]\n"
    "v_exp_f32 v244, v98\n"
    "v_mfma_f32_16x16x32_bf16 v[104:107], a[244:247], v[176:179], v[104:107]\n"
    "v_exp_f32 v245, v99\n"
    "v_mfma_f32_16x16x32_bf16 v[104:107], a[248:251], v[212:215], v[104:107]\n"
    "v_exp_f32 v246, v100\n"
    "v_mfma_f32_16x16x32_bf16 v[104:107], a[252:255], v[224:227], v[104:107]\n"
    "v_exp_f32 v247, v101\n"
    "v_mfma_f32_16x16x32_bf16 v[108:111], a[252:255], v[228:231], v[240:243]\n"
    "v_exp_f32 v248, v102\n"
    "v_mfma_f32_16x16x32_bf16 v[108:111], a[248:251], v[216:219], v[108:111]\n"
    "v_exp_f32 v249, v103\n"
    "v_mfma_f32_16x16x32_bf16 v[108:111], a[244:247], v[180:183], v[108:111]\n"
    "v_add_f32 v32, v252, v253\n"
    "v_cvt_pk_bf16_f32 v48, v252, v253\n"
    "v_mfma_f32_16x16x32_bf16 v[108:111], a[240:243], v[168:171], v[108:111]\n"
    "v_exp_f32 v250, v104\n"
    "v_mfma_f32_16x16x32_bf16 v[108:111], a[236:239], v[132:135], v[108:111]\n"
    "v_exp_f32 v251, v105\n"
    "v_mfma_f32_16x16x32_bf16 v[108:111], a[232:235], v[120:123], v[108:111]\n"
    "v_exp_f32 v252, v106\n"
    "v_mfma_f32_16x16x32_bf16 v[108:111], a[228:231], v[84:87], v[108:111]\n"
    "v_exp_f32 v253, v107\n"
    "v_mfma_f32_16x16x32_bf16 v[108:111], a[224:227], v[72:75], v[108:111]\n"
    "s_waitcnt lgkmcnt(6)\n"
    "v_add_f32 v32, v32, v244\n"
    "v_cvt_pk_bf16_f32 v49, v244, v245\n"
    "v_mfma_f32_16x16x32_bf16 v[136:139], a[192:195], v[64:67], v[232:235]\n"
    "v_add_f32 v32, v32, v245\n"
    "v_add_f32 v30, v30, v246\n"
    "v_mfma_f32_16x16x32_bf16 v[136:139], a[196:199], v[76:79], v[136:139]\n"
    "s_waitcnt lgkmcnt(4)\n"
    "v_cvt_pk_bf16_f32 v42, v246, v247\n"
    "v_add_f32 v30, v30, v247\n"
    "v_mfma_f32_16x16x32_bf16 v[136:139], a[200:203], v[112:115], v[136:139]\n"
    "ds_read_b128 a[252:255], v11 offset:57600\n"
    "v_add_f32 v30, v30, v248\n"
    "v_mfma_f32_16x16x32_bf16 v[136:139], a[204:207], v[124:127], v[136:139]\n"
    "s_waitcnt lgkmcnt(3)\n"
    "ds_read_b128 a[248:251], v10 offset:57600\n"
    "v_cvt_pk_bf16_f32 v43, v248, v249\n"
    "v_mfma_f32_16x16x32_bf16 v[136:139], a[208:211], v[160:163], v[136:139]\n"
    "ds_read_b128 a[244:247], v9 offset:57600\n"
    "v_add_f32 v30, v30, v249\n"
    "v_mfma_f32_16x16x32_bf16 v[136:139], a[212:215], v[172:175], v[136:139]\n"
    "s_waitcnt lgkmcnt(3)\n"
    "ds_read_b128 a[240:243], v8 offset:57600\n"
    "v_add_f32 v31, v31, v250\n"
    "v_mfma_f32_16x16x32_bf16 v[136:139], a[216:219], v[208:211], v[136:139]\n"
    "ds_read_b128 a[236:239], v11 offset:57344\n"
    "v_cvt_pk_bf16_f32 v46, v250, v251\n"
    "v_mfma_f32_16x16x32_bf16 v[136:139], a[220:223], v[220:223], v[136:139]\n"
    "ds_read_b128 a[232:235], v10 offset:57344\n"
    "v_add_f32 v31, v31, v251\n"
    "v_mfma_f32_16x16x32_bf16 v[140:143], a[220:223], v[224:227], v[236:239]\n"
    "ds_read_b128 a[228:231], v9 offset:57344\n"
    "v_add_f32 v31, v31, v252\n"
    "v_mfma_f32_16x16x32_bf16 v[140:143], a[216:219], v[212:215], v[140:143]\n"
    "ds_read_b128 a[224:227], v8 offset:57344\n"
    "v_cvt_pk_bf16_f32 v47, v252, v253\n"
    "v_mfma_f32_16x16x32_bf16 v[140:143], a[212:215], v[176:179], v[140:143]\n"
    "v_exp_f32 v244, v108\n"
    "v_mfma_f32_16x16x32_bf16 v[140:143], a[208:211], v[164:167], v[140:143]\n"
    "v_exp_f32 v245, v109\n"
    "v_mfma_f32_16x16x32_bf16 v[140:143], a[204:207], v[128:131], v[140:143]\n"
    "v_exp_f32 v246, v110\n"
    "v_mfma_f32_16x16x32_bf16 v[140:143], a[200:203], v[116:119], v[140:143]\n"
    "v_exp_f32 v247, v111\n"
    "v_mfma_f32_16x16x32_bf16 v[140:143], a[196:199], v[80:83], v[140:143]\n"
    "v_exp_f32 v248, v136\n"
    "v_mfma_f32_16x16x32_bf16 v[140:143], a[192:195], v[68:71], v[140:143]\n"
    "v_exp_f32 v249, v137\n"
    "v_mfma_f32_16x16x32_bf16 v[144:147], a[192:195], v[72:75], v[240:243]\n"
    "v_exp_f32 v250, v138\n"
    "v_mfma_f32_16x16x32_bf16 v[144:147], a[196:199], v[84:87], v[144:147]\n"
    "v_exp_f32 v251, v139\n"
    "v_mfma_f32_16x16x32_bf16 v[144:147], a[200:203], v[120:123], v[144:147]\n"
    "v_add_f32 v31, v31, v253\n"
    "v_add_f32 v32, v32, v244\n"
    "v_mfma_f32_16x16x32_bf16 v[144:147], a[204:207], v[132:135], v[144:147]\n"
    "v_exp_f32 v252, v140\n"
    "v_mfma_f32_16x16x32_bf16 v[144:147], a[208:211], v[168:171], v[144:147]\n"
    "v_exp_f32 v253, v141\n"
    "v_mfma_f32_16x16x32_bf16 v[144:147], a[212:215], v[180:183], v[144:147]\n"
    "v_cvt_pk_bf16_f32 v50, v244, v245\n"
    "v_add_f32 v32, v32, v245\n"
    "v_mfma_f32_16x16x32_bf16 v[144:147], a[216:219], v[216:219], v[144:147]\n"
    "v_exp_f32 v244, v142\n"
    "v_mfma_f32_16x16x32_bf16 v[144:147], a[220:223], v[228:231], v[144:147]\n"
    "s_waitcnt lgkmcnt(6)\n"
    "v_exp_f32 v245, v143\n"
    "v_mfma_f32_16x16x32_bf16 v[148:151], a[252:255], v[220:223], v[232:235]\n"
    "v_add_f32 v32, v32, v246\n"
    "v_cvt_pk_bf16_f32 v51, v246, v247\n"
    "v_mfma_f32_16x16x32_bf16 v[148:151], a[248:251], v[208:211], v[148:151]\n"
    "s_waitcnt lgkmcnt(4)\n"
    "v_add_f32 v32, v32, v247\n"
    "v_add_f32 v30, v30, v248\n"
    "v_mfma_f32_16x16x32_bf16 v[148:151], a[244:247], v[172:175], v[148:151]\n"
    "v_exp_f32 v246, v144\n"
    "v_mfma_f32_16x16x32_bf16 v[148:151], a[240:243], v[160:163], v[148:151]\n"
    "s_waitcnt lgkmcnt(2)\n"
    "v_exp_f32 v247, v145\n"
    "v_mfma_f32_16x16x32_bf16 v[148:151], a[236:239], v[124:127], v[148:151]\n"
    "v_cvt_pk_bf16_f32 v52, v248, v249\n"
    "v_add_f32 v30, v30, v249\n"
    "v_mfma_f32_16x16x32_bf16 v[148:151], a[232:235], v[112:115], v[148:151]\n"
    "s_waitcnt lgkmcnt(0)\n"
    "v_exp_f32 v248, v146\n"
    "v_mfma_f32_16x16x32_bf16 v[148:151], a[228:231], v[76:79], v[148:151]\n"
    "v_exp_f32 v249, v147\n"
    "v_mfma_f32_16x16x32_bf16 v[148:151], a[224:227], v[64:67], v[148:151]\n"
    "s_waitcnt lgkmcnt(0)\n"
    "s_barrier\n"
    "v_add_f32 v30, v30, v250\n"
    "v_mfma_f32_16x16x32_bf16 v[152:155], a[224:227], v[68:71], v[236:239]\n"
    "ds_read_b64_tr_b16 a[192:193], v12 offset:0\n"
    "v_cvt_pk_bf16_f32 v53, v250, v251\n"
    "v_mfma_f32_16x16x32_bf16 v[152:155], a[228:231], v[80:83], v[152:155]\n"
    "ds_read_b64_tr_b16 a[194:195], v12 offset:8192\n"
    "v_add_f32 v30, v30, v251\n"
    "v_mfma_f32_16x16x32_bf16 v[152:155], a[232:235], v[116:119], v[152:155]\n"
    "ds_read_b64_tr_b16 a[196:197], v12 offset:16384\n"
    "v_add_f32 v31, v31, v252\n"
    "v_mfma_f32_16x16x32_bf16 v[152:155], a[236:239], v[128:131], v[152:155]\n"
    "ds_read_b64_tr_b16 a[198:199], v12 offset:24576\n"
    "v_cvt_pk_bf16_f32 v56, v252, v253\n"
    "v_mfma_f32_16x16x32_bf16 v[152:155], a[240:243], v[164:167], v[152:155]\n"
    "ds_read_b64_tr_b16 a[200:201], v13 offset:0\n"
    "v_add_f32 v31, v31, v253\n"
    "v_mfma_f32_16x16x32_bf16 v[152:155], a[244:247], v[176:179], v[152:155]\n"
    "ds_read_b64_tr_b16 a[202:203], v13 offset:8192\n"
    "v_add_f32 v31, v31, v244\n"
    "v_mfma_f32_16x16x32_bf16 v[152:155], a[248:251], v[212:215], v[152:155]\n"
    "ds_read_b64_tr_b16 a[204:205], v13 offset:16384\n"
    "v_cvt_pk_bf16_f32 v57, v244, v245\n"
    "v_mfma_f32_16x16x32_bf16 v[152:155], a[252:255], v[224:227], v[152:155]\n"
    "ds_read_b64_tr_b16 a[206:207], v13 offset:24576\n"
    "v_add_f32 v31, v31, v245\n"
    "v_mfma_f32_16x16x32_bf16 v[156:159], a[252:255], v[228:231], v[240:243]\n"
    "ds_read_b64_tr_b16 a[208:209], v14 offset:0\n"
    "v_add_f32 v32, v32, v246\n"
    "v_mfma_f32_16x16x32_bf16 v[156:159], a[248:251], v[216:219], v[156:159]\n"
    "ds_read_b64_tr_b16 a[210:211], v14 offset:8192\n"
    "v_cvt_pk_bf16_f32 v60, v246, v247\n"
    "v_mfma_f32_16x16x32_bf16 v[156:159], a[244:247], v[180:183], v[156:159]\n"
    "ds_read_b64_tr_b16 a[212:213], v14 offset:16384\n"
    "v_add_f32 v32, v32, v247\n"
    "v_mfma_f32_16x16x32_bf16 v[156:159], a[240:243], v[168:171], v[156:159]\n"
    "ds_read_b64_tr_b16 a[214:215], v14 offset:24576\n"
    "v_add_f32 v32, v32, v248\n"
    "v_mfma_f32_16x16x32_bf16 v[156:159], a[236:239], v[132:135], v[156:159]\n"
    "ds_read_b64_tr_b16 a[216:217], v15 offset:0\n"
    "v_cvt_pk_bf16_f32 v61, v248, v249\n"
    "v_mfma_f32_16x16x32_bf16 v[156:159], a[232:235], v[120:123], v[156:159]\n"
    "ds_read_b64_tr_b16 a[218:219], v15 offset:8192\n"
    "v_add_f32 v32, v32, v249\n"
    "v_mfma_f32_16x16x32_bf16 v[156:159], a[228:231], v[84:87], v[156:159]\n"
    "ds_read_b64_tr_b16 a[220:221], v15 offset:16384\n"
    "v_mfma_f32_16x16x32_bf16 v[156:159], a[224:227], v[72:75], v[156:159]\n"
    "s_waitcnt lgkmcnt(11)\n"
    "ds_read_b64_tr_b16 a[222:223], v15 offset:24576\n"
    "v_mfma_f32_16x16x32_bf16 a[0:3], a[192:195], v[184:187], a[0:3]\n"
    "v_exp_f32 v250, v148\n"
    "v_mfma_f32_16x16x32_bf16 a[0:3], a[196:199], v[196:199], a[0:3]\n"
    "v_exp_f32 v251, v149\n"
    "v_mfma_f32_16x16x32_bf16 a[4:7], a[196:199], v[200:203], a[4:7]\n"
    "ds_read_b64_tr_b16 a[224:225], v16 offset:0\n"
    "v_add_f32 v30, v30, v250\n"
    "v_mfma_f32_16x16x32_bf16 a[4:7], a[192:195], v[188:191], a[4:7]\n"
    "ds_read_b64_tr_b16 a[226:227], v16 offset:8192\n"
    "v_cvt_pk_bf16_f32 v54, v250, v251\n"
    "v_mfma_f32_16x16x32_bf16 a[8:11], a[192:195], v[192:195], a[8:11]\n"
    "ds_read_b64_tr_b16 a[228:229], v16 offset:16384\n"
    "v_add_f32 v30, v30, v251\n"
    "v_mfma_f32_16x16x32_bf16 a[8:11], a[196:199], v[204:207], a[8:11]\n"
    "s_waitcnt lgkmcnt(11)\n"
    "ds_read_b64_tr_b16 a[230:231], v16 offset:24576\n"
    "v_mfma_f32_16x16x32_bf16 a[20:23], a[200:203], v[192:195], a[20:23]\n"
    "v_exp_f32 v252, v150\n"
    "v_mfma_f32_16x16x32_bf16 a[20:23], a[204:207], v[204:207], a[20:23]\n"
    "v_exp_f32 v253, v151\n"
    "v_mfma_f32_16x16x32_bf16 a[16:19], a[204:207], v[200:203], a[16:19]\n"
    "ds_read_b64_tr_b16 a[232:233], v17 offset:0\n"
    "v_add_f32 v30, v30, v252\n"
    "v_mfma_f32_16x16x32_bf16 a[16:19], a[200:203], v[188:191], a[16:19]\n"
    "ds_read_b64_tr_b16 a[234:235], v17 offset:8192\n"
    "v_cvt_pk_bf16_f32 v55, v252, v253\n"
    "v_mfma_f32_16x16x32_bf16 a[12:15], a[200:203], v[184:187], a[12:15]\n"
    "ds_read_b64_tr_b16 a[236:237], v17 offset:16384\n"
    "v_add_f32 v30, v30, v253\n"
    "v_mfma_f32_16x16x32_bf16 a[12:15], a[204:207], v[196:199], a[12:15]\n"
    "s_waitcnt lgkmcnt(11)\n"
    "ds_read_b64_tr_b16 a[238:239], v17 offset:24576\n"
    "v_mfma_f32_16x16x32_bf16 a[24:27], a[208:211], v[184:187], a[24:27]\n"
    "v_exp_f32 v244, v152\n"
    "v_mfma_f32_16x16x32_bf16 a[24:27], a[212:215], v[196:199], a[24:27]\n"
    "v_exp_f32 v245, v153\n"
    "v_mfma_f32_16x16x32_bf16 a[28:31], a[212:215], v[200:203], a[28:31]\n"
    "ds_read_b64_tr_b16 a[240:241], v18 offset:0\n"
    "v_add_f32 v31, v31, v244\n"
    "v_mfma_f32_16x16x32_bf16 a[28:31], a[208:211], v[188:191], a[28:31]\n"
    "ds_read_b64_tr_b16 a[242:243], v18 offset:8192\n"
    "v_cvt_pk_bf16_f32 v58, v244, v245\n"
    "v_mfma_f32_16x16x32_bf16 a[32:35], a[208:211], v[192:195], a[32:35]\n"
    "ds_read_b64_tr_b16 a[244:245], v18 offset:16384\n"
    "v_add_f32 v31, v31, v245\n"
    "v_mfma_f32_16x16x32_bf16 a[32:35], a[212:215], v[204:207], a[32:35]\n"
    "s_waitcnt lgkmcnt(11)\n"
    "ds_read_b64_tr_b16 a[246:247], v18 offset:24576\n"
    "v_mfma_f32_16x16x32_bf16 a[44:47], a[216:219], v[192:195], a[44:47]\n"
    "v_exp_f32 v246, v154\n"
    "v_mfma_f32_16x16x32_bf16 a[44:47], a[220:223], v[204:207], a[44:47]\n"
    "v_exp_f32 v247, v155\n"
    "v_mfma_f32_16x16x32_bf16 a[40:43], a[220:223], v[200:203], a[40:43]\n"
    "ds_read_b64_tr_b16 a[248:249], v19 offset:0\n"
    "v_add_f32 v31, v31, v246\n"
    "v_mfma_f32_16x16x32_bf16 a[40:43], a[216:219], v[188:191], a[40:43]\n"
    "ds_read_b64_tr_b16 a[250:251], v19 offset:8192\n"
    "v_cvt_pk_bf16_f32 v59, v246, v247\n"
    "v_mfma_f32_16x16x32_bf16 a[36:39], a[216:219], v[184:187], a[36:39]\n"
    "ds_read_b64_tr_b16 a[252:253], v19 offset:16384\n"
    "v_add_f32 v31, v31, v247\n"
    "v_mfma_f32_16x16x32_bf16 a[36:39], a[220:223], v[196:199], a[36:39]\n"
    "s_waitcnt lgkmcnt(11)\n"
    "ds_read_b64_tr_b16 a[254:255], v19 offset:24576\n"
    "v_mfma_f32_16x16x32_bf16 a[48:51], a[224:227], v[184:187], a[48:51]\n"
    "v_exp_f32 v248, v156\n"
    "v_mfma_f32_16x16x32_bf16 a[48:51], a[228:231], v[196:199], a[48:51]\n"
    "v_exp_f32 v249, v157\n"
    "v_mfma_f32_16x16x32_bf16 a[52:55], a[228:231], v[200:203], a[52:55]\n"
    "ds_read_b64_tr_b16 a[192:193], v12 offset:256\n"
    "v_add_f32 v32, v32, v248\n"
    "v_mfma_f32_16x16x32_bf16 a[52:55], a[224:227], v[188:191], a[52:55]\n"
    "ds_read_b64_tr_b16 a[194:195], v12 offset:8448\n"
    "v_cvt_pk_bf16_f32 v62, v248, v249\n"
    "v_mfma_f32_16x16x32_bf16 a[56:59], a[224:227], v[192:195], a[56:59]\n"
    "ds_read_b64_tr_b16 a[196:197], v12 offset:16640\n"
    "v_add_f32 v32, v32, v249\n"
    "v_mfma_f32_16x16x32_bf16 a[56:59], a[228:231], v[204:207], a[56:59]\n"
    "s_waitcnt lgkmcnt(11)\n"
    "ds_read_b64_tr_b16 a[198:199], v12 offset:24832\n"
    "v_mfma_f32_16x16x32_bf16 a[68:71], a[232:235], v[192:195], a[68:71]\n"
    "v_exp_f32 v250, v158\n"
    "v_mfma_f32_16x16x32_bf16 a[68:71], a[236:239], v[204:207], a[68:71]\n"
    "v_exp_f32 v251, v159\n"
    "v_mfma_f32_16x16x32_bf16 a[64:67], a[236:239], v[200:203], a[64:67]\n"
    "ds_read_b64_tr_b16 a[200:201], v13 offset:256\n"
    "v_add_f32 v32, v32, v250\n"
    "v_mfma_f32_16x16x32_bf16 a[64:67], a[232:235], v[188:191], a[64:67]\n"
    "ds_read_b64_tr_b16 a[202:203], v13 offset:8448\n"
    "v_cvt_pk_bf16_f32 v63, v250, v251\n"
    "v_mfma_f32_16x16x32_bf16 a[60:63], a[232:235], v[184:187], a[60:63]\n"
    "ds_read_b64_tr_b16 a[204:205], v13 offset:16640\n"
    "v_add_f32 v32, v32, v251\n"
    "v_mfma_f32_16x16x32_bf16 a[60:63], a[236:239], v[196:199], a[60:63]\n"
    "s_waitcnt lgkmcnt(11)\n"
    "ds_read_b64_tr_b16 a[206:207], v13 offset:24832\n"
    "v_max3_f32 v254, v30, v31, v32\n"
    "v_mfma_f32_16x16x32_bf16 a[72:75], a[240:243], v[184:187], a[72:75]\n"
    "v_cmp_lt_f32 vcc, s58, v254\n"
    "v_mfma_f32_16x16x32_bf16 a[72:75], a[244:247], v[196:199], a[72:75]\n"
    "v_mfma_f32_16x16x32_bf16 a[76:79], a[244:247], v[200:203], a[76:79]\n"
    "ds_read_b64_tr_b16 a[208:209], v14 offset:256\n"
    "v_mfma_f32_16x16x32_bf16 a[76:79], a[240:243], v[188:191], a[76:79]\n"
    "ds_read_b64_tr_b16 a[210:211], v14 offset:8448\n"
    "v_mfma_f32_16x16x32_bf16 a[80:83], a[240:243], v[192:195], a[80:83]\n"
    "ds_read_b64_tr_b16 a[212:213], v14 offset:16640\n"
    "v_mfma_f32_16x16x32_bf16 a[80:83], a[244:247], v[204:207], a[80:83]\n"
    "s_waitcnt lgkmcnt(11)\n"
    "ds_read_b64_tr_b16 a[214:215], v14 offset:24832\n"
    "v_mfma_f32_16x16x32_bf16 a[92:95], a[248:251], v[192:195], a[92:95]\n"
    "v_mfma_f32_16x16x32_bf16 a[92:95], a[252:255], v[204:207], a[92:95]\n"
    "v_mfma_f32_16x16x32_bf16 a[88:91], a[252:255], v[200:203], a[88:91]\n"
    "ds_read_b64_tr_b16 a[216:217], v15 offset:256\n"
    "v_mfma_f32_16x16x32_bf16 a[88:91], a[248:251], v[188:191], a[88:91]\n"
    "ds_read_b64_tr_b16 a[218:219], v15 offset:8448\n"
    "v_mfma_f32_16x16x32_bf16 a[84:87], a[248:251], v[184:187], a[84:87]\n"
    "ds_read_b64_tr_b16 a[220:221], v15 offset:16640\n"
    "v_mfma_f32_16x16x32_bf16 a[84:87], a[252:255], v[196:199], a[84:87]\n"
    "s_waitcnt lgkmcnt(11)\n"
    "ds_read_b64_tr_b16 a[222:223], v15 offset:24832\n"
    "v_mfma_f32_16x16x32_bf16 a[96:99], a[192:195], v[184:187], a[96:99]\n"
    "v_mfma_f32_16x16x32_bf16 a[96:99], a[196:199], v[196:199], a[96:99]\n"
    "v_mfma_f32_16x16x32_bf16 a[100:103], a[196:199], v[200:203], a[100:103]\n"
    "ds_read_b64_tr_b16 a[224:225], v16 offset:256\n"
    "v_mfma_f32_16x16x32_bf16 a[100:103], a[192:195], v[188:191], a[100:103]\n"
    "ds_read_b64_tr_b16 a[226:227], v16 offset:8448\n"
    "v_mfma_f32_16x16x32_bf16 a[104:107], a[192:195], v[192:195], a[104:107]\n"
    "ds_read_b64_tr_b16 a[228:229], v16 offset:16640\n"
    "v_mfma_f32_16x16x32_bf16 a[104:107], a[196:199], v[204:207], a[104:107]\n"
    "s_waitcnt lgkmcnt(11)\n"
    "ds_read_b64_tr_b16 a[230:231], v16 offset:24832\n"
    "v_mfma_f32_16x16x32_bf16 a[116:119], a[200:203], v[192:195], a[116:119]\n"
    "v_mfma_f32_16x16x32_bf16 a[116:119], a[204:207], v[204:207], a[116:119]\n"
    "v_mfma_f32_16x16x32_bf16 a[112:115], a[204:207], v[200:203], a[112:115]\n"
    "ds_read_b64_tr_b16 a[232:233], v17 offset:256\n"
    "v_mfma_f32_16x16x32_bf16 a[112:115], a[200:203], v[188:191], a[112:115]\n"
    "ds_read_b64_tr_b16 a[234:235], v17 offset:8448\n"
    "v_mfma_f32_16x16x32_bf16 a[108:111], a[200:203], v[184:187], a[108:111]\n"
    "ds_read_b64_tr_b16 a[236:237], v17 offset:16640\n"
    "v_mfma_f32_16x16x32_bf16 a[108:111], a[204:207], v[196:199], a[108:111]\n"
    "s_waitcnt lgkmcnt(11)\n"
    "ds_read_b64_tr_b16 a[238:239], v17 offset:24832\n"
    "v_mfma_f32_16x16x32_bf16 a[120:123], a[208:211], v[184:187], a[120:123]\n"
    "v_mfma_f32_16x16x32_bf16 a[120:123], a[212:215], v[196:199], a[120:123]\n"
    "v_mfma_f32_16x16x32_bf16 a[124:127], a[212:215], v[200:203], a[124:127]\n"
    "ds_read_b64_tr_b16 a[240:241], v18 offset:256\n"
    "v_mfma_f32_16x16x32_bf16 a[124:127], a[208:211], v[188:191], a[124:127]\n"
    "ds_read_b64_tr_b16 a[242:243], v18 offset:8448\n"
    "v_mfma_f32_16x16x32_bf16 a[128:131], a[208:211], v[192:195], a[128:131]\n"
    "ds_read_b64_tr_b16 a[244:245], v18 offset:16640\n"
    "v_mfma_f32_16x16x32_bf16 a[128:131], a[212:215], v[204:207], a[128:131]\n"
    "s_waitcnt lgkmcnt(11)\n"
    "ds_read_b64_tr_b16 a[246:247], v18 offset:24832\n"
    "v_mfma_f32_16x16x32_bf16 a[140:143], a[216:219], v[192:195], a[140:143]\n"
    "v_mfma_f32_16x16x32_bf16 a[140:143], a[220:223], v[204:207], a[140:143]\n"
    "v_mfma_f32_16x16x32_bf16 a[136:139], a[220:223], v[200:203], a[136:139]\n"
    "ds_read_b64_tr_b16 a[248:249], v19 offset:256\n"
    "v_mfma_f32_16x16x32_bf16 a[136:139], a[216:219], v[188:191], a[136:139]\n"
    "ds_read_b64_tr_b16 a[250:251], v19 offset:8448\n"
    "v_mfma_f32_16x16x32_bf16 a[132:135], a[216:219], v[184:187], a[132:135]\n"
    "ds_read_b64_tr_b16 a[252:253], v19 offset:16640\n"
    "v_mfma_f32_16x16x32_bf16 a[132:135], a[220:223], v[196:199], a[132:135]\n"
    "s_waitcnt lgkmcnt(11)\n"
    "ds_read_b64_tr_b16 a[254:255], v19 offset:24832\n"
    "v_mfma_f32_16x16x32_bf16 a[144:147], a[224:227], v[184:187], a[144:147]\n"
    "v_mfma_f32_16x16x32_bf16 a[144:147], a[228:231], v[196:199], a[144:147]\n"
    "v_mfma_f32_16x16x32_bf16 a[148:151], a[228:231], v[200:203], a[148:151]\n"
    "v_mfma_f32_16x16x32_bf16 a[148:151], a[224:227], v[188:191], a[148:151]\n"
    "v_mfma_f32_16x16x32_bf16 a[152:155], a[224:227], v[192:195], a[152:155]\n"
    "v_mfma_f32_16x16x32_bf16 a[152:155], a[228:231], v[204:207], a[152:155]\n"
    "s_waitcnt lgkmcnt(8)\n"
    "v_mfma_f32_16x16x32_bf16 a[164:167], a[232:235], v[192:195], a[164:167]\n"
    "v_mfma_f32_16x16x32_bf16 a[164:167], a[236:239], v[204:207], a[164:167]\n"
    "v_mfma_f32_16x16x32_bf16 a[160:163], a[236:239], v[200:203], a[160:163]\n"
    "v_mfma_f32_16x16x32_bf16 a[160:163], a[232:235], v[188:191], a[160:163]\n"
    "v_mfma_f32_16x16x32_bf16 a[156:159], a[232:235], v[184:187], a[156:159]\n"
    "v_mfma_f32_16x16x32_bf16 a[156:159], a[236:239], v[196:199], a[156:159]\n"
    "s_waitcnt lgkmcnt(0)\n"
    "s_barrier\n"
    "v_mfma_f32_16x16x32_bf16 a[168:171], a[240:243], v[184:187], a[168:171]\n"
    "ds_read_b64_tr_b16 a[192:193], v12 offset:32768\n"
    "v_mfma_f32_16x16x32_bf16 a[168:171], a[244:247], v[196:199], a[168:171]\n"
    "ds_read_b64_tr_b16 a[194:195], v12 offset:40960\n"
    "v_mfma_f32_16x16x32_bf16 a[172:175], a[244:247], v[200:203], a[172:175]\n"
    "ds_read_b64_tr_b16 a[196:197], v12 offset:49152\n"
    "v_mfma_f32_16x16x32_bf16 a[172:175], a[240:243], v[188:191], a[172:175]\n"
    "ds_read_b64_tr_b16 a[198:199], v12 offset:57344\n"
    "v_mfma_f32_16x16x32_bf16 a[176:179], a[240:243], v[192:195], a[176:179]\n"
    "ds_read_b64_tr_b16 a[200:201], v13 offset:32768\n"
    "v_mfma_f32_16x16x32_bf16 a[176:179], a[244:247], v[204:207], a[176:179]\n"
    "ds_read_b64_tr_b16 a[202:203], v13 offset:40960\n"
    "v_mfma_f32_16x16x32_bf16 a[188:191], a[248:251], v[192:195], a[188:191]\n"
    "ds_read_b64_tr_b16 a[204:205], v13 offset:49152\n"
    "v_mfma_f32_16x16x32_bf16 a[188:191], a[252:255], v[204:207], a[188:191]\n"
    "ds_read_b64_tr_b16 a[206:207], v13 offset:57344\n"
    "v_mfma_f32_16x16x32_bf16 a[184:187], a[252:255], v[200:203], a[184:187]\n"
    "v_mfma_f32_16x16x32_bf16 a[184:187], a[248:251], v[188:191], a[184:187]\n"
    "v_mfma_f32_16x16x32_bf16 a[180:183], a[248:251], v[184:187], a[180:183]\n"
    "v_mfma_f32_16x16x32_bf16 a[180:183], a[252:255], v[196:199], a[180:183]\n"
    "s_waitcnt lgkmcnt(4)\n"
    "s_cbranch_vccnz Lstub6_%=\n"
    "Lok6_%=:\n"
    "v_mfma_f32_16x16x32_bf16 a[0:3], a[192:195], v[40:43], a[0:3]\n"
    "s_waitcnt lgkmcnt(15)\n"
    "ds_read_b64_tr_b16 a[208:209], v14 offset:32768\n"
    "s_waitcnt vmcnt(0)\n"
    "v_mfma_f32_16x16x32_bf16 a[0:3], a[196:199], v[52:55], a[0:3]\n"
    "ds_read_b64_tr_b16 a[210:211], v14 offset:40960\n"
    "v_add_f32 v27, v27, v30\n"
    "v_mfma_f32_16x16x32_bf16 a[4:7], a[196:199], v[56:59], a[4:7]\n"
    "ds_read_b64_tr_b16 a[212:213], v14 offset:49152\n"
    "v_add_f32 v28, v28, v31\n"
    "v_mfma_f32_16x16x32_bf16 a[4:7], a[192:195], v[44:47], a[4:7]\n"
    "ds_read_b64_tr_b16 a[214:215], v14 offset:57344\n"
    "v_add_f32 v29, v29, v32\n"
    "v_mfma_f32_16x16x32_bf16 a[8:11], a[192:195], v[48:51], a[8:11]\n"
    "s_mul_i32 s72, s59, 3072\n"
    "ds_read_b64_tr_b16 a[216:217], v15 offset:32768\n"
    "v_mov_b32 v244, v27\n"
    "v_mfma_f32_16x16x32_bf16 a[8:11], a[196:199], v[60:63], a[8:11]\n"
    "s_waitcnt lgkmcnt(5)\n"
    "ds_read_b64_tr_b16 a[218:219], v15 offset:40960\n"
    "v_mov_b32 v245, v28\n"
    "v_mfma_f32_16x16x32_bf16 a[20:23], a[200:203], v[48:51], a[20:23]\n"
    "s_lshl_b32 s68, s35, 1\n"
    "ds_read_b64_tr_b16 a[220:221], v15 offset:49152\n"
    "v_mov_b32 v246, v29\n"
    "v_mfma_f32_16x16x32_bf16 a[20:23], a[204:207], v[60:63], a[20:23]\n"
    "s_mov_b32 s66, 0\n"
    "ds_read_b64_tr_b16 a[222:223], v15 offset:57344\n"
    "v_permlane32_swap_b32 v244, v27\n"
    "v_mfma_f32_16x16x32_bf16 a[16:19], a[204:207], v[56:59], a[16:19]\n"
    "s_lshl_b32 s67, s59, 7\n"
    "ds_read_b64_tr_b16 a[224:225], v16 offset:32768\n"
    "v_permlane32_swap_b32 v245, v28\n"
    "v_mfma_f32_16x16x32_bf16 a[16:19], a[200:203], v[44:47], a[16:19]\n"
    "s_lshl_b32 s48, s62, 3\n"
    "ds_read_b64_tr_b16 a[226:227], v16 offset:40960\n"
    "v_permlane32_swap_b32 v246, v29\n"
    "v_mfma_f32_16x16x32_bf16 a[12:15], a[200:203], v[40:43], a[12:15]\n"
    "s_add_u32 s67, s67, s48\n"
    "ds_read_b64_tr_b16 a[228:229], v16 offset:49152\n"
    "v_add_f32 v27, v244, v27\n"
    "v_mfma_f32_16x16x32_bf16 a[12:15], a[204:207], v[52:55], a[12:15]\n"
    "s_waitcnt lgkmcnt(7)\n"
    "ds_read_b64_tr_b16 a[230:231], v16 offset:57344\n"
    "v_add_f32 v28, v245, v28\n"
    "v_mfma_f32_16x16x32_bf16 a[24:27], a[208:211], v[40:43], a[24:27]\n"
    "s_sub_u32 s67, s67, 1152\n"
    "ds_read_b64_tr_b16 a[232:233], v17 offset:32768\n"
    "v_add_f32 v29, v246, v29\n"
    "v_mfma_f32_16x16x32_bf16 a[24:27], a[212:215], v[52:55], a[24:27]\n"
    "s_mov_b32 s64, 0\n"
    "ds_read_b64_tr_b16 a[234:235], v17 offset:40960\n"
    "v_mov_b32 v244, v27\n"
    "v_mfma_f32_16x16x32_bf16 a[28:31], a[212:215], v[56:59], a[28:31]\n"
    "s_mov_b32 s69, 0xfffe0000\n"
    "ds_read_b64_tr_b16 a[236:237], v17 offset:49152\n"
    "v_mov_b32 v245, v28\n"
    "v_mfma_f32_16x16x32_bf16 a[28:31], a[208:211], v[44:47], a[28:31]\n"
    "s_mov_b32 s70, 0x2c1111\n"
    "ds_read_b64_tr_b16 a[238:239], v17 offset:57344\n"
    "v_mov_b32 v246, v29\n"
    "v_mfma_f32_16x16x32_bf16 a[32:35], a[208:211], v[48:51], a[32:35]\n"
    "ds_read_b64_tr_b16 a[240:241], v18 offset:32768\n"
    "v_permlane16_swap_b32 v244, v27\n"
    "v_mfma_f32_16x16x32_bf16 a[32:35], a[212:215], v[60:63], a[32:35]\n"
    "s_waitcnt lgkmcnt(9)\n"
    "ds_read_b64_tr_b16 a[242:243], v18 offset:40960\n"
    "v_permlane16_swap_b32 v245, v28\n"
    "v_mfma_f32_16x16x32_bf16 a[44:47], a[216:219], v[48:51], a[44:47]\n"
    "ds_read_b64_tr_b16 a[244:245], v18 offset:49152\n"
    "v_permlane16_swap_b32 v246, v29\n"
    "v_mfma_f32_16x16x32_bf16 a[44:47], a[220:223], v[60:63], a[44:47]\n"
    "ds_read_b64_tr_b16 a[246:247], v18 offset:57344\n"
    "v_add_f32 v27, v244, v27\n"
    "v_mfma_f32_16x16x32_bf16 a[40:43], a[220:223], v[56:59], a[40:43]\n"
    "ds_read_b64_tr_b16 a[248:249], v19 offset:32768\n"
    "v_add_f32 v28, v245, v28\n"
    "v_mfma_f32_16x16x32_bf16 a[40:43], a[216:219], v[44:47], a[40:43]\n"
    "ds_read_b64_tr_b16 a[250:251], v19 offset:40960\n"
    "v_add_f32 v29, v246, v29\n"
    "v_mfma_f32_16x16x32_bf16 a[36:39], a[216:219], v[40:43], a[36:39]\n"
    "ds_read_b64_tr_b16 a[252:253], v19 offset:49152\n"
    "v_mov_b32 v37, 2.0\n"
    "v_mfma_f32_16x16x32_bf16 a[36:39], a[220:223], v[52:55], a[36:39]\n"
    "s_waitcnt lgkmcnt(11)\n"
    "ds_read_b64_tr_b16 a[254:255], v19 offset:57344\n"
    "v_add_u32 v24, s67, v26\n"
    "v_mfma_f32_16x16x32_bf16 a[48:51], a[224:227], v[40:43], a[48:51]\n"
    "v_rcp_f32 v33, v27\n"
    "v_mfma_f32_16x16x32_bf16 a[48:51], a[228:231], v[52:55], a[48:51]\n"
    "v_rcp_f32 v34, v28\n"
    "v_mfma_f32_16x16x32_bf16 a[52:55], a[228:231], v[56:59], a[52:55]\n"
    "ds_read_b64_tr_b16 a[192:193], v12 offset:33024\n"
    "v_accvgpr_read_b32 v88, a0\n"
    "v_mfma_f32_16x16x32_bf16 a[52:55], a[224:227], v[44:47], a[52:55]\n"
    "ds_read_b64_tr_b16 a[194:195], v12 offset:41216\n"
    "v_accvgpr_read_b32 v89, a1\n"
    "v_mfma_f32_16x16x32_bf16 a[56:59], a[224:227], v[48:51], a[56:59]\n"
    "ds_read_b64_tr_b16 a[196:197], v12 offset:49408\n"
    "v_accvgpr_read_b32 v90, a2\n"
    "v_mfma_f32_16x16x32_bf16 a[56:59], a[228:231], v[60:63], a[56:59]\n"
    "s_waitcnt lgkmcnt(11)\n"
    "ds_read_b64_tr_b16 a[198:199], v12 offset:57600\n"
    "v_accvgpr_read_b32 v91, a3\n"
    "v_mfma_f32_16x16x32_bf16 a[68:71], a[232:235], v[48:51], a[68:71]\n"
    "v_rcp_f32 v35, v29\n"
    "v_mfma_f32_16x16x32_bf16 a[68:71], a[236:239], v[60:63], a[68:71]\n"
    "v_accvgpr_read_b32 v92, a12\n"
    "v_accvgpr_read_b32 v93, a13\n"
    "v_mfma_f32_16x16x32_bf16 a[64:67], a[236:239], v[56:59], a[64:67]\n"
    "ds_read_b64_tr_b16 a[200:201], v13 offset:33024\n"
    "v_accvgpr_read_b32 v94, a14\n"
    "v_mfma_f32_16x16x32_bf16 a[64:67], a[232:235], v[44:47], a[64:67]\n"
    "ds_read_b64_tr_b16 a[202:203], v13 offset:41216\n"
    "v_accvgpr_read_b32 v95, a15\n"
    "v_mfma_f32_16x16x32_bf16 a[60:63], a[232:235], v[40:43], a[60:63]\n"
    "ds_read_b64_tr_b16 a[204:205], v13 offset:49408\n"
    "v_cvt_pk_bf16_f32 v64, v88, v89\n"
    "v_mfma_f32_16x16x32_bf16 a[60:63], a[236:239], v[52:55], a[60:63]\n"
    "s_waitcnt lgkmcnt(11)\n"
    "ds_read_b64_tr_b16 a[206:207], v13 offset:57600\n"
    "v_cvt_pk_bf16_f32 v65, v90, v91\n"
    "v_mfma_f32_16x16x32_bf16 a[72:75], a[240:243], v[40:43], a[72:75]\n"
    "v_cvt_pk_bf16_f32 v66, v92, v93\n"
    "v_cvt_pk_bf16_f32 v67, v94, v95\n"
    "v_mfma_f32_16x16x32_bf16 a[72:75], a[244:247], v[52:55], a[72:75]\n"
    "v_accvgpr_read_b32 v96, a4\n"
    "v_accvgpr_read_b32 v97, a5\n"
    "v_mfma_f32_16x16x32_bf16 a[76:79], a[244:247], v[56:59], a[76:79]\n"
    "ds_read_b64_tr_b16 a[208:209], v14 offset:33024\n"
    "v_accvgpr_read_b32 v98, a6\n"
    "v_mfma_f32_16x16x32_bf16 a[76:79], a[240:243], v[44:47], a[76:79]\n"
    "ds_read_b64_tr_b16 a[210:211], v14 offset:41216\n"
    "v_accvgpr_read_b32 v99, a7\n"
    "v_mfma_f32_16x16x32_bf16 a[80:83], a[240:243], v[48:51], a[80:83]\n"
    "ds_read_b64_tr_b16 a[212:213], v14 offset:49408\n"
    "v_accvgpr_read_b32 v100, a16\n"
    "v_mfma_f32_16x16x32_bf16 a[80:83], a[244:247], v[60:63], a[80:83]\n"
    "s_waitcnt lgkmcnt(11)\n"
    "ds_read_b64_tr_b16 a[214:215], v14 offset:57600\n"
    "v_accvgpr_read_b32 v101, a17\n"
    "v_mfma_f32_16x16x32_bf16 a[92:95], a[248:251], v[48:51], a[92:95]\n"
    "v_accvgpr_read_b32 v102, a18\n"
    "v_accvgpr_read_b32 v103, a19\n"
    "v_mfma_f32_16x16x32_bf16 a[92:95], a[252:255], v[60:63], a[92:95]\n"
    "v_cvt_pk_bf16_f32 v68, v96, v97\n"
    "v_cvt_pk_bf16_f32 v69, v98, v99\n"
    "v_mfma_f32_16x16x32_bf16 a[88:91], a[252:255], v[56:59], a[88:91]\n"
    "ds_read_b64_tr_b16 a[216:217], v15 offset:33024\n"
    "v_cvt_pk_bf16_f32 v70, v100, v101\n"
    "v_mfma_f32_16x16x32_bf16 a[88:91], a[248:251], v[44:47], a[88:91]\n"
    "ds_read_b64_tr_b16 a[218:219], v15 offset:41216\n"
    "v_cvt_pk_bf16_f32 v71, v102, v103\n"
    "v_mfma_f32_16x16x32_bf16 a[84:87], a[248:251], v[40:43], a[84:87]\n"
    "ds_read_b64_tr_b16 a[220:221], v15 offset:49408\n"
    "v_accvgpr_read_b32 v104, a8\n"
    "v_mfma_f32_16x16x32_bf16 a[84:87], a[252:255], v[52:55], a[84:87]\n"
    "s_waitcnt lgkmcnt(11)\n"
    "ds_read_b64_tr_b16 a[222:223], v15 offset:57600\n"
    "v_accvgpr_read_b32 v105, a9\n"
    "v_mfma_f32_16x16x32_bf16 a[96:99], a[192:195], v[40:43], a[96:99]\n"
    "v_accvgpr_read_b32 v106, a10\n"
    "v_accvgpr_read_b32 v107, a11\n"
    "v_mfma_f32_16x16x32_bf16 a[96:99], a[196:199], v[52:55], a[96:99]\n"
    "v_accvgpr_read_b32 v108, a20\n"
    "v_accvgpr_read_b32 v109, a21\n"
    "v_mfma_f32_16x16x32_bf16 a[100:103], a[196:199], v[56:59], a[100:103]\n"
    "ds_read_b64_tr_b16 a[224:225], v16 offset:33024\n"
    "v_accvgpr_read_b32 v110, a22\n"
    "v_mfma_f32_16x16x32_bf16 a[100:103], a[192:195], v[44:47], a[100:103]\n"
    "ds_read_b64_tr_b16 a[226:227], v16 offset:41216\n"
    "v_accvgpr_read_b32 v111, a23\n"
    "v_mfma_f32_16x16x32_bf16 a[104:107], a[192:195], v[48:51], a[104:107]\n"
    "ds_read_b64_tr_b16 a[228:229], v16 offset:49408\n"
    "v_cvt_pk_bf16_f32 v72, v104, v105\n"
    "v_mfma_f32_16x16x32_bf16 a[104:107], a[196:199], v[60:63], a[104:107]\n"
    "s_waitcnt lgkmcnt(11)\n"
    "ds_read_b64_tr_b16 a[230:231], v16 offset:57600\n"
    "v_cvt_pk_bf16_f32 v73, v106, v107\n"
    "v_mfma_f32_16x16x32_bf16 a[116:119], a[200:203], v[48:51], a[116:119]\n"
    "v_cvt_pk_bf16_f32 v74, v108, v109\n"
    "v_cvt_pk_bf16_f32 v75, v110, v111\n"
    "v_mfma_f32_16x16x32_bf16 a[116:119], a[204:207], v[60:63], a[116:119]\n"
    "v_accvgpr_read_b32 v88, a24\n"
    "v_accvgpr_read_b32 v89, a25\n"
    "v_mfma_f32_16x16x32_bf16 a[112:115], a[204:207], v[56:59], a[112:115]\n"
    "ds_read_b64_tr_b16 a[232:233], v17 offset:33024\n"
    "v_accvgpr_read_b32 v90, a26\n"
    "v_mfma_f32_16x16x32_bf16 a[112:115], a[200:203], v[44:47], a[112:115]\n"
    "ds_read_b64_tr_b16 a[234:235], v17 offset:41216\n"
    "v_accvgpr_read_b32 v91, a27\n"
    "v_mfma_f32_16x16x32_bf16 a[108:111], a[200:203], v[40:43], a[108:111]\n"
    "ds_read_b64_tr_b16 a[236:237], v17 offset:49408\n"
    "v_accvgpr_read_b32 v92, a36\n"
    "v_mfma_f32_16x16x32_bf16 a[108:111], a[204:207], v[52:55], a[108:111]\n"
    "s_waitcnt lgkmcnt(11)\n"
    "ds_read_b64_tr_b16 a[238:239], v17 offset:57600\n"
    "v_accvgpr_read_b32 v93, a37\n"
    "v_mfma_f32_16x16x32_bf16 a[120:123], a[208:211], v[40:43], a[120:123]\n"
    "v_accvgpr_read_b32 v94, a38\n"
    "v_accvgpr_read_b32 v95, a39\n"
    "v_mfma_f32_16x16x32_bf16 a[120:123], a[212:215], v[52:55], a[120:123]\n"
    "v_cvt_pk_bf16_f32 v76, v88, v89\n"
    "v_cvt_pk_bf16_f32 v77, v90, v91\n"
    "v_mfma_f32_16x16x32_bf16 a[124:127], a[212:215], v[56:59], a[124:127]\n"
    "ds_read_b64_tr_b16 a[240:241], v18 offset:33024\n"
    "v_cvt_pk_bf16_f32 v78, v92, v93\n"
    "v_mfma_f32_16x16x32_bf16 a[124:127], a[208:211], v[44:47], a[124:127]\n"
    "ds_read_b64_tr_b16 a[242:243], v18 offset:41216\n"
    "v_cvt_pk_bf16_f32 v79, v94, v95\n"
    "v_mfma_f32_16x16x32_bf16 a[128:131], a[208:211], v[48:51], a[128:131]\n"
    "ds_read_b64_tr_b16 a[244:245], v18 offset:49408\n"
    "v_accvgpr_read_b32 v96, a28\n"
    "v_mfma_f32_16x16x32_bf16 a[128:131], a[212:215], v[60:63], a[128:131]\n"
    "s_waitcnt lgkmcnt(11)\n"
    "ds_read_b64_tr_b16 a[246:247], v18 offset:57600\n"
    "v_accvgpr_read_b32 v97, a29\n"
    "v_mfma_f32_16x16x32_bf16 a[140:143], a[216:219], v[48:51], a[140:143]\n"
    "v_accvgpr_read_b32 v98, a30\n"
    "v_accvgpr_read_b32 v99, a31\n"
    "v_mfma_f32_16x16x32_bf16 a[140:143], a[220:223], v[60:63], a[140:143]\n"
    "v_accvgpr_read_b32 v100, a40\n"
    "v_accvgpr_read_b32 v101, a41\n"
    "v_mfma_f32_16x16x32_bf16 a[136:139], a[220:223], v[56:59], a[136:139]\n"
    "ds_read_b64_tr_b16 a[248:249], v19 offset:33024\n"
    "v_accvgpr_read_b32 v102, a42\n"
    "v_mfma_f32_16x16x32_bf16 a[136:139], a[216:219], v[44:47], a[136:139]\n"
    "ds_read_b64_tr_b16 a[250:251], v19 offset:41216\n"
    "v_accvgpr_read_b32 v103, a43\n"
    "v_mfma_f32_16x16x32_bf16 a[132:135], a[216:219], v[40:43], a[132:135]\n"
    "ds_read_b64_tr_b16 a[252:253], v19 offset:49408\n"
    "v_cvt_pk_bf16_f32 v80, v96, v97\n"
    "v_mfma_f32_16x16x32_bf16 a[132:135], a[220:223], v[52:55], a[132:135]\n"
    "s_waitcnt lgkmcnt(11)\n"
    "ds_read_b64_tr_b16 a[254:255], v19 offset:57600\n"
    "v_cvt_pk_bf16_f32 v81, v98, v99\n"
    "v_mfma_f32_16x16x32_bf16 a[144:147], a[224:227], v[40:43], a[144:147]\n"
    "v_cvt_pk_bf16_f32 v82, v100, v101\n"
    "v_cvt_pk_bf16_f32 v83, v102, v103\n"
    "v_mfma_f32_16x16x32_bf16 a[144:147], a[228:231], v[52:55], a[144:147]\n"
    "v_accvgpr_read_b32 v104, a32\n"
    "v_accvgpr_read_b32 v105, a33\n"
    "v_mfma_f32_16x16x32_bf16 a[148:151], a[228:231], v[56:59], a[148:151]\n"
    "v_accvgpr_read_b32 v106, a34\n"
    "v_accvgpr_read_b32 v107, a35\n"
    "v_mfma_f32_16x16x32_bf16 a[148:151], a[224:227], v[44:47], a[148:151]\n"
    "v_accvgpr_read_b32 v108, a44\n"
    "v_accvgpr_read_b32 v109, a45\n"
    "v_mfma_f32_16x16x32_bf16 a[152:155], a[224:227], v[48:51], a[152:155]\n"
    "v_accvgpr_read_b32 v110, a46\n"
    "v_accvgpr_read_b32 v111, a47\n"
    "v_mfma_f32_16x16x32_bf16 a[152:155], a[228:231], v[60:63], a[152:155]\n"
    "s_waitcnt lgkmcnt(0)\n"
    "s_barrier\n"
    "v_cvt_pk_bf16_f32 v84, v104, v105\n"
    "v_mfma_f32_16x16x32_bf16 a[164:167], a[232:235], v[48:51], a[164:167]\n"
    "ds_read_b64_tr_b16 a[192:193], v20 offset:0\n"
    "v_cvt_pk_bf16_f32 v85, v106, v107\n"
    "v_mfma_f32_16x16x32_bf16 a[164:167], a[236:239], v[60:63], a[164:167]\n"
    "ds_read_b64_tr_b16 a[194:195], v20 offset:2048\n"
    "v_cvt_pk_bf16_f32 v86, v108, v109\n"
    "v_mfma_f32_16x16x32_bf16 a[160:163], a[236:239], v[56:59], a[160:163]\n"
    "ds_read_b64_tr_b16 a[196:197], v20 offset:4096\n"
    "v_cvt_pk_bf16_f32 v87, v110, v111\n"
    "v_mfma_f32_16x16x32_bf16 a[160:163], a[232:235], v[44:47], a[160:163]\n"
    "ds_read_b64_tr_b16 a[198:199], v20 offset:6144\n"
    "v_accvgpr_read_b32 v88, a48\n"
    "v_mfma_f32_16x16x32_bf16 a[156:159], a[232:235], v[40:43], a[156:159]\n"
    "ds_read_b64_tr_b16 a[200:201], v20 offset:8192\n"
    "v_accvgpr_read_b32 v89, a49\n"
    "v_mfma_f32_16x16x32_bf16 a[156:159], a[236:239], v[52:55], a[156:159]\n"
    "ds_read_b64_tr_b16 a[202:203], v20 offset:10240\n"
    "v_accvgpr_read_b32 v90, a50\n"
    "v_mfma_f32_16x16x32_bf16 a[168:171], a[240:243], v[40:43], a[168:171]\n"
    "ds_read_b64_tr_b16 a[204:205], v20 offset:12288\n"
    "v_accvgpr_read_b32 v91, a51\n"
    "v_mfma_f32_16x16x32_bf16 a[168:171], a[244:247], v[52:55], a[168:171]\n"
    "ds_read_b64_tr_b16 a[206:207], v20 offset:14336\n"
    "v_accvgpr_read_b32 v92, a60\n"
    "v_mfma_f32_16x16x32_bf16 a[172:175], a[244:247], v[56:59], a[172:175]\n"
    "ds_read_b64_tr_b16 a[208:209], v20 offset:16384\n"
    "v_accvgpr_read_b32 v93, a61\n"
    "v_mfma_f32_16x16x32_bf16 a[172:175], a[240:243], v[44:47], a[172:175]\n"
    "ds_read_b64_tr_b16 a[210:211], v20 offset:18432\n"
    "v_accvgpr_read_b32 v94, a62\n"
    "v_mfma_f32_16x16x32_bf16 a[176:179], a[240:243], v[48:51], a[176:179]\n"
    "ds_read_b64_tr_b16 a[212:213], v20 offset:20480\n"
    "v_accvgpr_read_b32 v95, a63\n"
    "v_mfma_f32_16x16x32_bf16 a[176:179], a[244:247], v[60:63], a[176:179]\n"
    "ds_read_b64_tr_b16 a[214:215], v20 offset:22528\n"
    "v_cvt_pk_bf16_f32 v112, v88, v89\n"
    "v_mfma_f32_16x16x32_bf16 a[188:191], a[248:251], v[48:51], a[188:191]\n"
    "ds_read_b64_tr_b16 a[216:217], v20 offset:24576\n"
    "v_cvt_pk_bf16_f32 v113, v90, v91\n"
    "v_mfma_f32_16x16x32_bf16 a[188:191], a[252:255], v[60:63], a[188:191]\n"
    "ds_read_b64_tr_b16 a[218:219], v20 offset:26624\n"
    "v_cvt_pk_bf16_f32 v114, v92, v93\n"
    "v_mfma_f32_16x16x32_bf16 a[184:187], a[252:255], v[56:59], a[184:187]\n"
    "ds_read_b64_tr_b16 a[220:221], v20 offset:28672\n"
    "v_cvt_pk_bf16_f32 v115, v94, v95\n"
    "v_mfma_f32_16x16x32_bf16 a[184:187], a[248:251], v[44:47], a[184:187]\n"
    "ds_read_b64_tr_b16 a[222:223], v20 offset:30720\n"
    "v_accvgpr_read_b32 v96, a52\n"
    "v_mfma_f32_16x16x32_bf16 a[180:183], a[248:251], v[40:43], a[180:183]\n"
    "v_accvgpr_read_b32 v97, a53\n"
    "v_accvgpr_read_b32 v98, a54\n"
    "v_mfma_f32_16x16x32_bf16 a[180:183], a[252:255], v[52:55], a[180:183]\n"
    "s_waitcnt lgkmcnt(12)\n"
    "v_accvgpr_read_b32 v99, a55\n"
    "v_accvgpr_read_b32 v100, a64\n"
    "v_add_co_u32 v38, vcc, 1, v38\n"
    "s_nop 1\n"
    "v_addc_co_u32 v39, vcc, 0, v39, vcc\n"
    "v_or_b32 v39, 0x80000000, v39\n"
    "v_accvgpr_read_b32 v101, a65\n"
    "v_accvgpr_read_b32 v102, a66\n"
    "v_accvgpr_read_b32 v103, a67\n"
    "v_cvt_pk_bf16_f32 v116, v96, v97\n"
    "v_cvt_pk_bf16_f32 v117, v98, v99\n"
    "v_cvt_pk_bf16_f32 v118, v100, v101\n"
    "v_cvt_pk_bf16_f32 v119, v102, v103\n"
    "v_accvgpr_read_b32 v104, a56\n"
    "v_accvgpr_read_b32 v105, a57\n"
    "v_accvgpr_read_b32 v106, a58\n"
    "v_accvgpr_read_b32 v107, a59\n"
    "v_accvgpr_read_b32 v108, a68\n"
    "v_accvgpr_read_b32 v109, a69\n"
    "v_accvgpr_read_b32 v110, a70\n"
    "v_accvgpr_read_b32 v111, a71\n"
    "v_cvt_pk_bf16_f32 v120, v104, v105\n"
    "v_cvt_pk_bf16_f32 v121, v106, v107\n"
    "v_cvt_pk_bf16_f32 v122, v108, v109\n"
    "v_cvt_pk_bf16_f32 v123, v110, v111\n"
    "v_accvgpr_read_b32 v88, a72\n"
    "v_accvgpr_read_b32 v89, a73\n"
    "v_accvgpr_read_b32 v90, a74\n"
    "v_accvgpr_read_b32 v91, a75\n"
    "v_accvgpr_read_b32 v92, a84\n"
    "v_accvgpr_read_b32 v93, a85\n"
    "v_accvgpr_read_b32 v94, a86\n"
    "v_accvgpr_read_b32 v95, a87\n"
    "v_cvt_pk_bf16_f32 v124, v88, v89\n"
    "v_cvt_pk_bf16_f32 v125, v90, v91\n"
    "v_cvt_pk_bf16_f32 v126, v92, v93\n"
    "v_cvt_pk_bf16_f32 v127, v94, v95\n"
    "v_accvgpr_read_b32 v96, a76\n"
    "v_accvgpr_read_b32 v97, a77\n"
    "v_accvgpr_read_b32 v98, a78\n"
    "v_accvgpr_read_b32 v99, a79\n"
    "v_accvgpr_read_b32 v100, a88\n"
    "v_accvgpr_read_b32 v101, a89\n"
    "v_accvgpr_read_b32 v102, a90\n"
    "v_accvgpr_read_b32 v103, a91\n"
    "v_cvt_pk_bf16_f32 v128, v96, v97\n"
    "v_cvt_pk_bf16_f32 v129, v98, v99\n"
    "v_cvt_pk_bf16_f32 v130, v100, v101\n"
    "v_cvt_pk_bf16_f32 v131, v102, v103\n"
    "v_accvgpr_read_b32 v104, a80\n"
    "v_accvgpr_read_b32 v105, a81\n"
    "v_accvgpr_read_b32 v106, a82\n"
    "v_accvgpr_read_b32 v107, a83\n"
    "v_accvgpr_read_b32 v108, a92\n"
    "v_accvgpr_read_b32 v109, a93\n"
    "v_accvgpr_read_b32 v110, a94\n"
    "v_accvgpr_read_b32 v111, a95\n"
    "v_cvt_pk_bf16_f32 v132, v104, v105\n"
    "v_cvt_pk_bf16_f32 v133, v106, v107\n"
    "v_cvt_pk_bf16_f32 v134, v108, v109\n"
    "v_cvt_pk_bf16_f32 v135, v110, v111\n"
    "v_accvgpr_read_b32 v88, a96\n"
    "v_accvgpr_read_b32 v89, a97\n"
    "v_accvgpr_read_b32 v90, a98\n"
    "v_accvgpr_read_b32 v91, a99\n"
    "v_accvgpr_read_b32 v92, a108\n"
    "v_accvgpr_read_b32 v93, a109\n"
    "v_accvgpr_read_b32 v94, a110\n"
    "v_accvgpr_read_b32 v95, a111\n"
    "v_cvt_pk_bf16_f32 v160, v88, v89\n"
    "v_cvt_pk_bf16_f32 v161, v90, v91\n"
    "v_cvt_pk_bf16_f32 v162, v92, v93\n"
    "v_cvt_pk_bf16_f32 v163, v94, v95\n"
    "v_accvgpr_read_b32 v96, a100\n"
    "v_accvgpr_read_b32 v97, a101\n"
    "v_accvgpr_read_b32 v98, a102\n"
    "v_accvgpr_read_b32 v99, a103\n"
    "v_accvgpr_read_b32 v100, a112\n"
    "v_accvgpr_read_b32 v101, a113\n"
    "v_accvgpr_read_b32 v102, a114\n"
    "v_accvgpr_read_b32 v103, a115\n"
    "v_cvt_pk_bf16_f32 v164, v96, v97\n"
    "v_cvt_pk_bf16_f32 v165, v98, v99\n"
    "v_cvt_pk_bf16_f32 v166, v100, v101\n"
    "v_cvt_pk_bf16_f32 v167, v102, v103\n"
    "v_accvgpr_read_b32 v104, a104\n"
    "v_accvgpr_read_b32 v105, a105\n"
    "v_accvgpr_read_b32 v106, a106\n"
    "v_accvgpr_read_b32 v107, a107\n"
    "v_accvgpr_read_b32 v108, a116\n"
    "v_accvgpr_read_b32 v109, a117\n"
    "v_accvgpr_read_b32 v110, a118\n"
    "v_accvgpr_read_b32 v111, a119\n"
    "v_cvt_pk_bf16_f32 v168, v104, v105\n"
    "v_cvt_pk_bf16_f32 v169, v106, v107\n"
    "v_cvt_pk_bf16_f32 v170, v108, v109\n"
    "v_cvt_pk_bf16_f32 v171, v110, v111\n"
    "v_accvgpr_read_b32 v88, a120\n"
    "v_accvgpr_read_b32 v89, a121\n"
    "v_accvgpr_read_b32 v90, a122\n"
    "v_accvgpr_read_b32 v91, a123\n"
    "v_accvgpr_read_b32 v92, a132\n"
    "v_accvgpr_read_b32 v93, a133\n"
    "v_accvgpr_read_b32 v94, a134\n"
    "v_accvgpr_read_b32 v95, a135\n"
    "v_cvt_pk_bf16_f32 v172, v88, v89\n"
    "v_cvt_pk_bf16_f32 v173, v90, v91\n"
    "v_cvt_pk_bf16_f32 v174, v92, v93\n"
    "v_cvt_pk_bf16_f32 v175, v94, v95\n"
    "v_accvgpr_read_b32 v96, a124\n"
    "v_accvgpr_read_b32 v97, a125\n"
    "v_accvgpr_read_b32 v98, a126\n"
    "v_accvgpr_read_b32 v99, a127\n"
    "v_accvgpr_read_b32 v100, a136\n"
    "v_accvgpr_read_b32 v101, a137\n"
    "v_accvgpr_read_b32 v102, a138\n"
    "v_accvgpr_read_b32 v103, a139\n"
    "v_cvt_pk_bf16_f32 v176, v96, v97\n"
    "v_cvt_pk_bf16_f32 v177, v98, v99\n"
    "v_cvt_pk_bf16_f32 v178, v100, v101\n"
    "v_cvt_pk_bf16_f32 v179, v102, v103\n"
    "v_accvgpr_read_b32 v104, a128\n"
    "v_accvgpr_read_b32 v105, a129\n"
    "v_accvgpr_read_b32 v106, a130\n"
    "v_accvgpr_read_b32 v107, a131\n"
    "v_accvgpr_read_b32 v108, a140\n"
    "v_accvgpr_read_b32 v109, a141\n"
    "v_accvgpr_read_b32 v110, a142\n"
    "v_accvgpr_read_b32 v111, a143\n"
    "v_cvt_pk_bf16_f32 v180, v104, v105\n"
    "v_cvt_pk_bf16_f32 v181, v106, v107\n"
    "v_cvt_pk_bf16_f32 v182, v108, v109\n"
    "v_cvt_pk_bf16_f32 v183, v110, v111\n"
    "v_accvgpr_read_b32 v88, a144\n"
    "v_accvgpr_read_b32 v89, a145\n"
    "v_accvgpr_read_b32 v90, a146\n"
    "v_accvgpr_read_b32 v91, a147\n"
    "v_accvgpr_read_b32 v92, a156\n"
    "v_accvgpr_read_b32 v93, a157\n"
    "v_accvgpr_read_b32 v94, a158\n"
    "v_accvgpr_read_b32 v95, a159\n"
    "v_cvt_pk_bf16_f32 v208, v88, v89\n"
    "v_cvt_pk_bf16_f32 v209, v90, v91\n"
    "v_cvt_pk_bf16_f32 v210, v92, v93\n"
    "v_cvt_pk_bf16_f32 v211, v94, v95\n"
    "v_accvgpr_read_b32 v96, a148\n"
    "v_accvgpr_read_b32 v97, a149\n"
    "v_accvgpr_read_b32 v98, a150\n"
    "v_accvgpr_read_b32 v99, a151\n"
    "v_accvgpr_read_b32 v100, a160\n"
    "v_accvgpr_read_b32 v101, a161\n"
    "v_accvgpr_read_b32 v102, a162\n"
    "v_accvgpr_read_b32 v103, a163\n"
    "v_cvt_pk_bf16_f32 v212, v96, v97\n"
    "v_cvt_pk_bf16_f32 v213, v98, v99\n"
    "v_cvt_pk_bf16_f32 v214, v100, v101\n"
    "v_cvt_pk_bf16_f32 v215, v102, v103\n"
    "v_accvgpr_read_b32 v104, a152\n"
    "v_accvgpr_read_b32 v105, a153\n"
    "v_accvgpr_read_b32 v106, a154\n"
    "v_accvgpr_read_b32 v107, a155\n"
    "v_accvgpr_read_b32 v108, a164\n"
    "v_accvgpr_read_b32 v109, a165\n"
    "v_accvgpr_read_b32 v110, a166\n"
    "v_accvgpr_read_b32 v111, a167\n"
    "v_cvt_pk_bf16_f32 v216, v104, v105\n"
    "v_cvt_pk_bf16_f32 v217, v106, v107\n"
    "v_cvt_pk_bf16_f32 v218, v108, v109\n"
    "v_cvt_pk_bf16_f32 v219, v110, v111\n"
    "v_accvgpr_read_b32 v88, a168\n"
    "v_accvgpr_read_b32 v89, a169\n"
    "v_accvgpr_read_b32 v90, a170\n"
    "v_accvgpr_read_b32 v91, a171\n"
    "v_accvgpr_read_b32 v92, a180\n"
    "v_accvgpr_read_b32 v93, a181\n"
    "v_accvgpr_read_b32 v94, a182\n"
    "v_accvgpr_read_b32 v95, a183\n"
    "v_cvt_pk_bf16_f32 v220, v88, v89\n"
    "v_cvt_pk_bf16_f32 v221, v90, v91\n"
    "v_cvt_pk_bf16_f32 v222, v92, v93\n"
    "v_cvt_pk_bf16_f32 v223, v94, v95\n"
    "v_accvgpr_read_b32 v96, a172\n"
    "v_accvgpr_read_b32 v97, a173\n"
    "v_accvgpr_read_b32 v98, a174\n"
    "v_accvgpr_read_b32 v99, a175\n"
    "v_accvgpr_read_b32 v100, a184\n"
    "v_accvgpr_read_b32 v101, a185\n"
    "v_accvgpr_read_b32 v102, a186\n"
    "v_accvgpr_read_b32 v103, a187\n"
    "v_cvt_pk_bf16_f32 v224, v96, v97\n"
    "v_cvt_pk_bf16_f32 v225, v98, v99\n"
    "v_cvt_pk_bf16_f32 v226, v100, v101\n"
    "v_cvt_pk_bf16_f32 v227, v102, v103\n"
    "v_accvgpr_read_b32 v104, a176\n"
    "v_accvgpr_read_b32 v105, a177\n"
    "v_accvgpr_read_b32 v106, a178\n"
    "v_accvgpr_read_b32 v107, a179\n"
    "v_accvgpr_read_b32 v108, a188\n"
    "v_accvgpr_read_b32 v109, a189\n"
    "v_accvgpr_read_b32 v110, a190\n"
    "v_accvgpr_read_b32 v111, a191\n"
    "v_cvt_pk_bf16_f32 v228, v104, v105\n"
    "v_cvt_pk_bf16_f32 v229, v106, v107\n"
    "v_cvt_pk_bf16_f32 v230, v108, v109\n"
    "v_cvt_pk_bf16_f32 v231, v110, v111\n"
    "Lo_%=:\n"
    "v_mfma_f32_16x16x32_bf16 v[88:91], a[192:195], v[64:67], 0\n"
    "s_waitcnt lgkmcnt(10)\n"
    "v_mfma_f32_16x16x32_bf16 v[88:91], a[196:199], v[76:79], v[88:91]\n"
    "v_mfma_f32_16x16x32_bf16 v[88:91], a[200:203], v[112:115], v[88:91]\n"
    "s_waitcnt lgkmcnt(6)\n"
    "ds_read_b64_tr_b16 a[252:253], v21 offset:28672\n"
    "v_mfma_f32_16x16x32_bf16 v[88:91], a[204:207], v[124:127], v[88:91]\n"
    "ds_read_b64_tr_b16 a[254:255], v21 offset:30720\n"
    "v_mul_f32 v156, v156, v35\n"
    "v_mfma_f32_16x16x32_bf16 v[88:91], a[208:211], v[160:163], v[88:91]\n"
    "s_waitcnt lgkmcnt(4)\n"
    "ds_read_b64_tr_b16 a[248:249], v21 offset:24576\n"
    "v_mul_f32 v157, v157, v35\n"
    "v_mfma_f32_16x16x32_bf16 v[88:91], a[212:215], v[172:175], v[88:91]\n"
    "ds_read_b64_tr_b16 a[250:251], v21 offset:26624\n"
    "v_mul_f32 v158, v158, v35\n"
    "v_mfma_f32_16x16x32_bf16 v[88:91], a[216:219], v[208:211], v[88:91]\n"
    "s_waitcnt lgkmcnt(4)\n"
    "ds_read_b64_tr_b16 a[244:245], v21 offset:20480\n"
    "v_mul_f32 v159, v159, v35\n"
    "v_mfma_f32_16x16x32_bf16 v[88:91], a[220:223], v[220:223], v[88:91]\n"
    "ds_read_b64_tr_b16 a[246:247], v21 offset:22528\n"
    "v_cvt_pk_bf16_f32 v50, v156, v157\n"
    "v_mfma_f32_16x16x32_bf16 v[92:95], a[220:223], v[224:227], 0\n"
    "ds_read_b64_tr_b16 a[240:241], v21 offset:16384\n"
    "v_cvt_pk_bf16_f32 v51, v158, v159\n"
    "v_mfma_f32_16x16x32_bf16 v[92:95], a[216:219], v[212:215], v[92:95]\n"
    "ds_read_b64_tr_b16 a[242:243], v21 offset:18432\n"
    "v_mfma_f32_16x16x32_bf16 v[92:95], a[212:215], v[176:179], v[92:95]\n"
    "ds_read_b64_tr_b16 a[236:237], v21 offset:12288\n"
    "v_mfma_f32_16x16x32_bf16 v[92:95], a[208:211], v[164:167], v[92:95]\n"
    "ds_read_b64_tr_b16 a[238:239], v21 offset:14336\n"
    "v_mul_f32 v88, v88, v33\n"
    "v_mfma_f32_16x16x32_bf16 v[92:95], a[204:207], v[128:131], v[92:95]\n"
    "ds_read_b64_tr_b16 a[232:233], v21 offset:8192\n"
    "v_mul_f32 v89, v89, v33\n"
    "v_mfma_f32_16x16x32_bf16 v[92:95], a[200:203], v[116:119], v[92:95]\n"
    "ds_read_b64_tr_b16 a[234:235], v21 offset:10240\n"
    "v_mul_f32 v90, v90, v33\n"
    "v_mfma_f32_16x16x32_bf16 v[92:95], a[196:199], v[80:83], v[92:95]\n"
    "ds_read_b64_tr_b16 a[228:229], v21 offset:4096\n"
    "v_mul_f32 v91, v91, v33\n"
    "v_mfma_f32_16x16x32_bf16 v[92:95], a[192:195], v[68:71], v[92:95]\n"
    "buffer_store_dwordx4 v[48:51], v37, s[28:31], s66 offen offset:2048 sc1\n"
    "s_mul_i32 s66, s68, 9216\n"
    "v_mfma_f32_16x16x32_bf16 v[96:99], a[192:195], v[72:75], 0\n"
    "s_add_u32 s66, s66, s72\n"
    "ds_read_b64_tr_b16 a[230:231], v21 offset:6144\n"
    "s_add_u32 s68, s68, 1\n"
    "v_mfma_f32_16x16x32_bf16 v[96:99], a[196:199], v[84:87], v[96:99]\n"
    "s_cmp_eq_u32 s68, 14\n"
    "s_cselect_b32 s68, 0, s68\n"
    "ds_read_b64_tr_b16 a[224:225], v21 offset:0\n"
    "v_mfma_f32_16x16x32_bf16 v[96:99], a[200:203], v[120:123], v[96:99]\n"
    "s_waitcnt vmcnt(8)\n"
    "ds_read_b64_tr_b16 a[226:227], v21 offset:2048\n"
    "v_mov_b32 v37, v36\n"
    "v_mfma_f32_16x16x32_bf16 v[96:99], a[204:207], v[132:135], v[96:99]\n"
    "v_cvt_pk_bf16_f32 v40, v88, v89\n"
    "v_cvt_pk_bf16_f32 v41, v90, v91\n"
    "v_mfma_f32_16x16x32_bf16 v[96:99], a[208:211], v[168:171], v[96:99]\n"
    "v_mul_f32 v92, v92, v34\n"
    "v_mul_f32 v93, v93, v34\n"
    "v_mfma_f32_16x16x32_bf16 v[96:99], a[212:215], v[180:183], v[96:99]\n"
    "v_mul_f32 v94, v94, v34\n"
    "v_mul_f32 v95, v95, v34\n"
    "v_mfma_f32_16x16x32_bf16 v[96:99], a[216:219], v[216:219], v[96:99]\n"
    "v_cvt_pk_bf16_f32 v44, v92, v93\n"
    "v_cvt_pk_bf16_f32 v45, v94, v95\n"
    "v_mfma_f32_16x16x32_bf16 v[96:99], a[220:223], v[228:231], v[96:99]\n"
    "s_waitcnt lgkmcnt(12)\n"
    "buffer_store_dwordx2 v[38:39], v24, s[36:39], 0 offen sc1\n"
    "v_add_u32 v24, 0x180, v24\n"
    "v_mfma_f32_16x16x32_bf16 v[100:103], a[252:255], v[220:223], 0\n"
    "v_mfma_f32_16x16x32_bf16 v[100:103], a[248:251], v[208:211], v[100:103]\n"
    "s_waitcnt lgkmcnt(8)\n"
    "v_mfma_f32_16x16x32_bf16 v[100:103], a[244:247], v[172:175], v[100:103]\n"
    "ds_read_b64_tr_b16 a[192:193], v22 offset:0\n"
    "v_mfma_f32_16x16x32_bf16 v[100:103], a[240:243], v[160:163], v[100:103]\n"
    "s_waitcnt lgkmcnt(5)\n"
    "ds_read_b64_tr_b16 a[194:195], v22 offset:2048\n"
    "v_mul_f32 v96, v96, v35\n"
    "v_mfma_f32_16x16x32_bf16 v[100:103], a[236:239], v[124:127], v[100:103]\n"
    "ds_read_b64_tr_b16 a[196:197], v22 offset:4096\n"
    "v_mul_f32 v97, v97, v35\n"
    "v_mfma_f32_16x16x32_bf16 v[100:103], a[232:235], v[112:115], v[100:103]\n"
    "s_waitcnt lgkmcnt(3)\n"
    "ds_read_b64_tr_b16 a[198:199], v22 offset:6144\n"
    "v_mul_f32 v98, v98, v35\n"
    "v_mfma_f32_16x16x32_bf16 v[100:103], a[228:231], v[76:79], v[100:103]\n"
    "ds_read_b64_tr_b16 a[200:201], v22 offset:8192\n"
    "v_mul_f32 v99, v99, v35\n"
    "v_mfma_f32_16x16x32_bf16 v[100:103], a[224:227], v[64:67], v[100:103]\n"
    "ds_read_b64_tr_b16 a[202:203], v22 offset:10240\n"
    "v_cvt_pk_bf16_f32 v48, v96, v97\n"
    "v_mfma_f32_16x16x32_bf16 v[104:107], a[224:227], v[68:71], 0\n"
    "ds_read_b64_tr_b16 a[204:205], v22 offset:12288\n"
    "v_cvt_pk_bf16_f32 v49, v98, v99\n"
    "v_mfma_f32_16x16x32_bf16 v[104:107], a[228:231], v[80:83], v[104:107]\n"
    "ds_read_b64_tr_b16 a[206:207], v22 offset:14336\n"
    "v_mfma_f32_16x16x32_bf16 v[104:107], a[232:235], v[116:119], v[104:107]\n"
    "ds_read_b64_tr_b16 a[208:209], v22 offset:16384\n"
    "v_mul_f32 v100, v100, v33\n"
    "v_mfma_f32_16x16x32_bf16 v[104:107], a[236:239], v[128:131], v[104:107]\n"
    "ds_read_b64_tr_b16 a[210:211], v22 offset:18432\n"
    "v_mul_f32 v101, v101, v33\n"
    "v_mfma_f32_16x16x32_bf16 v[104:107], a[240:243], v[164:167], v[104:107]\n"
    "ds_read_b64_tr_b16 a[212:213], v22 offset:20480\n"
    "v_mul_f32 v102, v102, v33\n"
    "v_mfma_f32_16x16x32_bf16 v[104:107], a[244:247], v[176:179], v[104:107]\n"
    "ds_read_b64_tr_b16 a[214:215], v22 offset:22528\n"
    "v_mul_f32 v103, v103, v33\n"
    "v_mfma_f32_16x16x32_bf16 v[104:107], a[248:251], v[212:215], v[104:107]\n"
    "ds_read_b64_tr_b16 a[216:217], v22 offset:24576\n"
    "v_cvt_pk_bf16_f32 v42, v100, v101\n"
    "v_mfma_f32_16x16x32_bf16 v[104:107], a[252:255], v[224:227], v[104:107]\n"
    "ds_read_b64_tr_b16 a[218:219], v22 offset:26624\n"
    "v_cvt_pk_bf16_f32 v43, v102, v103\n"
    "v_mfma_f32_16x16x32_bf16 v[108:111], a[252:255], v[228:231], 0\n"
    "ds_read_b64_tr_b16 a[220:221], v22 offset:28672\n"
    "v_mfma_f32_16x16x32_bf16 v[108:111], a[248:251], v[216:219], v[108:111]\n"
    "ds_read_b64_tr_b16 a[222:223], v22 offset:30720\n"
    "v_mfma_f32_16x16x32_bf16 v[108:111], a[244:247], v[180:183], v[108:111]\n"
    "v_mfma_f32_16x16x32_bf16 v[108:111], a[240:243], v[168:171], v[108:111]\n"
    "v_mul_f32 v104, v104, v34\n"
    "v_mul_f32 v105, v105, v34\n"
    "v_mfma_f32_16x16x32_bf16 v[108:111], a[236:239], v[132:135], v[108:111]\n"
    "v_mul_f32 v106, v106, v34\n"
    "v_mul_f32 v107, v107, v34\n"
    "v_mfma_f32_16x16x32_bf16 v[108:111], a[232:235], v[120:123], v[108:111]\n"
    "v_cvt_pk_bf16_f32 v46, v104, v105\n"
    "v_cvt_pk_bf16_f32 v47, v106, v107\n"
    "v_mfma_f32_16x16x32_bf16 v[108:111], a[228:231], v[84:87], v[108:111]\n"
    "v_mfma_f32_16x16x32_bf16 v[108:111], a[224:227], v[72:75], v[108:111]\n"
    "s_waitcnt lgkmcnt(12)\n"
    "buffer_store_dwordx4 v[40:43], v37, s[28:31], s66 offen offset:0 sc1\n"
    "v_mfma_f32_16x16x32_bf16 v[136:139], a[192:195], v[64:67], 0\n"
    "v_mfma_f32_16x16x32_bf16 v[136:139], a[196:199], v[76:79], v[136:139]\n"
    "s_waitcnt lgkmcnt(8)\n"
    "v_mfma_f32_16x16x32_bf16 v[136:139], a[200:203], v[112:115], v[136:139]\n"
    "ds_read_b64_tr_b16 a[252:253], v23 offset:28672\n"
    "v_mfma_f32_16x16x32_bf16 v[136:139], a[204:207], v[124:127], v[136:139]\n"
    "s_waitcnt lgkmcnt(5)\n"
    "ds_read_b64_tr_b16 a[254:255], v23 offset:30720\n"
    "v_mul_f32 v108, v108, v35\n"
    "v_mfma_f32_16x16x32_bf16 v[136:139], a[208:211], v[160:163], v[136:139]\n"
    "ds_read_b64_tr_b16 a[248:249], v23 offset:24576\n"
    "v_mul_f32 v109, v109, v35\n"
    "v_mfma_f32_16x16x32_bf16 v[136:139], a[212:215], v[172:175], v[136:139]\n"
    "s_waitcnt lgkmcnt(3)\n"
    "ds_read_b64_tr_b16 a[250:251], v23 offset:26624\n"
    "v_mul_f32 v110, v110, v35\n"
    "v_mfma_f32_16x16x32_bf16 v[136:139], a[216:219], v[208:211], v[136:139]\n"
    "ds_read_b64_tr_b16 a[244:245], v23 offset:20480\n"
    "v_mul_f32 v111, v111, v35\n"
    "v_mfma_f32_16x16x32_bf16 v[136:139], a[220:223], v[220:223], v[136:139]\n"
    "buffer_store_dwordx4 v[44:47], v37, s[28:31], s66 offen offset:1024 sc1\n"
    "v_cvt_pk_bf16_f32 v50, v108, v109\n"
    "v_mfma_f32_16x16x32_bf16 v[140:143], a[220:223], v[224:227], 0\n"
    "ds_read_b64_tr_b16 a[246:247], v23 offset:22528\n"
    "v_cvt_pk_bf16_f32 v51, v110, v111\n"
    "v_mfma_f32_16x16x32_bf16 v[140:143], a[216:219], v[212:215], v[140:143]\n"
    "ds_read_b64_tr_b16 a[240:241], v23 offset:16384\n"
    "v_mfma_f32_16x16x32_bf16 v[140:143], a[212:215], v[176:179], v[140:143]\n"
    "ds_read_b64_tr_b16 a[242:243], v23 offset:18432\n"
    "v_mfma_f32_16x16x32_bf16 v[140:143], a[208:211], v[164:167], v[140:143]\n"
    "ds_read_b64_tr_b16 a[236:237], v23 offset:12288\n"
    "v_mul_f32 v136, v136, v33\n"
    "v_mfma_f32_16x16x32_bf16 v[140:143], a[204:207], v[128:131], v[140:143]\n"
    "ds_read_b64_tr_b16 a[238:239], v23 offset:14336\n"
    "v_mul_f32 v137, v137, v33\n"
    "v_mfma_f32_16x16x32_bf16 v[140:143], a[200:203], v[116:119], v[140:143]\n"
    "ds_read_b64_tr_b16 a[232:233], v23 offset:8192\n"
    "v_mul_f32 v138, v138, v33\n"
    "v_mfma_f32_16x16x32_bf16 v[140:143], a[196:199], v[80:83], v[140:143]\n"
    "ds_read_b64_tr_b16 a[234:235], v23 offset:10240\n"
    "v_mul_f32 v139, v139, v33\n"
    "v_mfma_f32_16x16x32_bf16 v[140:143], a[192:195], v[68:71], v[140:143]\n"
    "buffer_store_dwordx4 v[48:51], v37, s[28:31], s66 offen offset:2048 sc1\n"
    "s_mul_i32 s66, s68, 9216\n"
    "v_mfma_f32_16x16x32_bf16 v[144:147], a[192:195], v[72:75], 0\n"
    "s_add_u32 s66, s66, s72\n"
    "ds_read_b64_tr_b16 a[228:229], v23 offset:4096\n"
    "s_add_u32 s68, s68, 1\n"
    "v_mfma_f32_16x16x32_bf16 v[144:147], a[196:199], v[84:87], v[144:147]\n"
    "s_cmp_eq_u32 s68, 14\n"
    "s_cselect_b32 s68, 0, s68\n"
    "ds_read_b64_tr_b16 a[230:231], v23 offset:6144\n"
    "v_mfma_f32_16x16x32_bf16 v[144:147], a[200:203], v[120:123], v[144:147]\n"
    "s_waitcnt vmcnt(8)\n"
    "ds_read_b64_tr_b16 a[224:225], v23 offset:0\n"
    "v_cvt_pk_bf16_f32 v40, v136, v137\n"
    "v_mfma_f32_16x16x32_bf16 v[144:147], a[204:207], v[132:135], v[144:147]\n"
    "ds_read_b64_tr_b16 a[226:227], v23 offset:2048\n"
    "v_add_u32 v20, s69, v20\n"
    "v_mfma_f32_16x16x32_bf16 v[144:147], a[208:211], v[168:171], v[144:147]\n"
    "v_add_u32 v21, s69, v21\n"
    "v_add_u32 v22, s69, v22\n"
    "v_mfma_f32_16x16x32_bf16 v[144:147], a[212:215], v[180:183], v[144:147]\n"
    "v_add_u32 v23, s69, v23\n"
    "v_cvt_pk_bf16_f32 v41, v138, v139\n"
    "v_mfma_f32_16x16x32_bf16 v[144:147], a[216:219], v[216:219], v[144:147]\n"
    "v_mul_f32 v140, v140, v34\n"
    "v_mul_f32 v141, v141, v34\n"
    "v_mfma_f32_16x16x32_bf16 v[144:147], a[220:223], v[228:231], v[144:147]\n"
    "s_waitcnt lgkmcnt(0)\n"
    "s_cmp_gt_u32 s64, 2\n"
    "s_cbranch_scc1 Lnb_%=\n"
    "s_barrier\n"
    "Lnb_%=:\n"
    "v_mfma_f32_16x16x32_bf16 v[148:151], a[252:255], v[220:223], 0\n"
    "v_mul_f32 v142, v142, v34\n"
    "v_mul_f32 v143, v143, v34\n"
    "v_mfma_f32_16x16x32_bf16 v[148:151], a[248:251], v[208:211], v[148:151]\n"
    "v_cvt_pk_bf16_f32 v44, v140, v141\n"
    "v_cvt_pk_bf16_f32 v45, v142, v143\n"
    "v_mfma_f32_16x16x32_bf16 v[148:151], a[244:247], v[172:175], v[148:151]\n"
    "s_bfe_i32 s69, s70, 0x40000\n"
    "ds_read_b64_tr_b16 a[192:193], v20 offset:0\n"
    "s_lshl_b32 s69, s69, 15\n"
    "v_mfma_f32_16x16x32_bf16 v[148:151], a[240:243], v[160:163], v[148:151]\n"
    "s_lshr_b32 s70, s70, 4\n"
    "ds_read_b64_tr_b16 a[194:195], v20 offset:2048\n"
    "s_add_u32 s64, s64, 1\n"
    "v_mfma_f32_16x16x32_bf16 v[148:151], a[236:239], v[124:127], v[148:151]\n"
    "ds_read_b64_tr_b16 a[196:197], v20 offset:4096\n"
    "v_mul_f32 v144, v144, v35\n"
    "v_mfma_f32_16x16x32_bf16 v[148:151], a[232:235], v[112:115], v[148:151]\n"
    "ds_read_b64_tr_b16 a[198:199], v20 offset:6144\n"
    "v_mul_f32 v145, v145, v35\n"
    "v_mfma_f32_16x16x32_bf16 v[148:151], a[228:231], v[76:79], v[148:151]\n"
    "ds_read_b64_tr_b16 a[200:201], v20 offset:8192\n"
    "v_mul_f32 v146, v146, v35\n"
    "v_mfma_f32_16x16x32_bf16 v[148:151], a[224:227], v[64:67], v[148:151]\n"
    "buffer_store_dwordx2 v[38:39], v24, s[36:39], 0 offen sc1\n"
    "v_add_u32 v24, 0x180, v24\n"
    "v_mfma_f32_16x16x32_bf16 v[152:155], a[224:227], v[68:71], 0\n"
    "ds_read_b64_tr_b16 a[202:203], v20 offset:10240\n"
    "v_mul_f32 v147, v147, v35\n"
    "v_mfma_f32_16x16x32_bf16 v[152:155], a[228:231], v[80:83], v[152:155]\n"
    "ds_read_b64_tr_b16 a[204:205], v20 offset:12288\n"
    "v_cvt_pk_bf16_f32 v48, v144, v145\n"
    "v_mfma_f32_16x16x32_bf16 v[152:155], a[232:235], v[116:119], v[152:155]\n"
    "ds_read_b64_tr_b16 a[206:207], v20 offset:14336\n"
    "v_cvt_pk_bf16_f32 v49, v146, v147\n"
    "v_mfma_f32_16x16x32_bf16 v[152:155], a[236:239], v[128:131], v[152:155]\n"
    "ds_read_b64_tr_b16 a[208:209], v20 offset:16384\n"
    "v_mul_f32 v148, v148, v33\n"
    "v_mfma_f32_16x16x32_bf16 v[152:155], a[240:243], v[164:167], v[152:155]\n"
    "ds_read_b64_tr_b16 a[210:211], v20 offset:18432\n"
    "v_mul_f32 v149, v149, v33\n"
    "v_mfma_f32_16x16x32_bf16 v[152:155], a[244:247], v[176:179], v[152:155]\n"
    "ds_read_b64_tr_b16 a[212:213], v20 offset:20480\n"
    "v_mul_f32 v150, v150, v33\n"
    "v_mfma_f32_16x16x32_bf16 v[152:155], a[248:251], v[212:215], v[152:155]\n"
    "ds_read_b64_tr_b16 a[214:215], v20 offset:22528\n"
    "v_mul_f32 v151, v151, v33\n"
    "v_mfma_f32_16x16x32_bf16 v[152:155], a[252:255], v[224:227], v[152:155]\n"
    "ds_read_b64_tr_b16 a[216:217], v20 offset:24576\n"
    "v_cvt_pk_bf16_f32 v42, v148, v149\n"
    "v_mfma_f32_16x16x32_bf16 v[156:159], a[252:255], v[228:231], 0\n"
    "ds_read_b64_tr_b16 a[218:219], v20 offset:26624\n"
    "v_cvt_pk_bf16_f32 v43, v150, v151\n"
    "v_mfma_f32_16x16x32_bf16 v[156:159], a[248:251], v[216:219], v[156:159]\n"
    "ds_read_b64_tr_b16 a[220:221], v20 offset:28672\n"
    "v_mfma_f32_16x16x32_bf16 v[156:159], a[244:247], v[180:183], v[156:159]\n"
    "ds_read_b64_tr_b16 a[222:223], v20 offset:30720\n"
    "v_mul_f32 v152, v152, v34\n"
    "v_mfma_f32_16x16x32_bf16 v[156:159], a[240:243], v[168:171], v[156:159]\n"
    "v_mul_f32 v153, v153, v34\n"
    "v_mul_f32 v154, v154, v34\n"
    "v_mfma_f32_16x16x32_bf16 v[156:159], a[236:239], v[132:135], v[156:159]\n"
    "v_mul_f32 v155, v155, v34\n"
    "v_cvt_pk_bf16_f32 v46, v152, v153\n"
    "v_mfma_f32_16x16x32_bf16 v[156:159], a[232:235], v[120:123], v[156:159]\n"
    "v_cvt_pk_bf16_f32 v47, v154, v155\n"
    "v_mfma_f32_16x16x32_bf16 v[156:159], a[228:231], v[84:87], v[156:159]\n"
    "v_mfma_f32_16x16x32_bf16 v[156:159], a[224:227], v[72:75], v[156:159]\n"
    "s_waitcnt lgkmcnt(12)\n"
    "buffer_store_dwordx4 v[40:43], v37, s[28:31], s66 offen offset:0 sc1\n"
    "buffer_store_dwordx4 v[44:47], v37, s[28:31], s66 offen offset:1024 sc1\n"
    "s_cmp_lg_u32 s64, 7\n"
    "s_cbranch_scc1 Lo_%=\n"
    "s_add_u32 s49, s59, 5\n"
    "s_lshl_b32 s49, s49, 4\n"
    "s_add_u32 s49, s49, s62\n"
    "s_min_u32 s75, s49, 125\n"
    "s_mul_i32 s73, s75, 57\n"
    "s_lshr_b32 s73, s73, 9\n"
    "s_mul_i32 s48, s73, 9\n"
    "s_sub_u32 s74, s75, s48\n"
    "s_cmp_lt_u32 s49, 126\n"
    "s_cselect_b32 s75, 1, 0\n"
    "v_mul_f32 v156, v156, v35\n"
    "v_mul_f32 v157, v157, v35\n"
    "v_mul_f32 v158, v158, v35\n"
    "v_mul_f32 v159, v159, v35\n"
    "v_cvt_pk_bf16_f32 v50, v156, v157\n"
    "v_cvt_pk_bf16_f32 v51, v158, v159\n"
    "buffer_store_dwordx4 v[48:51], v37, s[28:31], s66 offen offset:2048 sc1\n"
    "s_mul_i32 s66, s68, 9216\n"
    "s_add_u32 s66, s66, s72\n"
    "s_add_u32 s68, s68, 1\n"
    "s_cmp_eq_u32 s68, 14\n"
    "s_cselect_b32 s68, 0, s68\n"
    "v_mov_b32 v37, v36\n"
    "s_waitcnt vmcnt(8)\n"
    "buffer_store_dwordx2 v[38:39], v24, s[36:39], 0 offen sc1\n"
    "v_add_u32 v24, 0x180, v24\n"
    "s_waitcnt vmcnt(5)\n"
    "buffer_store_dwordx2 v[38:39], v24, s[36:39], 0 offen sc1\n"
    "v_add_u32 v24, 0x180, v24\n"
    "s_waitcnt vmcnt(2)\n"
    "buffer_store_dwordx2 v[38:39], v24, s[36:39], 0 offen sc1\n"
    "v_add_u32 v24, 0x180, v24\n"
    "s_mov_b32 s76, 0x40000\n"
    "s_mov_b32 s69, 0\n"
    "s_mul_i32 s49, s74, 43\n"
    "s_lshr_b32 s49, s49, 7\n"
    "s_mul_i32 s51, s73, 3\n"
    "s_add_u32 s49, s49, s51\n"
    "s_lshl_b32 s49, s49, 7\n"
    "buffer_load_dwordx2 v[212:213], v6, s[36:39], s49 offen sc1\n"
    "v_and_b32 v52, 1, v2\n"
    "v_lshlrev_b32 v52, 4, v52\n"
    "v_mov_b32 v53, 0x3f80\n"
    "v_lshlrev_b32 v52, v52, v53\n"
    "v_bfe_u32 v53, v2, 1, 1\n"
    "v_lshrrev_b32 v54, 3, v2\n"
    "v_lshl_add_u32 v53, v54, 1, v53\n"
    "v_bfe_u32 v54, v2, 2, 1\n"
    "v_lshl_add_u32 v54, v54, 1, 0\n"
    "v_cmp_eq_u32 vcc, v54, v3\n"
    "s_nop 1\n"
    "v_cndmask_b32 v55, 0, v52, vcc\n"
    "v_cmp_eq_u32 vcc, 0, v53\n"
    "s_nop 1\n"
    "v_cndmask_b32 v56, 0, v55, vcc\n"
    "v_cmp_eq_u32 vcc, 1, v53\n"
    "s_nop 1\n"
    "v_cndmask_b32 v57, 0, v55, vcc\n"
    "v_cmp_eq_u32 vcc, 2, v53\n"
    "s_nop 1\n"
    "v_cndmask_b32 v58, 0, v55, vcc\n"
    "v_cmp_eq_u32 vcc, 3, v53\n"
    "s_nop 1\n"
    "v_cndmask_b32 v59, 0, v55, vcc\n"
    "v_bfe_u32 v54, v2, 2, 1\n"
    "v_lshl_add_u32 v54, v54, 1, 1\n"
    "v_cmp_eq_u32 vcc, v54, v3\n"
    "s_nop 1\n"
    "v_cndmask_b32 v55, 0, v52, vcc\n"
    "v_cmp_eq_u32 vcc, 0, v53\n"
    "s_nop 1\n"
    "v_cndmask_b32 v60, 0, v55, vcc\n"
    "v_cmp_eq_u32 vcc, 1, v53\n"
    "s_nop 1\n"
    "v_cndmask_b32 v61, 0, v55, vcc\n"
    "v_cmp_eq_u32 vcc, 2, v53\n"
    "s_nop 1\n"
    "v_cndmask_b32 v62, 0, v55, vcc\n"
    "v_cmp_eq_u32 vcc, 3, v53\n"
    "s_nop 1\n"
    "v_cndmask_b32 v63, 0, v55, vcc\n"
    "s_waitcnt vmcnt(0) lgkmcnt(0)\n"
    "s_cmp_eq_u32 s75, 0\n"
    "s_cbranch_scc1 Lrdone_%=\n"
    "s_branch Lchk_%=\n"
    "Lrare0_%=:\n"
    "s_nop 7\n"
    "s_nop 3\n"
    "v_max3_f32 v244, v88, v89, v90\n"
    "v_max3_f32 v244, v244, v91, v100\n"
    "v_max3_f32 v244, v244, v101, v102\n"
    "v_max3_f32 v244, v244, v103, v136\n"
    "v_max3_f32 v244, v244, v137, v138\n"
    "v_max3_f32 v244, v244, v139, v148\n"
    "v_max3_f32 v244, v244, v149, v150\n"
    "v_max_f32 v244, v244, v151\n"
    "v_max3_f32 v245, v92, v93, v94\n"
    "v_max3_f32 v245, v245, v95, v104\n"
    "v_max3_f32 v245, v245, v105, v106\n"
    "v_max3_f32 v245, v245, v107, v140\n"
    "v_max3_f32 v245, v245, v141, v142\n"
    "v_max3_f32 v245, v245, v143, v152\n"
    "v_max3_f32 v245, v245, v153, v154\n"
    "v_max_f32 v245, v245, v155\n"
    "v_max3_f32 v246, v96, v97, v98\n"
    "v_max3_f32 v246, v246, v99, v108\n"
    "v_max3_f32 v246, v246, v109, v110\n"
    "v_max3_f32 v246, v246, v111, v144\n"
    "v_max3_f32 v246, v246, v145, v146\n"
    "v_max3_f32 v246, v246, v147, v156\n"
    "v_max3_f32 v246, v246, v157, v158\n"
    "v_max_f32 v246, v246, v159\n"
    "v_mov_b32 v247, v244\n"
    "v_mov_b32 v248, v245\n"
    "v_mov_b32 v249, v246\n"
    "v_permlane32_swap_b32 v247, v244\n"
    "v_permlane32_swap_b32 v248, v245\n"
    "v_permlane32_swap_b32 v249, v246\n"
    "v_max_f32 v244, v247, v244\n"
    "v_max_f32 v245, v248, v245\n"
    "v_max_f32 v246, v249, v246\n"
    "v_mov_b32 v247, v244\n"
    "v_mov_b32 v248, v245\n"
    "v_mov_b32 v249, v246\n"
    "v_permlane16_swap_b32 v247, v244\n"
    "v_permlane16_swap_b32 v248, v245\n"
    "v_permlane16_swap_b32 v249, v246\n"
    "v_max_f32 v244, v247, v244\n"
    "v_max_f32 v245, v248, v245\n"
    "v_max_f32 v246, v249, v246\n"
    "v_max_f32 v244, 0, v244\n"
    "v_max_f32 v245, 0, v245\n"
    "v_max_f32 v246, 0, v246\n"
    "v_exp_f32 v250, -v244\n"
    "v_exp_f32 v251, -v245\n"
    "v_exp_f32 v252, -v246\n"
    "v_sub_f32 v88, v88, v244\n"
    "v_sub_f32 v89, v89, v244\n"
    "v_sub_f32 v90, v90, v244\n"
    "v_sub_f32 v91, v91, v244\n"
    "v_sub_f32 v100, v100, v244\n"
    "v_sub_f32 v101, v101, v244\n"
    "v_sub_f32 v102, v102, v244\n"
    "v_sub_f32 v103, v103, v244\n"
    "v_sub_f32 v136, v136, v244\n"
    "v_sub_f32 v137, v137, v244\n"
    "v_sub_f32 v138, v138, v244\n"
    "v_sub_f32 v139, v139, v244\n"
    "v_sub_f32 v148, v148, v244\n"
    "v_sub_f32 v149, v149, v244\n"
    "v_sub_f32 v150, v150, v244\n"
    "v_sub_f32 v151, v151, v244\n"
    "v_sub_f32 v232, v232, v244\n"
    "v_sub_f32 v233, v233, v244\n"
    "v_sub_f32 v234, v234, v244\n"
    "v_sub_f32 v235, v235, v244\n"
    "v_mul_f32 v27, v27, v250\n"
    "v_sub_f32 v92, v92, v245\n"
    "v_sub_f32 v93, v93, v245\n"
    "v_sub_f32 v94, v94, v245\n"
    "v_sub_f32 v95, v95, v245\n"
    "v_sub_f32 v104, v104, v245\n"
    "v_sub_f32 v105, v105, v245\n"
    "v_sub_f32 v106, v106, v245\n"
    "v_sub_f32 v107, v107, v245\n"
    "v_sub_f32 v140, v140, v245\n"
    "v_sub_f32 v141, v141, v245\n"
    "v_sub_f32 v142, v142, v245\n"
    "v_sub_f32 v143, v143, v245\n"
    "v_sub_f32 v152, v152, v245\n"
    "v_sub_f32 v153, v153, v245\n"
    "v_sub_f32 v154, v154, v245\n"
    "v_sub_f32 v155, v155, v245\n"
    "v_sub_f32 v236, v236, v245\n"
    "v_sub_f32 v237, v237, v245\n"
    "v_sub_f32 v238, v238, v245\n"
    "v_sub_f32 v239, v239, v245\n"
    "v_mul_f32 v28, v28, v251\n"
    "v_sub_f32 v96, v96, v246\n"
    "v_sub_f32 v97, v97, v246\n"
    "v_sub_f32 v98, v98, v246\n"
    "v_sub_f32 v99, v99, v246\n"
    "v_sub_f32 v108, v108, v246\n"
    "v_sub_f32 v109, v109, v246\n"
    "v_sub_f32 v110, v110, v246\n"
    "v_sub_f32 v111, v111, v246\n"
    "v_sub_f32 v144, v144, v246\n"
    "v_sub_f32 v145, v145, v246\n"
    "v_sub_f32 v146, v146, v246\n"
    "v_sub_f32 v147, v147, v246\n"
    "v_sub_f32 v156, v156, v246\n"
    "v_sub_f32 v157, v157, v246\n"
    "v_sub_f32 v158, v158, v246\n"
    "v_sub_f32 v159, v159, v246\n"
    "v_sub_f32 v240, v240, v246\n"
    "v_sub_f32 v241, v241, v246\n"
    "v_sub_f32 v242, v242, v246\n"
    "v_sub_f32 v243, v243, v246\n"
    "v_mul_f32 v29, v29, v252\n"
    "v_accvgpr_read_b32 v247, a0\n"
    "v_accvgpr_read_b32 v248, a1\n"
    "v_accvgpr_read_b32 v249, a2\n"
    "v_accvgpr_read_b32 v253, a3\n"
    "v_mul_f32 v247, v247, v250\n"
    "v_mul_f32 v248, v248, v250\n"
    "v_mul_f32 v249, v249, v250\n"
    "v_mul_f32 v253, v253, v250\n"
    "v_accvgpr_write_b32 a0, v247\n"
    "v_accvgpr_write_b32 a1, v248\n"
    "v_accvgpr_write_b32 a2, v249\n"
    "v_accvgpr_write_b32 a3, v253\n"
    "v_accvgpr_read_b32 v247, a4\n"
    "v_accvgpr_read_b32 v248, a5\n"
    "v_accvgpr_read_b32 v249, a6\n"
    "v_accvgpr_read_b32 v253, a7\n"
    "v_mul_f32 v247, v247, v251\n"
    "v_mul_f32 v248, v248, v251\n"
    "v_mul_f32 v249, v249, v251\n"
    "v_mul_f32 v253, v253, v251\n"
    "v_accvgpr_write_b32 a4, v247\n"
    "v_accvgpr_write_b32 a5, v248\n"
    "v_accvgpr_write_b32 a6, v249\n"
    "v_accvgpr_write_b32 a7, v253\n"
    "v_accvgpr_read_b32 v247, a8\n"
    "v_accvgpr_read_b32 v248, a9\n"
    "v_accvgpr_read_b32 v249, a10\n"
    "v_accvgpr_read_b32 v253, a11\n"
    "v_mul_f32 v247, v247, v252\n"
    "v_mul_f32 v248, v248, v252\n"
    "v_mul_f32 v249, v249, v252\n"
    "v_mul_f32 v253, v253, v252\n"
    "v_accvgpr_write_b32 a8, v247\n"
    "v_accvgpr_write_b32 a9, v248\n"
    "v_accvgpr_write_b32 a10, v249\n"
    "v_accvgpr_write_b32 a11, v253\n"
    "v_accvgpr_read_b32 v247, a12\n"
    "v_accvgpr_read_b32 v248, a13\n"
    "v_accvgpr_read_b32 v249, a14\n"
    "v_accvgpr_read_b32 v253, a15\n"
    "v_mul_f32 v247, v247, v250\n"
    "v_mul_f32 v248, v248, v250\n"
    "v_mul_f32 v249, v249, v250\n"
    "v_mul_f32 v253, v253, v250\n"
    "v_accvgpr_write_b32 a12, v247\n"
    "v_accvgpr_write_b32 a13, v248\n"
    "v_accvgpr_write_b32 a14, v249\n"
    "v_accvgpr_write_b32 a15, v253\n"
    "v_accvgpr_read_b32 v247, a16\n"
    "v_accvgpr_read_b32 v248, a17\n"
    "v_accvgpr_read_b32 v249, a18\n"
    "v_accvgpr_read_b32 v253, a19\n"
    "v_mul_f32 v247, v247, v251\n"
    "v_mul_f32 v248, v248, v251\n"
    "v_mul_f32 v249, v249, v251\n"
    "v_mul_f32 v253, v253, v251\n"
    "v_accvgpr_write_b32 a16, v247\n"
    "v_accvgpr_write_b32 a17, v248\n"
    "v_accvgpr_write_b32 a18, v249\n"
    "v_accvgpr_write_b32 a19, v253\n"
    "v_accvgpr_read_b32 v247, a20\n"
    "v_accvgpr_read_b32 v248, a21\n"
    "v_accvgpr_read_b32 v249, a22\n"
    "v_accvgpr_read_b32 v253, a23\n"
    "v_mul_f32 v247, v247, v252\n"
    "v_mul_f32 v248, v248, v252\n"
    "v_mul_f32 v249, v249, v252\n"
    "v_mul_f32 v253, v253, v252\n"
    "v_accvgpr_write_b32 a20, v247\n"
    "v_accvgpr_write_b32 a21, v248\n"
    "v_accvgpr_write_b32 a22, v249\n"
    "v_accvgpr_write_b32 a23, v253\n"
    "v_accvgpr_read_b32 v247, a24\n"
    "v_accvgpr_read_b32 v248, a25\n"
    "v_accvgpr_read_b32 v249, a26\n"
    "v_accvgpr_read_b32 v253, a27\n"
    "v_mul_f32 v247, v247, v250\n"
    "v_mul_f32 v248, v248, v250\n"
    "v_mul_f32 v249, v249, v250\n"
    "v_mul_f32 v253, v253, v250\n"
    "v_accvgpr_write_b32 a24, v247\n"
    "v_accvgpr_write_b32 a25, v248\n"
    "v_accvgpr_write_b32 a26, v249\n"
    "v_accvgpr_write_b32 a27, v253\n"
    "v_accvgpr_read_b32 v247, a28\n"
    "v_accvgpr_read_b32 v248, a29\n"
    "v_accvgpr_read_b32 v249, a30\n"
    "v_accvgpr_read_b32 v253, a31\n"
    "v_mul_f32 v247, v247, v251\n"
    "v_mul_f32 v248, v248, v251\n"
    "v_mul_f32 v249, v249, v251\n"
    "v_mul_f32 v253, v253, v251\n"
    "v_accvgpr_write_b32 a28, v247\n"
    "v_accvgpr_write_b32 a29, v248\n"
    "v_accvgpr_write_b32 a30, v249\n"
    "v_accvgpr_write_b32 a31, v253\n"
    "v_accvgpr_read_b32 v247, a32\n"
    "v_accvgpr_read_b32 v248, a33\n"
    "v_accvgpr_read_b32 v249, a34\n"
    "v_accvgpr_read_b32 v253, a35\n"
    "v_mul_f32 v247, v247, v252\n"
    "v_mul_f32 v248, v248, v252\n"
    "v_mul_f32 v249, v249, v252\n"
    "v_mul_f32 v253, v253, v252\n"
    "v_accvgpr_write_b32 a32, v247\n"
    "v_accvgpr_write_b32 a33, v248\n"
    "v_accvgpr_write_b32 a34, v249\n"
    "v_accvgpr_write_b32 a35, v253\n"
    "v_accvgpr_read_b32 v247, a36\n"
    "v_accvgpr_read_b32 v248, a37\n"
    "v_accvgpr_read_b32 v249, a38\n"
    "v_accvgpr_read_b32 v253, a39\n"
    "v_mul_f32 v247, v247, v250\n"
    "v_mul_f32 v248, v248, v250\n"
    "v_mul_f32 v249, v249, v250\n"
    "v_mul_f32 v253, v253, v250\n"
    "v_accvgpr_write_b32 a36, v247\n"
    "v_accvgpr_write_b32 a37, v248\n"
    "v_accvgpr_write_b32 a38, v249\n"
    "v_accvgpr_write_b32 a39, v253\n"
    "v_accvgpr_read_b32 v247, a40\n"
    "v_accvgpr_read_b32 v248, a41\n"
    "v_accvgpr_read_b32 v249, a42\n"
    "v_accvgpr_read_b32 v253, a43\n"
    "v_mul_f32 v247, v247, v251\n"
    "v_mul_f32 v248, v248, v251\n"
    "v_mul_f32 v249, v249, v251\n"
    "v_mul_f32 v253, v253, v251\n"
    "v_accvgpr_write_b32 a40, v247\n"
    "v_accvgpr_write_b32 a41, v248\n"
    "v_accvgpr_write_b32 a42, v249\n"
    "v_accvgpr_write_b32 a43, v253\n"
    "v_accvgpr_read_b32 v247, a44\n"
    "v_accvgpr_read_b32 v248, a45\n"
    "v_accvgpr_read_b32 v249, a46\n"
    "v_accvgpr_read_b32 v253, a47\n"
    "v_mul_f32 v247, v247, v252\n"
    "v_mul_f32 v248, v248, v252\n"
    "v_mul_f32 v249, v249, v252\n"
    "v_mul_f32 v253, v253, v252\n"
    "v_accvgpr_write_b32 a44, v247\n"
    "v_accvgpr_write_b32 a45, v248\n"
    "v_accvgpr_write_b32 a46, v249\n"
    "v_accvgpr_write_b32 a47, v253\n"
    "v_accvgpr_read_b32 v247, a48\n"
    "v_accvgpr_read_b32 v248, a49\n"
    "v_accvgpr_read_b32 v249, a50\n"
    "v_accvgpr_read_b32 v253, a51\n"
    "v_mul_f32 v247, v247, v250\n"
    "v_mul_f32 v248, v248, v250\n"
    "v_mul_f32 v249, v249, v250\n"
    "v_mul_f32 v253, v253, v250\n"
    "v_accvgpr_write_b32 a48, v247\n"
    "v_accvgpr_write_b32 a49, v248\n"
    "v_accvgpr_write_b32 a50, v249\n"
    "v_accvgpr_write_b32 a51, v253\n"
    "v_accvgpr_read_b32 v247, a52\n"
    "v_accvgpr_read_b32 v248, a53\n"
    "v_accvgpr_read_b32 v249, a54\n"
    "v_accvgpr_read_b32 v253, a55\n"
    "v_mul_f32 v247, v247, v251\n"
    "v_mul_f32 v248, v248, v251\n"
    "v_mul_f32 v249, v249, v251\n"
    "v_mul_f32 v253, v253, v251\n"
    "v_accvgpr_write_b32 a52, v247\n"
    "v_accvgpr_write_b32 a53, v248\n"
    "v_accvgpr_write_b32 a54, v249\n"
    "v_accvgpr_write_b32 a55, v253\n"
    "v_accvgpr_read_b32 v247, a56\n"
    "v_accvgpr_read_b32 v248, a57\n"
    "v_accvgpr_read_b32 v249, a58\n"
    "v_accvgpr_read_b32 v253, a59\n"
    "v_mul_f32 v247, v247, v252\n"
    "v_mul_f32 v248, v248, v252\n"
    "v_mul_f32 v249, v249, v252\n"
    "v_mul_f32 v253, v253, v252\n"
    "v_accvgpr_write_b32 a56, v247\n"
    "v_accvgpr_write_b32 a57, v248\n"
    "v_accvgpr_write_b32 a58, v249\n"
    "v_accvgpr_write_b32 a59, v253\n"
    "v_accvgpr_read_b32 v247, a60\n"
    "v_accvgpr_read_b32 v248, a61\n"
    "v_accvgpr_read_b32 v249, a62\n"
    "v_accvgpr_read_b32 v253, a63\n"
    "v_mul_f32 v247, v247, v250\n"
    "v_mul_f32 v248, v248, v250\n"
    "v_mul_f32 v249, v249, v250\n"
    "v_mul_f32 v253, v253, v250\n"
    "v_accvgpr_write_b32 a60, v247\n"
    "v_accvgpr_write_b32 a61, v248\n"
    "v_accvgpr_write_b32 a62, v249\n"
    "v_accvgpr_write_b32 a63, v253\n"
    "v_accvgpr_read_b32 v247, a64\n"
    "v_accvgpr_read_b32 v248, a65\n"
    "v_accvgpr_read_b32 v249, a66\n"
    "v_accvgpr_read_b32 v253, a67\n"
    "v_mul_f32 v247, v247, v251\n"
    "v_mul_f32 v248, v248, v251\n"
    "v_mul_f32 v249, v249, v251\n"
    "v_mul_f32 v253, v253, v251\n"
    "v_accvgpr_write_b32 a64, v247\n"
    "v_accvgpr_write_b32 a65, v248\n"
    "v_accvgpr_write_b32 a66, v249\n"
    "v_accvgpr_write_b32 a67, v253\n"
    "v_accvgpr_read_b32 v247, a68\n"
    "v_accvgpr_read_b32 v248, a69\n"
    "v_accvgpr_read_b32 v249, a70\n"
    "v_accvgpr_read_b32 v253, a71\n"
    "v_mul_f32 v247, v247, v252\n"
    "v_mul_f32 v248, v248, v252\n"
    "v_mul_f32 v249, v249, v252\n"
    "v_mul_f32 v253, v253, v252\n"
    "v_accvgpr_write_b32 a68, v247\n"
    "v_accvgpr_write_b32 a69, v248\n"
    "v_accvgpr_write_b32 a70, v249\n"
    "v_accvgpr_write_b32 a71, v253\n"
    "v_accvgpr_read_b32 v247, a72\n"
    "v_accvgpr_read_b32 v248, a73\n"
    "v_accvgpr_read_b32 v249, a74\n"
    "v_accvgpr_read_b32 v253, a75\n"
    "v_mul_f32 v247, v247, v250\n"
    "v_mul_f32 v248, v248, v250\n"
    "v_mul_f32 v249, v249, v250\n"
    "v_mul_f32 v253, v253, v250\n"
    "v_accvgpr_write_b32 a72, v247\n"
    "v_accvgpr_write_b32 a73, v248\n"
    "v_accvgpr_write_b32 a74, v249\n"
    "v_accvgpr_write_b32 a75, v253\n"
    "v_accvgpr_read_b32 v247, a76\n"
    "v_accvgpr_read_b32 v248, a77\n"
    "v_accvgpr_read_b32 v249, a78\n"
    "v_accvgpr_read_b32 v253, a79\n"
    "v_mul_f32 v247, v247, v251\n"
    "v_mul_f32 v248, v248, v251\n"
    "v_mul_f32 v249, v249, v251\n"
    "v_mul_f32 v253, v253, v251\n"
    "v_accvgpr_write_b32 a76, v247\n"
    "v_accvgpr_write_b32 a77, v248\n"
    "v_accvgpr_write_b32 a78, v249\n"
    "v_accvgpr_write_b32 a79, v253\n"
    "v_accvgpr_read_b32 v247, a80\n"
    "v_accvgpr_read_b32 v248, a81\n"
    "v_accvgpr_read_b32 v249, a82\n"
    "v_accvgpr_read_b32 v253, a83\n"
    "v_mul_f32 v247, v247, v252\n"
    "v_mul_f32 v248, v248, v252\n"
    "v_mul_f32 v249, v249, v252\n"
    "v_mul_f32 v253, v253, v252\n"
    "v_accvgpr_write_b32 a80, v247\n"
    "v_accvgpr_write_b32 a81, v248\n"
    "v_accvgpr_write_b32 a82, v249\n"
    "v_accvgpr_write_b32 a83, v253\n"
    "v_accvgpr_read_b32 v247, a84\n"
    "v_accvgpr_read_b32 v248, a85\n"
    "v_accvgpr_read_b32 v249, a86\n"
    "v_accvgpr_read_b32 v253, a87\n"
    "v_mul_f32 v247, v247, v250\n"
    "v_mul_f32 v248, v248, v250\n"
    "v_mul_f32 v249, v249, v250\n"
    "v_mul_f32 v253, v253, v250\n"
    "v_accvgpr_write_b32 a84, v247\n"
    "v_accvgpr_write_b32 a85, v248\n"
    "v_accvgpr_write_b32 a86, v249\n"
    "v_accvgpr_write_b32 a87, v253\n"
    "v_accvgpr_read_b32 v247, a88\n"
    "v_accvgpr_read_b32 v248, a89\n"
    "v_accvgpr_read_b32 v249, a90\n"
    "v_accvgpr_read_b32 v253, a91\n"
    "v_mul_f32 v247, v247, v251\n"
    "v_mul_f32 v248, v248, v251\n"
    "v_mul_f32 v249, v249, v251\n"
    "v_mul_f32 v253, v253, v251\n"
    "v_accvgpr_write_b32 a88, v247\n"
    "v_accvgpr_write_b32 a89, v248\n"
    "v_accvgpr_write_b32 a90, v249\n"
    "v_accvgpr_write_b32 a91, v253\n"
    "v_accvgpr_read_b32 v247, a92\n"
    "v_accvgpr_read_b32 v248, a93\n"
    "v_accvgpr_read_b32 v249, a94\n"
    "v_accvgpr_read_b32 v253, a95\n"
    "v_mul_f32 v247, v247, v252\n"
    "v_mul_f32 v248, v248, v252\n"
    "v_mul_f32 v249, v249, v252\n"
    "v_mul_f32 v253, v253, v252\n"
    "v_accvgpr_write_b32 a92, v247\n"
    "v_accvgpr_write_b32 a93, v248\n"
    "v_accvgpr_write_b32 a94, v249\n"
    "v_accvgpr_write_b32 a95, v253\n"
    "v_accvgpr_read_b32 v247, a96\n"
    "v_accvgpr_read_b32 v248, a97\n"
    "v_accvgpr_read_b32 v249, a98\n"
    "v_accvgpr_read_b32 v253, a99\n"
    "v_mul_f32 v247, v247, v250\n"
    "v_mul_f32 v248, v248, v250\n"
    "v_mul_f32 v249, v249, v250\n"
    "v_mul_f32 v253, v253, v250\n"
    "v_accvgpr_write_b32 a96, v247\n"
    "v_accvgpr_write_b32 a97, v248\n"
    "v_accvgpr_write_b32 a98, v249\n"
    "v_accvgpr_write_b32 a99, v253\n"
    "v_accvgpr_read_b32 v247, a100\n"
    "v_accvgpr_read_b32 v248, a101\n"
    "v_accvgpr_read_b32 v249, a102\n"
    "v_accvgpr_read_b32 v253, a103\n"
    "v_mul_f32 v247, v247, v251\n"
    "v_mul_f32 v248, v248, v251\n"
    "v_mul_f32 v249, v249, v251\n"
    "v_mul_f32 v253, v253, v251\n"
    "v_accvgpr_write_b32 a100, v247\n"
    "v_accvgpr_write_b32 a101, v248\n"
    "v_accvgpr_write_b32 a102, v249\n"
    "v_accvgpr_write_b32 a103, v253\n"
    "v_accvgpr_read_b32 v247, a104\n"
    "v_accvgpr_read_b32 v248, a105\n"
    "v_accvgpr_read_b32 v249, a106\n"
    "v_accvgpr_read_b32 v253, a107\n"
    "v_mul_f32 v247, v247, v252\n"
    "v_mul_f32 v248, v248, v252\n"
    "v_mul_f32 v249, v249, v252\n"
    "v_mul_f32 v253, v253, v252\n"
    "v_accvgpr_write_b32 a104, v247\n"
    "v_accvgpr_write_b32 a105, v248\n"
    "v_accvgpr_write_b32 a106, v249\n"
    "v_accvgpr_write_b32 a107, v253\n"
    "v_accvgpr_read_b32 v247, a108\n"
    "v_accvgpr_read_b32 v248, a109\n"
    "v_accvgpr_read_b32 v249, a110\n"
    "v_accvgpr_read_b32 v253, a111\n"
    "v_mul_f32 v247, v247, v250\n"
    "v_mul_f32 v248, v248, v250\n"
    "v_mul_f32 v249, v249, v250\n"
    "v_mul_f32 v253, v253, v250\n"
    "v_accvgpr_write_b32 a108, v247\n"
    "v_accvgpr_write_b32 a109, v248\n"
    "v_accvgpr_write_b32 a110, v249\n"
    "v_accvgpr_write_b32 a111, v253\n"
    "v_accvgpr_read_b32 v247, a112\n"
    "v_accvgpr_read_b32 v248, a113\n"
    "v_accvgpr_read_b32 v249, a114\n"
    "v_accvgpr_read_b32 v253, a115\n"
    "v_mul_f32 v247, v247, v251\n"
    "v_mul_f32 v248, v248, v251\n"
    "v_mul_f32 v249, v249, v251\n"
    "v_mul_f32 v253, v253, v251\n"
    "v_accvgpr_write_b32 a112, v247\n"
    "v_accvgpr_write_b32 a113, v248\n"
    "v_accvgpr_write_b32 a114, v249\n"
    "v_accvgpr_write_b32 a115, v253\n"
    "v_accvgpr_read_b32 v247, a116\n"
    "v_accvgpr_read_b32 v248, a117\n"
    "v_accvgpr_read_b32 v249, a118\n"
    "v_accvgpr_read_b32 v253, a119\n"
    "v_mul_f32 v247, v247, v252\n"
    "v_mul_f32 v248, v248, v252\n"
    "v_mul_f32 v249, v249, v252\n"
    "v_mul_f32 v253, v253, v252\n"
    "v_accvgpr_write_b32 a116, v247\n"
    "v_accvgpr_write_b32 a117, v248\n"
    "v_accvgpr_write_b32 a118, v249\n"
    "v_accvgpr_write_b32 a119, v253\n"
    "v_accvgpr_read_b32 v247, a120\n"
    "v_accvgpr_read_b32 v248, a121\n"
    "v_accvgpr_read_b32 v249, a122\n"
    "v_accvgpr_read_b32 v253, a123\n"
    "v_mul_f32 v247, v247, v250\n"
    "v_mul_f32 v248, v248, v250\n"
    "v_mul_f32 v249, v249, v250\n"
    "v_mul_f32 v253, v253, v250\n"
    "v_accvgpr_write_b32 a120, v247\n"
    "v_accvgpr_write_b32 a121, v248\n"
    "v_accvgpr_write_b32 a122, v249\n"
    "v_accvgpr_write_b32 a123, v253\n"
    "v_accvgpr_read_b32 v247, a124\n"
    "v_accvgpr_read_b32 v248, a125\n"
    "v_accvgpr_read_b32 v249, a126\n"
    "v_accvgpr_read_b32 v253, a127\n"
    "v_mul_f32 v247, v247, v251\n"
    "v_mul_f32 v248, v248, v251\n"
    "v_mul_f32 v249, v249, v251\n"
    "v_mul_f32 v253, v253, v251\n"
    "v_accvgpr_write_b32 a124, v247\n"
    "v_accvgpr_write_b32 a125, v248\n"
    "v_accvgpr_write_b32 a126, v249\n"
    "v_accvgpr_write_b32 a127, v253\n"
    "v_accvgpr_read_b32 v247, a128\n"
    "v_accvgpr_read_b32 v248, a129\n"
    "v_accvgpr_read_b32 v249, a130\n"
    "v_accvgpr_read_b32 v253, a131\n"
    "v_mul_f32 v247, v247, v252\n"
    "v_mul_f32 v248, v248, v252\n"
    "v_mul_f32 v249, v249, v252\n"
    "v_mul_f32 v253, v253, v252\n"
    "v_accvgpr_write_b32 a128, v247\n"
    "v_accvgpr_write_b32 a129, v248\n"
    "v_accvgpr_write_b32 a130, v249\n"
    "v_accvgpr_write_b32 a131, v253\n"
    "v_accvgpr_read_b32 v247, a132\n"
    "v_accvgpr_read_b32 v248, a133\n"
    "v_accvgpr_read_b32 v249, a134\n"
    "v_accvgpr_read_b32 v253, a135\n"
    "v_mul_f32 v247, v247, v250\n"
    "v_mul_f32 v248, v248, v250\n"
    "v_mul_f32 v249, v249, v250\n"
    "v_mul_f32 v253, v253, v250\n"
    "v_accvgpr_write_b32 a132, v247\n"
    "v_accvgpr_write_b32 a133, v248\n"
    "v_accvgpr_write_b32 a134, v249\n"
    "v_accvgpr_write_b32 a135, v253\n"
    "v_accvgpr_read_b32 v247, a136\n"
    "v_accvgpr_read_b32 v248, a137\n"
    "v_accvgpr_read_b32 v249, a138\n"
    "v_accvgpr_read_b32 v253, a139\n"
    "v_mul_f32 v247, v247, v251\n"
    "v_mul_f32 v248, v248, v251\n"
    "v_mul_f32 v249, v249, v251\n"
    "v_mul_f32 v253, v253, v251\n"
    "v_accvgpr_write_b32 a136, v247\n"
    "v_accvgpr_write_b32 a137, v248\n"
    "v_accvgpr_write_b32 a138, v249\n"
    "v_accvgpr_write_b32 a139, v253\n"
    "v_accvgpr_read_b32 v247, a140\n"
    "v_accvgpr_read_b32 v248, a141\n"
    "v_accvgpr_read_b32 v249, a142\n"
    "v_accvgpr_read_b32 v253, a143\n"
    "v_mul_f32 v247, v247, v252\n"
    "v_mul_f32 v248, v248, v252\n"
    "v_mul_f32 v249, v249, v252\n"
    "v_mul_f32 v253, v253, v252\n"
    "v_accvgpr_write_b32 a140, v247\n"
    "v_accvgpr_write_b32 a141, v248\n"
    "v_accvgpr_write_b32 a142, v249\n"
    "v_accvgpr_write_b32 a143, v253\n"
    "v_accvgpr_read_b32 v247, a144\n"
    "v_accvgpr_read_b32 v248, a145\n"
    "v_accvgpr_read_b32 v249, a146\n"
    "v_accvgpr_read_b32 v253, a147\n"
    "v_mul_f32 v247, v247, v250\n"
    "v_mul_f32 v248, v248, v250\n"
    "v_mul_f32 v249, v249, v250\n"
    "v_mul_f32 v253, v253, v250\n"
    "v_accvgpr_write_b32 a144, v247\n"
    "v_accvgpr_write_b32 a145, v248\n"
    "v_accvgpr_write_b32 a146, v249\n"
    "v_accvgpr_write_b32 a147, v253\n"
    "v_accvgpr_read_b32 v247, a148\n"
    "v_accvgpr_read_b32 v248, a149\n"
    "v_accvgpr_read_b32 v249, a150\n"
    "v_accvgpr_read_b32 v253, a151\n"
    "v_mul_f32 v247, v247, v251\n"
    "v_mul_f32 v248, v248, v251\n"
    "v_mul_f32 v249, v249, v251\n"
    "v_mul_f32 v253, v253, v251\n"
    "v_accvgpr_write_b32 a148, v247\n"
    "v_accvgpr_write_b32 a149, v248\n"
    "v_accvgpr_write_b32 a150, v249\n"
    "v_accvgpr_write_b32 a151, v253\n"
    "v_accvgpr_read_b32 v247, a152\n"
    "v_accvgpr_read_b32 v248, a153\n"
    "v_accvgpr_read_b32 v249, a154\n"
    "v_accvgpr_read_b32 v253, a155\n"
    "v_mul_f32 v247, v247, v252\n"
    "v_mul_f32 v248, v248, v252\n"
    "v_mul_f32 v249, v249, v252\n"
    "v_mul_f32 v253, v253, v252\n"
    "v_accvgpr_write_b32 a152, v247\n"
    "v_accvgpr_write_b32 a153, v248\n"
    "v_accvgpr_write_b32 a154, v249\n"
    "v_accvgpr_write_b32 a155, v253\n"
    "v_accvgpr_read_b32 v247, a156\n"
    "v_accvgpr_read_b32 v248, a157\n"
    "v_accvgpr_read_b32 v249, a158\n"
    "v_accvgpr_read_b32 v253, a159\n"
    "v_mul_f32 v247, v247, v250\n"
    "v_mul_f32 v248, v248, v250\n"
    "v_mul_f32 v249, v249, v250\n"
    "v_mul_f32 v253, v253, v250\n"
    "v_accvgpr_write_b32 a156, v247\n"
    "v_accvgpr_write_b32 a157, v248\n"
    "v_accvgpr_write_b32 a158, v249\n"
    "v_accvgpr_write_b32 a159, v253\n"
    "v_accvgpr_read_b32 v247, a160\n"
    "v_accvgpr_read_b32 v248, a161\n"
    "v_accvgpr_read_b32 v249, a162\n"
    "v_accvgpr_read_b32 v253, a163\n"
    "v_mul_f32 v247, v247, v251\n"
    "v_mul_f32 v248, v248, v251\n"
    "v_mul_f32 v249, v249, v251\n"
    "v_mul_f32 v253, v253, v251\n"
    "v_accvgpr_write_b32 a160, v247\n"
    "v_accvgpr_write_b32 a161, v248\n"
    "v_accvgpr_write_b32 a162, v249\n"
    "v_accvgpr_write_b32 a163, v253\n"
    "v_accvgpr_read_b32 v247, a164\n"
    "v_accvgpr_read_b32 v248, a165\n"
    "v_accvgpr_read_b32 v249, a166\n"
    "v_accvgpr_read_b32 v253, a167\n"
    "v_mul_f32 v247, v247, v252\n"
    "v_mul_f32 v248, v248, v252\n"
    "v_mul_f32 v249, v249, v252\n"
    "v_mul_f32 v253, v253, v252\n"
    "v_accvgpr_write_b32 a164, v247\n"
    "v_accvgpr_write_b32 a165, v248\n"
    "v_accvgpr_write_b32 a166, v249\n"
    "v_accvgpr_write_b32 a167, v253\n"
    "v_accvgpr_read_b32 v247, a168\n"
    "v_accvgpr_read_b32 v248, a169\n"
    "v_accvgpr_read_b32 v249, a170\n"
    "v_accvgpr_read_b32 v253, a171\n"
    "v_mul_f32 v247, v247, v250\n"
    "v_mul_f32 v248, v248, v250\n"
    "v_mul_f32 v249, v249, v250\n"
    "v_mul_f32 v253, v253, v250\n"
    "v_accvgpr_write_b32 a168, v247\n"
    "v_accvgpr_write_b32 a169, v248\n"
    "v_accvgpr_write_b32 a170, v249\n"
    "v_accvgpr_write_b32 a171, v253\n"
    "v_accvgpr_read_b32 v247, a172\n"
    "v_accvgpr_read_b32 v248, a173\n"
    "v_accvgpr_read_b32 v249, a174\n"
    "v_accvgpr_read_b32 v253, a175\n"
    "v_mul_f32 v247, v247, v251\n"
    "v_mul_f32 v248, v248, v251\n"
    "v_mul_f32 v249, v249, v251\n"
    "v_mul_f32 v253, v253, v251\n"
    "v_accvgpr_write_b32 a172, v247\n"
    "v_accvgpr_write_b32 a173, v248\n"
    "v_accvgpr_write_b32 a174, v249\n"
    "v_accvgpr_write_b32 a175, v253\n"
    "v_accvgpr_read_b32 v247, a176\n"
    "v_accvgpr_read_b32 v248, a177\n"
    "v_accvgpr_read_b32 v249, a178\n"
    "v_accvgpr_read_b32 v253, a179\n"
    "v_mul_f32 v247, v247, v252\n"
    "v_mul_f32 v248, v248, v252\n"
    "v_mul_f32 v249, v249, v252\n"
    "v_mul_f32 v253, v253, v252\n"
    "v_accvgpr_write_b32 a176, v247\n"
    "v_accvgpr_write_b32 a177, v248\n"
    "v_accvgpr_write_b32 a178, v249\n"
    "v_accvgpr_write_b32 a179, v253\n"
    "v_accvgpr_read_b32 v247, a180\n"
    "v_accvgpr_read_b32 v248, a181\n"
    "v_accvgpr_read_b32 v249, a182\n"
    "v_accvgpr_read_b32 v253, a183\n"
    "v_mul_f32 v247, v247, v250\n"
    "v_mul_f32 v248, v248, v250\n"
    "v_mul_f32 v249, v249, v250\n"
    "v_mul_f32 v253, v253, v250\n"
    "v_accvgpr_write_b32 a180, v247\n"
    "v_accvgpr_write_b32 a181, v248\n"
    "v_accvgpr_write_b32 a182, v249\n"
    "v_accvgpr_write_b32 a183, v253\n"
    "v_accvgpr_read_b32 v247, a184\n"
    "v_accvgpr_read_b32 v248, a185\n"
    "v_accvgpr_read_b32 v249, a186\n"
    "v_accvgpr_read_b32 v253, a187\n"
    "v_mul_f32 v247, v247, v251\n"
    "v_mul_f32 v248, v248, v251\n"
    "v_mul_f32 v249, v249, v251\n"
    "v_mul_f32 v253, v253, v251\n"
    "v_accvgpr_write_b32 a184, v247\n"
    "v_accvgpr_write_b32 a185, v248\n"
    "v_accvgpr_write_b32 a186, v249\n"
    "v_accvgpr_write_b32 a187, v253\n"
    "v_accvgpr_read_b32 v247, a188\n"
    "v_accvgpr_read_b32 v248, a189\n"
    "v_accvgpr_read_b32 v249, a190\n"
    "v_accvgpr_read_b32 v253, a191\n"
    "v_mul_f32 v247, v247, v252\n"
    "v_mul_f32 v248, v248, v252\n"
    "v_mul_f32 v249, v249, v252\n"
    "v_mul_f32 v253, v253, v252\n"
    "v_accvgpr_write_b32 a188, v247\n"
    "v_accvgpr_write_b32 a189, v248\n"
    "v_accvgpr_write_b32 a190, v249\n"
    "v_accvgpr_write_b32 a191, v253\n"
    "v_exp_f32 v244, v88\n"
    "v_exp_f32 v245, v89\n"
    "v_exp_f32 v246, v90\n"
    "v_exp_f32 v247, v91\n"
    "v_add_f32 v30, v244, v245\n"
    "v_cvt_pk_bf16_f32 v184, v244, v245\n"
    "v_add_f32 v30, v30, v246\n"
    "v_cvt_pk_bf16_f32 v185, v246, v247\n"
    "v_add_f32 v30, v30, v247\n"
    "v_exp_f32 v248, v92\n"
    "v_exp_f32 v249, v93\n"
    "v_exp_f32 v250, v94\n"
    "v_exp_f32 v251, v95\n"
    "v_add_f32 v31, v248, v249\n"
    "v_cvt_pk_bf16_f32 v188, v248, v249\n"
    "v_add_f32 v31, v31, v250\n"
    "v_cvt_pk_bf16_f32 v189, v250, v251\n"
    "v_add_f32 v31, v31, v251\n"
    "v_exp_f32 v252, v96\n"
    "v_exp_f32 v253, v97\n"
    "v_exp_f32 v244, v98\n"
    "v_exp_f32 v245, v99\n"
    "v_add_f32 v32, v252, v253\n"
    "v_cvt_pk_bf16_f32 v192, v252, v253\n"
    "v_add_f32 v32, v32, v244\n"
    "v_cvt_pk_bf16_f32 v193, v244, v245\n"
    "v_add_f32 v32, v32, v245\n"
    "v_exp_f32 v246, v100\n"
    "v_exp_f32 v247, v101\n"
    "v_exp_f32 v248, v102\n"
    "v_exp_f32 v249, v103\n"
    "v_add_f32 v30, v30, v246\n"
    "v_cvt_pk_bf16_f32 v186, v246, v247\n"
    "v_add_f32 v30, v30, v247\n"
    "v_add_f32 v30, v30, v248\n"
    "v_cvt_pk_bf16_f32 v187, v248, v249\n"
    "v_add_f32 v30, v30, v249\n"
    "v_exp_f32 v250, v104\n"
    "v_exp_f32 v251, v105\n"
    "v_exp_f32 v252, v106\n"
    "v_exp_f32 v253, v107\n"
    "v_add_f32 v31, v31, v250\n"
    "v_cvt_pk_bf16_f32 v190, v250, v251\n"
    "v_add_f32 v31, v31, v251\n"
    "v_add_f32 v31, v31, v252\n"
    "v_cvt_pk_bf16_f32 v191, v252, v253\n"
    "v_add_f32 v31, v31, v253\n"
    "v_exp_f32 v244, v108\n"
    "v_exp_f32 v245, v109\n"
    "v_exp_f32 v246, v110\n"
    "v_exp_f32 v247, v111\n"
    "v_add_f32 v32, v32, v244\n"
    "v_cvt_pk_bf16_f32 v194, v244, v245\n"
    "v_add_f32 v32, v32, v245\n"
    "v_add_f32 v32, v32, v246\n"
    "v_cvt_pk_bf16_f32 v195, v246, v247\n"
    "v_add_f32 v32, v32, v247\n"
    "v_exp_f32 v248, v136\n"
    "v_exp_f32 v249, v137\n"
    "v_exp_f32 v250, v138\n"
    "v_exp_f32 v251, v139\n"
    "v_add_f32 v30, v30, v248\n"
    "v_cvt_pk_bf16_f32 v196, v248, v249\n"
    "v_add_f32 v30, v30, v249\n"
    "v_add_f32 v30, v30, v250\n"
    "v_cvt_pk_bf16_f32 v197, v250, v251\n"
    "v_add_f32 v30, v30, v251\n"
    "v_exp_f32 v252, v140\n"
    "v_exp_f32 v253, v141\n"
    "v_exp_f32 v244, v142\n"
    "v_exp_f32 v245, v143\n"
    "v_add_f32 v31, v31, v252\n"
    "v_cvt_pk_bf16_f32 v200, v252, v253\n"
    "v_add_f32 v31, v31, v253\n"
    "v_add_f32 v31, v31, v244\n"
    "v_cvt_pk_bf16_f32 v201, v244, v245\n"
    "v_add_f32 v31, v31, v245\n"
    "v_exp_f32 v246, v144\n"
    "v_exp_f32 v247, v145\n"
    "v_exp_f32 v248, v146\n"
    "v_exp_f32 v249, v147\n"
    "v_add_f32 v32, v32, v246\n"
    "v_cvt_pk_bf16_f32 v204, v246, v247\n"
    "v_add_f32 v32, v32, v247\n"
    "v_add_f32 v32, v32, v248\n"
    "v_cvt_pk_bf16_f32 v205, v248, v249\n"
    "v_add_f32 v32, v32, v249\n"
    "v_exp_f32 v250, v148\n"
    "v_exp_f32 v251, v149\n"
    "v_exp_f32 v252, v150\n"
    "v_exp_f32 v253, v151\n"
    "v_add_f32 v30, v30, v250\n"
    "v_cvt_pk_bf16_f32 v198, v250, v251\n"
    "v_add_f32 v30, v30, v251\n"
    "v_add_f32 v30, v30, v252\n"
    "v_cvt_pk_bf16_f32 v199, v252, v253\n"
    "v_add_f32 v30, v30, v253\n"
    "v_exp_f32 v244, v152\n"
    "v_exp_f32 v245, v153\n"
    "v_exp_f32 v246, v154\n"
    "v_exp_f32 v247, v155\n"
    "v_add_f32 v31, v31, v244\n"
    "v_cvt_pk_bf16_f32 v202, v244, v245\n"
    "v_add_f32 v31, v31, v245\n"
    "v_add_f32 v31, v31, v246\n"
    "v_cvt_pk_bf16_f32 v203, v246, v247\n"
    "v_add_f32 v31, v31, v247\n"
    "v_exp_f32 v248, v156\n"
    "v_exp_f32 v249, v157\n"
    "v_exp_f32 v250, v158\n"
    "v_exp_f32 v251, v159\n"
    "v_add_f32 v32, v32, v248\n"
    "v_cvt_pk_bf16_f32 v206, v248, v249\n"
    "v_add_f32 v32, v32, v249\n"
    "v_add_f32 v32, v32, v250\n"
    "v_cvt_pk_bf16_f32 v207, v250, v251\n"
    "v_add_f32 v32, v32, v251\n"
    "s_nop 3\n"
    "s_setpc_b64 s[56:57]\n"
    "Lrare1_%=:\n"
    "s_nop 7\n"
    "s_nop 3\n"
    "v_max3_f32 v244, v88, v89, v90\n"
    "v_max3_f32 v244, v244, v91, v100\n"
    "v_max3_f32 v244, v244, v101, v102\n"
    "v_max3_f32 v244, v244, v103, v136\n"
    "v_max3_f32 v244, v244, v137, v138\n"
    "v_max3_f32 v244, v244, v139, v148\n"
    "v_max3_f32 v244, v244, v149, v150\n"
    "v_max_f32 v244, v244, v151\n"
    "v_max3_f32 v245, v92, v93, v94\n"
    "v_max3_f32 v245, v245, v95, v104\n"
    "v_max3_f32 v245, v245, v105, v106\n"
    "v_max3_f32 v245, v245, v107, v140\n"
    "v_max3_f32 v245, v245, v141, v142\n"
    "v_max3_f32 v245, v245, v143, v152\n"
    "v_max3_f32 v245, v245, v153, v154\n"
    "v_max_f32 v245, v245, v155\n"
    "v_max3_f32 v246, v96, v97, v98\n"
    "v_max3_f32 v246, v246, v99, v108\n"
    "v_max3_f32 v246, v246, v109, v110\n"
    "v_max3_f32 v246, v246, v111, v144\n"
    "v_max3_f32 v246, v246, v145, v146\n"
    "v_max3_f32 v246, v246, v147, v156\n"
    "v_max3_f32 v246, v246, v157, v158\n"
    "v_max_f32 v246, v246, v159\n"
    "v_mov_b32 v247, v244\n"
    "v_mov_b32 v248, v245\n"
    "v_mov_b32 v249, v246\n"
    "v_permlane32_swap_b32 v247, v244\n"
    "v_permlane32_swap_b32 v248, v245\n"
    "v_permlane32_swap_b32 v249, v246\n"
    "v_max_f32 v244, v247, v244\n"
    "v_max_f32 v245, v248, v245\n"
    "v_max_f32 v246, v249, v246\n"
    "v_mov_b32 v247, v244\n"
    "v_mov_b32 v248, v245\n"
    "v_mov_b32 v249, v246\n"
    "v_permlane16_swap_b32 v247, v244\n"
    "v_permlane16_swap_b32 v248, v245\n"
    "v_permlane16_swap_b32 v249, v246\n"
    "v_max_f32 v244, v247, v244\n"
    "v_max_f32 v245, v248, v245\n"
    "v_max_f32 v246, v249, v246\n"
    "v_max_f32 v244, 0, v244\n"
    "v_max_f32 v245, 0, v245\n"
    "v_max_f32 v246, 0, v246\n"
    "v_exp_f32 v250, -v244\n"
    "v_exp_f32 v251, -v245\n"
    "v_exp_f32 v252, -v246\n"
    "v_sub_f32 v88, v88, v244\n"
    "v_sub_f32 v89, v89, v244\n"
    "v_sub_f32 v90, v90, v244\n"
    "v_sub_f32 v91, v91, v244\n"
    "v_sub_f32 v100, v100, v244\n"
    "v_sub_f32 v101, v101, v244\n"
    "v_sub_f32 v102, v102, v244\n"
    "v_sub_f32 v103, v103, v244\n"
    "v_sub_f32 v136, v136, v244\n"
    "v_sub_f32 v137, v137, v244\n"
    "v_sub_f32 v138, v138, v244\n"
    "v_sub_f32 v139, v139, v244\n"
    "v_sub_f32 v148, v148, v244\n"
    "v_sub_f32 v149, v149, v244\n"
    "v_sub_f32 v150, v150, v244\n"
    "v_sub_f32 v151, v151, v244\n"
    "v_sub_f32 v232, v232, v244\n"
    "v_sub_f32 v233, v233, v244\n"
    "v_sub_f32 v234, v234, v244\n"
    "v_sub_f32 v235, v235, v244\n"
    "v_mul_f32 v27, v27, v250\n"
    "v_sub_f32 v92, v92, v245\n"
    "v_sub_f32 v93, v93, v245\n"
    "v_sub_f32 v94, v94, v245\n"
    "v_sub_f32 v95, v95, v245\n"
    "v_sub_f32 v104, v104, v245\n"
    "v_sub_f32 v105, v105, v245\n"
    "v_sub_f32 v106, v106, v245\n"
    "v_sub_f32 v107, v107, v245\n"
    "v_sub_f32 v140, v140, v245\n"
    "v_sub_f32 v141, v141, v245\n"
    "v_sub_f32 v142, v142, v245\n"
    "v_sub_f32 v143, v143, v245\n"
    "v_sub_f32 v152, v152, v245\n"
    "v_sub_f32 v153, v153, v245\n"
    "v_sub_f32 v154, v154, v245\n"
    "v_sub_f32 v155, v155, v245\n"
    "v_sub_f32 v236, v236, v245\n"
    "v_sub_f32 v237, v237, v245\n"
    "v_sub_f32 v238, v238, v245\n"
    "v_sub_f32 v239, v239, v245\n"
    "v_mul_f32 v28, v28, v251\n"
    "v_sub_f32 v96, v96, v246\n"
    "v_sub_f32 v97, v97, v246\n"
    "v_sub_f32 v98, v98, v246\n"
    "v_sub_f32 v99, v99, v246\n"
    "v_sub_f32 v108, v108, v246\n"
    "v_sub_f32 v109, v109, v246\n"
    "v_sub_f32 v110, v110, v246\n"
    "v_sub_f32 v111, v111, v246\n"
    "v_sub_f32 v144, v144, v246\n"
    "v_sub_f32 v145, v145, v246\n"
    "v_sub_f32 v146, v146, v246\n"
    "v_sub_f32 v147, v147, v246\n"
    "v_sub_f32 v156, v156, v246\n"
    "v_sub_f32 v157, v157, v246\n"
    "v_sub_f32 v158, v158, v246\n"
    "v_sub_f32 v159, v159, v246\n"
    "v_sub_f32 v240, v240, v246\n"
    "v_sub_f32 v241, v241, v246\n"
    "v_sub_f32 v242, v242, v246\n"
    "v_sub_f32 v243, v243, v246\n"
    "v_mul_f32 v29, v29, v252\n"
    "v_accvgpr_read_b32 v247, a0\n"
    "v_accvgpr_read_b32 v248, a1\n"
    "v_accvgpr_read_b32 v249, a2\n"
    "v_accvgpr_read_b32 v253, a3\n"
    "v_mul_f32 v247, v247, v250\n"
    "v_mul_f32 v248, v248, v250\n"
    "v_mul_f32 v249, v249, v250\n"
    "v_mul_f32 v253, v253, v250\n"
    "v_accvgpr_write_b32 a0, v247\n"
    "v_accvgpr_write_b32 a1, v248\n"
    "v_accvgpr_write_b32 a2, v249\n"
    "v_accvgpr_write_b32 a3, v253\n"
    "v_accvgpr_read_b32 v247, a4\n"
    "v_accvgpr_read_b32 v248, a5\n"
    "v_accvgpr_read_b32 v249, a6\n"
    "v_accvgpr_read_b32 v253, a7\n"
    "v_mul_f32 v247, v247, v251\n"
    "v_mul_f32 v248, v248, v251\n"
    "v_mul_f32 v249, v249, v251\n"
    "v_mul_f32 v253, v253, v251\n"
    "v_accvgpr_write_b32 a4, v247\n"
    "v_accvgpr_write_b32 a5, v248\n"
    "v_accvgpr_write_b32 a6, v249\n"
    "v_accvgpr_write_b32 a7, v253\n"
    "v_accvgpr_read_b32 v247, a8\n"
    "v_accvgpr_read_b32 v248, a9\n"
    "v_accvgpr_read_b32 v249, a10\n"
    "v_accvgpr_read_b32 v253, a11\n"
    "v_mul_f32 v247, v247, v252\n"
    "v_mul_f32 v248, v248, v252\n"
    "v_mul_f32 v249, v249, v252\n"
    "v_mul_f32 v253, v253, v252\n"
    "v_accvgpr_write_b32 a8, v247\n"
    "v_accvgpr_write_b32 a9, v248\n"
    "v_accvgpr_write_b32 a10, v249\n"
    "v_accvgpr_write_b32 a11, v253\n"
    "v_accvgpr_read_b32 v247, a12\n"
    "v_accvgpr_read_b32 v248, a13\n"
    "v_accvgpr_read_b32 v249, a14\n"
    "v_accvgpr_read_b32 v253, a15\n"
    "v_mul_f32 v247, v247, v250\n"
    "v_mul_f32 v248, v248, v250\n"
    "v_mul_f32 v249, v249, v250\n"
    "v_mul_f32 v253, v253, v250\n"
    "v_accvgpr_write_b32 a12, v247\n"
    "v_accvgpr_write_b32 a13, v248\n"
    "v_accvgpr_write_b32 a14, v249\n"
    "v_accvgpr_write_b32 a15, v253\n"
    "v_accvgpr_read_b32 v247, a16\n"
    "v_accvgpr_read_b32 v248, a17\n"
    "v_accvgpr_read_b32 v249, a18\n"
    "v_accvgpr_read_b32 v253, a19\n"
    "v_mul_f32 v247, v247, v251\n"
    "v_mul_f32 v248, v248, v251\n"
    "v_mul_f32 v249, v249, v251\n"
    "v_mul_f32 v253, v253, v251\n"
    "v_accvgpr_write_b32 a16, v247\n"
    "v_accvgpr_write_b32 a17, v248\n"
    "v_accvgpr_write_b32 a18, v249\n"
    "v_accvgpr_write_b32 a19, v253\n"
    "v_accvgpr_read_b32 v247, a20\n"
    "v_accvgpr_read_b32 v248, a21\n"
    "v_accvgpr_read_b32 v249, a22\n"
    "v_accvgpr_read_b32 v253, a23\n"
    "v_mul_f32 v247, v247, v252\n"
    "v_mul_f32 v248, v248, v252\n"
    "v_mul_f32 v249, v249, v252\n"
    "v_mul_f32 v253, v253, v252\n"
    "v_accvgpr_write_b32 a20, v247\n"
    "v_accvgpr_write_b32 a21, v248\n"
    "v_accvgpr_write_b32 a22, v249\n"
    "v_accvgpr_write_b32 a23, v253\n"
    "v_accvgpr_read_b32 v247, a24\n"
    "v_accvgpr_read_b32 v248, a25\n"
    "v_accvgpr_read_b32 v249, a26\n"
    "v_accvgpr_read_b32 v253, a27\n"
    "v_mul_f32 v247, v247, v250\n"
    "v_mul_f32 v248, v248, v250\n"
    "v_mul_f32 v249, v249, v250\n"
    "v_mul_f32 v253, v253, v250\n"
    "v_accvgpr_write_b32 a24, v247\n"
    "v_accvgpr_write_b32 a25, v248\n"
    "v_accvgpr_write_b32 a26, v249\n"
    "v_accvgpr_write_b32 a27, v253\n"
    "v_accvgpr_read_b32 v247, a28\n"
    "v_accvgpr_read_b32 v248, a29\n"
    "v_accvgpr_read_b32 v249, a30\n"
    "v_accvgpr_read_b32 v253, a31\n"
    "v_mul_f32 v247, v247, v251\n"
    "v_mul_f32 v248, v248, v251\n"
    "v_mul_f32 v249, v249, v251\n"
    "v_mul_f32 v253, v253, v251\n"
    "v_accvgpr_write_b32 a28, v247\n"
    "v_accvgpr_write_b32 a29, v248\n"
    "v_accvgpr_write_b32 a30, v249\n"
    "v_accvgpr_write_b32 a31, v253\n"
    "v_accvgpr_read_b32 v247, a32\n"
    "v_accvgpr_read_b32 v248, a33\n"
    "v_accvgpr_read_b32 v249, a34\n"
    "v_accvgpr_read_b32 v253, a35\n"
    "v_mul_f32 v247, v247, v252\n"
    "v_mul_f32 v248, v248, v252\n"
    "v_mul_f32 v249, v249, v252\n"
    "v_mul_f32 v253, v253, v252\n"
    "v_accvgpr_write_b32 a32, v247\n"
    "v_accvgpr_write_b32 a33, v248\n"
    "v_accvgpr_write_b32 a34, v249\n"
    "v_accvgpr_write_b32 a35, v253\n"
    "v_accvgpr_read_b32 v247, a36\n"
    "v_accvgpr_read_b32 v248, a37\n"
    "v_accvgpr_read_b32 v249, a38\n"
    "v_accvgpr_read_b32 v253, a39\n"
    "v_mul_f32 v247, v247, v250\n"
    "v_mul_f32 v248, v248, v250\n"
    "v_mul_f32 v249, v249, v250\n"
    "v_mul_f32 v253, v253, v250\n"
    "v_accvgpr_write_b32 a36, v247\n"
    "v_accvgpr_write_b32 a37, v248\n"
    "v_accvgpr_write_b32 a38, v249\n"
    "v_accvgpr_write_b32 a39, v253\n"
    "v_accvgpr_read_b32 v247, a40\n"
    "v_accvgpr_read_b32 v248, a41\n"
    "v_accvgpr_read_b32 v249, a42\n"
    "v_accvgpr_read_b32 v253, a43\n"
    "v_mul_f32 v247, v247, v251\n"
    "v_mul_f32 v248, v248, v251\n"
    "v_mul_f32 v249, v249, v251\n"
    "v_mul_f32 v253, v253, v251\n"
    "v_accvgpr_write_b32 a40, v247\n"
    "v_accvgpr_write_b32 a41, v248\n"
    "v_accvgpr_write_b32 a42, v249\n"
    "v_accvgpr_write_b32 a43, v253\n"
    "v_accvgpr_read_b32 v247, a44\n"
    "v_accvgpr_read_b32 v248, a45\n"
    "v_accvgpr_read_b32 v249, a46\n"
    "v_accvgpr_read_b32 v253, a47\n"
    "v_mul_f32 v247, v247, v252\n"
    "v_mul_f32 v248, v248, v252\n"
    "v_mul_f32 v249, v249, v252\n"
    "v_mul_f32 v253, v253, v252\n"
    "v_accvgpr_write_b32 a44, v247\n"
    "v_accvgpr_write_b32 a45, v248\n"
    "v_accvgpr_write_b32 a46, v249\n"
    "v_accvgpr_write_b32 a47, v253\n"
    "v_accvgpr_read_b32 v247, a48\n"
    "v_accvgpr_read_b32 v248, a49\n"
    "v_accvgpr_read_b32 v249, a50\n"
    "v_accvgpr_read_b32 v253, a51\n"
    "v_mul_f32 v247, v247, v250\n"
    "v_mul_f32 v248, v248, v250\n"
    "v_mul_f32 v249, v249, v250\n"
    "v_mul_f32 v253, v253, v250\n"
    "v_accvgpr_write_b32 a48, v247\n"
    "v_accvgpr_write_b32 a49, v248\n"
    "v_accvgpr_write_b32 a50, v249\n"
    "v_accvgpr_write_b32 a51, v253\n"
    "v_accvgpr_read_b32 v247, a52\n"
    "v_accvgpr_read_b32 v248, a53\n"
    "v_accvgpr_read_b32 v249, a54\n"
    "v_accvgpr_read_b32 v253, a55\n"
    "v_mul_f32 v247, v247, v251\n"
    "v_mul_f32 v248, v248, v251\n"
    "v_mul_f32 v249, v249, v251\n"
    "v_mul_f32 v253, v253, v251\n"
    "v_accvgpr_write_b32 a52, v247\n"
    "v_accvgpr_write_b32 a53, v248\n"
    "v_accvgpr_write_b32 a54, v249\n"
    "v_accvgpr_write_b32 a55, v253\n"
    "v_accvgpr_read_b32 v247, a56\n"
    "v_accvgpr_read_b32 v248, a57\n"
    "v_accvgpr_read_b32 v249, a58\n"
    "v_accvgpr_read_b32 v253, a59\n"
    "v_mul_f32 v247, v247, v252\n"
    "v_mul_f32 v248, v248, v252\n"
    "v_mul_f32 v249, v249, v252\n"
    "v_mul_f32 v253, v253, v252\n"
    "v_accvgpr_write_b32 a56, v247\n"
    "v_accvgpr_write_b32 a57, v248\n"
    "v_accvgpr_write_b32 a58, v249\n"
    "v_accvgpr_write_b32 a59, v253\n"
    "v_accvgpr_read_b32 v247, a60\n"
    "v_accvgpr_read_b32 v248, a61\n"
    "v_accvgpr_read_b32 v249, a62\n"
    "v_accvgpr_read_b32 v253, a63\n"
    "v_mul_f32 v247, v247, v250\n"
    "v_mul_f32 v248, v248, v250\n"
    "v_mul_f32 v249, v249, v250\n"
    "v_mul_f32 v253, v253, v250\n"
    "v_accvgpr_write_b32 a60, v247\n"
    "v_accvgpr_write_b32 a61, v248\n"
    "v_accvgpr_write_b32 a62, v249\n"
    "v_accvgpr_write_b32 a63, v253\n"
    "v_accvgpr_read_b32 v247, a64\n"
    "v_accvgpr_read_b32 v248, a65\n"
    "v_accvgpr_read_b32 v249, a66\n"
    "v_accvgpr_read_b32 v253, a67\n"
    "v_mul_f32 v247, v247, v251\n"
    "v_mul_f32 v248, v248, v251\n"
    "v_mul_f32 v249, v249, v251\n"
    "v_mul_f32 v253, v253, v251\n"
    "v_accvgpr_write_b32 a64, v247\n"
    "v_accvgpr_write_b32 a65, v248\n"
    "v_accvgpr_write_b32 a66, v249\n"
    "v_accvgpr_write_b32 a67, v253\n"
    "v_accvgpr_read_b32 v247, a68\n"
    "v_accvgpr_read_b32 v248, a69\n"
    "v_accvgpr_read_b32 v249, a70\n"
    "v_accvgpr_read_b32 v253, a71\n"
    "v_mul_f32 v247, v247, v252\n"
    "v_mul_f32 v248, v248, v252\n"
    "v_mul_f32 v249, v249, v252\n"
    "v_mul_f32 v253, v253, v252\n"
    "v_accvgpr_write_b32 a68, v247\n"
    "v_accvgpr_write_b32 a69, v248\n"
    "v_accvgpr_write_b32 a70, v249\n"
    "v_accvgpr_write_b32 a71, v253\n"
    "v_accvgpr_read_b32 v247, a72\n"
    "v_accvgpr_read_b32 v248, a73\n"
    "v_accvgpr_read_b32 v249, a74\n"
    "v_accvgpr_read_b32 v253, a75\n"
    "v_mul_f32 v247, v247, v250\n"
    "v_mul_f32 v248, v248, v250\n"
    "v_mul_f32 v249, v249, v250\n"
    "v_mul_f32 v253, v253, v250\n"
    "v_accvgpr_write_b32 a72, v247\n"
    "v_accvgpr_write_b32 a73, v248\n"
    "v_accvgpr_write_b32 a74, v249\n"
    "v_accvgpr_write_b32 a75, v253\n"
    "v_accvgpr_read_b32 v247, a76\n"
    "v_accvgpr_read_b32 v248, a77\n"
    "v_accvgpr_read_b32 v249, a78\n"
    "v_accvgpr_read_b32 v253, a79\n"
    "v_mul_f32 v247, v247, v251\n"
    "v_mul_f32 v248, v248, v251\n"
    "v_mul_f32 v249, v249, v251\n"
    "v_mul_f32 v253, v253, v251\n"
    "v_accvgpr_write_b32 a76, v247\n"
    "v_accvgpr_write_b32 a77, v248\n"
    "v_accvgpr_write_b32 a78, v249\n"
    "v_accvgpr_write_b32 a79, v253\n"
    "v_accvgpr_read_b32 v247, a80\n"
    "v_accvgpr_read_b32 v248, a81\n"
    "v_accvgpr_read_b32 v249, a82\n"
    "v_accvgpr_read_b32 v253, a83\n"
    "v_mul_f32 v247, v247, v252\n"
    "v_mul_f32 v248, v248, v252\n"
    "v_mul_f32 v249, v249, v252\n"
    "v_mul_f32 v253, v253, v252\n"
    "v_accvgpr_write_b32 a80, v247\n"
    "v_accvgpr_write_b32 a81, v248\n"
    "v_accvgpr_write_b32 a82, v249\n"
    "v_accvgpr_write_b32 a83, v253\n"
    "v_accvgpr_read_b32 v247, a84\n"
    "v_accvgpr_read_b32 v248, a85\n"
    "v_accvgpr_read_b32 v249, a86\n"
    "v_accvgpr_read_b32 v253, a87\n"
    "v_mul_f32 v247, v247, v250\n"
    "v_mul_f32 v248, v248, v250\n"
    "v_mul_f32 v249, v249, v250\n"
    "v_mul_f32 v253, v253, v250\n"
    "v_accvgpr_write_b32 a84, v247\n"
    "v_accvgpr_write_b32 a85, v248\n"
    "v_accvgpr_write_b32 a86, v249\n"
    "v_accvgpr_write_b32 a87, v253\n"
    "v_accvgpr_read_b32 v247, a88\n"
    "v_accvgpr_read_b32 v248, a89\n"
    "v_accvgpr_read_b32 v249, a90\n"
    "v_accvgpr_read_b32 v253, a91\n"
    "v_mul_f32 v247, v247, v251\n"
    "v_mul_f32 v248, v248, v251\n"
    "v_mul_f32 v249, v249, v251\n"
    "v_mul_f32 v253, v253, v251\n"
    "v_accvgpr_write_b32 a88, v247\n"
    "v_accvgpr_write_b32 a89, v248\n"
    "v_accvgpr_write_b32 a90, v249\n"
    "v_accvgpr_write_b32 a91, v253\n"
    "v_accvgpr_read_b32 v247, a92\n"
    "v_accvgpr_read_b32 v248, a93\n"
    "v_accvgpr_read_b32 v249, a94\n"
    "v_accvgpr_read_b32 v253, a95\n"
    "v_mul_f32 v247, v247, v252\n"
    "v_mul_f32 v248, v248, v252\n"
    "v_mul_f32 v249, v249, v252\n"
    "v_mul_f32 v253, v253, v252\n"
    "v_accvgpr_write_b32 a92, v247\n"
    "v_accvgpr_write_b32 a93, v248\n"
    "v_accvgpr_write_b32 a94, v249\n"
    "v_accvgpr_write_b32 a95, v253\n"
    "v_accvgpr_read_b32 v247, a96\n"
    "v_accvgpr_read_b32 v248, a97\n"
    "v_accvgpr_read_b32 v249, a98\n"
    "v_accvgpr_read_b32 v253, a99\n"
    "v_mul_f32 v247, v247, v250\n"
    "v_mul_f32 v248, v248, v250\n"
    "v_mul_f32 v249, v249, v250\n"
    "v_mul_f32 v253, v253, v250\n"
    "v_accvgpr_write_b32 a96, v247\n"
    "v_accvgpr_write_b32 a97, v248\n"
    "v_accvgpr_write_b32 a98, v249\n"
    "v_accvgpr_write_b32 a99, v253\n"
    "v_accvgpr_read_b32 v247, a100\n"
    "v_accvgpr_read_b32 v248, a101\n"
    "v_accvgpr_read_b32 v249, a102\n"
    "v_accvgpr_read_b32 v253, a103\n"
    "v_mul_f32 v247, v247, v251\n"
    "v_mul_f32 v248, v248, v251\n"
    "v_mul_f32 v249, v249, v251\n"
    "v_mul_f32 v253, v253, v251\n"
    "v_accvgpr_write_b32 a100, v247\n"
    "v_accvgpr_write_b32 a101, v248\n"
    "v_accvgpr_write_b32 a102, v249\n"
    "v_accvgpr_write_b32 a103, v253\n"
    "v_accvgpr_read_b32 v247, a104\n"
    "v_accvgpr_read_b32 v248, a105\n"
    "v_accvgpr_read_b32 v249, a106\n"
    "v_accvgpr_read_b32 v253, a107\n"
    "v_mul_f32 v247, v247, v252\n"
    "v_mul_f32 v248, v248, v252\n"
    "v_mul_f32 v249, v249, v252\n"
    "v_mul_f32 v253, v253, v252\n"
    "v_accvgpr_write_b32 a104, v247\n"
    "v_accvgpr_write_b32 a105, v248\n"
    "v_accvgpr_write_b32 a106, v249\n"
    "v_accvgpr_write_b32 a107, v253\n"
    "v_accvgpr_read_b32 v247, a108\n"
    "v_accvgpr_read_b32 v248, a109\n"
    "v_accvgpr_read_b32 v249, a110\n"
    "v_accvgpr_read_b32 v253, a111\n"
    "v_mul_f32 v247, v247, v250\n"
    "v_mul_f32 v248, v248, v250\n"
    "v_mul_f32 v249, v249, v250\n"
    "v_mul_f32 v253, v253, v250\n"
    "v_accvgpr_write_b32 a108, v247\n"
    "v_accvgpr_write_b32 a109, v248\n"
    "v_accvgpr_write_b32 a110, v249\n"
    "v_accvgpr_write_b32 a111, v253\n"
    "v_accvgpr_read_b32 v247, a112\n"
    "v_accvgpr_read_b32 v248, a113\n"
    "v_accvgpr_read_b32 v249, a114\n"
    "v_accvgpr_read_b32 v253, a115\n"
    "v_mul_f32 v247, v247, v251\n"
    "v_mul_f32 v248, v248, v251\n"
    "v_mul_f32 v249, v249, v251\n"
    "v_mul_f32 v253, v253, v251\n"
    "v_accvgpr_write_b32 a112, v247\n"
    "v_accvgpr_write_b32 a113, v248\n"
    "v_accvgpr_write_b32 a114, v249\n"
    "v_accvgpr_write_b32 a115, v253\n"
    "v_accvgpr_read_b32 v247, a116\n"
    "v_accvgpr_read_b32 v248, a117\n"
    "v_accvgpr_read_b32 v249, a118\n"
    "v_accvgpr_read_b32 v253, a119\n"
    "v_mul_f32 v247, v247, v252\n"
    "v_mul_f32 v248, v248, v252\n"
    "v_mul_f32 v249, v249, v252\n"
    "v_mul_f32 v253, v253, v252\n"
    "v_accvgpr_write_b32 a116, v247\n"
    "v_accvgpr_write_b32 a117, v248\n"
    "v_accvgpr_write_b32 a118, v249\n"
    "v_accvgpr_write_b32 a119, v253\n"
    "v_accvgpr_read_b32 v247, a120\n"
    "v_accvgpr_read_b32 v248, a121\n"
    "v_accvgpr_read_b32 v249, a122\n"
    "v_accvgpr_read_b32 v253, a123\n"
    "v_mul_f32 v247, v247, v250\n"
    "v_mul_f32 v248, v248, v250\n"
    "v_mul_f32 v249, v249, v250\n"
    "v_mul_f32 v253, v253, v250\n"
    "v_accvgpr_write_b32 a120, v247\n"
    "v_accvgpr_write_b32 a121, v248\n"
    "v_accvgpr_write_b32 a122, v249\n"
    "v_accvgpr_write_b32 a123, v253\n"
    "v_accvgpr_read_b32 v247, a124\n"
    "v_accvgpr_read_b32 v248, a125\n"
    "v_accvgpr_read_b32 v249, a126\n"
    "v_accvgpr_read_b32 v253, a127\n"
    "v_mul_f32 v247, v247, v251\n"
    "v_mul_f32 v248, v248, v251\n"
    "v_mul_f32 v249, v249, v251\n"
    "v_mul_f32 v253, v253, v251\n"
    "v_accvgpr_write_b32 a124, v247\n"
    "v_accvgpr_write_b32 a125, v248\n"
    "v_accvgpr_write_b32 a126, v249\n"
    "v_accvgpr_write_b32 a127, v253\n"
    "v_accvgpr_read_b32 v247, a128\n"
    "v_accvgpr_read_b32 v248, a129\n"
    "v_accvgpr_read_b32 v249, a130\n"
    "v_accvgpr_read_b32 v253, a131\n"
    "v_mul_f32 v247, v247, v252\n"
    "v_mul_f32 v248, v248, v252\n"
    "v_mul_f32 v249, v249, v252\n"
    "v_mul_f32 v253, v253, v252\n"
    "v_accvgpr_write_b32 a128, v247\n"
    "v_accvgpr_write_b32 a129, v248\n"
    "v_accvgpr_write_b32 a130, v249\n"
    "v_accvgpr_write_b32 a131, v253\n"
    "v_accvgpr_read_b32 v247, a132\n"
    "v_accvgpr_read_b32 v248, a133\n"
    "v_accvgpr_read_b32 v249, a134\n"
    "v_accvgpr_read_b32 v253, a135\n"
    "v_mul_f32 v247, v247, v250\n"
    "v_mul_f32 v248, v248, v250\n"
    "v_mul_f32 v249, v249, v250\n"
    "v_mul_f32 v253, v253, v250\n"
    "v_accvgpr_write_b32 a132, v247\n"
    "v_accvgpr_write_b32 a133, v248\n"
    "v_accvgpr_write_b32 a134, v249\n"
    "v_accvgpr_write_b32 a135, v253\n"
    "v_accvgpr_read_b32 v247, a136\n"
    "v_accvgpr_read_b32 v248, a137\n"
    "v_accvgpr_read_b32 v249, a138\n"
    "v_accvgpr_read_b32 v253, a139\n"
    "v_mul_f32 v247, v247, v251\n"
    "v_mul_f32 v248, v248, v251\n"
    "v_mul_f32 v249, v249, v251\n"
    "v_mul_f32 v253, v253, v251\n"
    "v_accvgpr_write_b32 a136, v247\n"
    "v_accvgpr_write_b32 a137, v248\n"
    "v_accvgpr_write_b32 a138, v249\n"
    "v_accvgpr_write_b32 a139, v253\n"
    "v_accvgpr_read_b32 v247, a140\n"
    "v_accvgpr_read_b32 v248, a141\n"
    "v_accvgpr_read_b32 v249, a142\n"
    "v_accvgpr_read_b32 v253, a143\n"
    "v_mul_f32 v247, v247, v252\n"
    "v_mul_f32 v248, v248, v252\n"
    "v_mul_f32 v249, v249, v252\n"
    "v_mul_f32 v253, v253, v252\n"
    "v_accvgpr_write_b32 a140, v247\n"
    "v_accvgpr_write_b32 a141, v248\n"
    "v_accvgpr_write_b32 a142, v249\n"
    "v_accvgpr_write_b32 a143, v253\n"
    "v_accvgpr_read_b32 v247, a144\n"
    "v_accvgpr_read_b32 v248, a145\n"
    "v_accvgpr_read_b32 v249, a146\n"
    "v_accvgpr_read_b32 v253, a147\n"
    "v_mul_f32 v247, v247, v250\n"
    "v_mul_f32 v248, v248, v250\n"
    "v_mul_f32 v249, v249, v250\n"
    "v_mul_f32 v253, v253, v250\n"
    "v_accvgpr_write_b32 a144, v247\n"
    "v_accvgpr_write_b32 a145, v248\n"
    "v_accvgpr_write_b32 a146, v249\n"
    "v_accvgpr_write_b32 a147, v253\n"
    "v_accvgpr_read_b32 v247, a148\n"
    "v_accvgpr_read_b32 v248, a149\n"
    "v_accvgpr_read_b32 v249, a150\n"
    "v_accvgpr_read_b32 v253, a151\n"
    "v_mul_f32 v247, v247, v251\n"
    "v_mul_f32 v248, v248, v251\n"
    "v_mul_f32 v249, v249, v251\n"
    "v_mul_f32 v253, v253, v251\n"
    "v_accvgpr_write_b32 a148, v247\n"
    "v_accvgpr_write_b32 a149, v248\n"
    "v_accvgpr_write_b32 a150, v249\n"
    "v_accvgpr_write_b32 a151, v253\n"
    "v_accvgpr_read_b32 v247, a152\n"
    "v_accvgpr_read_b32 v248, a153\n"
    "v_accvgpr_read_b32 v249, a154\n"
    "v_accvgpr_read_b32 v253, a155\n"
    "v_mul_f32 v247, v247, v252\n"
    "v_mul_f32 v248, v248, v252\n"
    "v_mul_f32 v249, v249, v252\n"
    "v_mul_f32 v253, v253, v252\n"
    "v_accvgpr_write_b32 a152, v247\n"
    "v_accvgpr_write_b32 a153, v248\n"
    "v_accvgpr_write_b32 a154, v249\n"
    "v_accvgpr_write_b32 a155, v253\n"
    "v_accvgpr_read_b32 v247, a156\n"
    "v_accvgpr_read_b32 v248, a157\n"
    "v_accvgpr_read_b32 v249, a158\n"
    "v_accvgpr_read_b32 v253, a159\n"
    "v_mul_f32 v247, v247, v250\n"
    "v_mul_f32 v248, v248, v250\n"
    "v_mul_f32 v249, v249, v250\n"
    "v_mul_f32 v253, v253, v250\n"
    "v_accvgpr_write_b32 a156, v247\n"
    "v_accvgpr_write_b32 a157, v248\n"
    "v_accvgpr_write_b32 a158, v249\n"
    "v_accvgpr_write_b32 a159, v253\n"
    "v_accvgpr_read_b32 v247, a160\n"
    "v_accvgpr_read_b32 v248, a161\n"
    "v_accvgpr_read_b32 v249, a162\n"
    "v_accvgpr_read_b32 v253, a163\n"
    "v_mul_f32 v247, v247, v251\n"
    "v_mul_f32 v248, v248, v251\n"
    "v_mul_f32 v249, v249, v251\n"
    "v_mul_f32 v253, v253, v251\n"
    "v_accvgpr_write_b32 a160, v247\n"
    "v_accvgpr_write_b32 a161, v248\n"
    "v_accvgpr_write_b32 a162, v249\n"
    "v_accvgpr_write_b32 a163, v253\n"
    "v_accvgpr_read_b32 v247, a164\n"
    "v_accvgpr_read_b32 v248, a165\n"
    "v_accvgpr_read_b32 v249, a166\n"
    "v_accvgpr_read_b32 v253, a167\n"
    "v_mul_f32 v247, v247, v252\n"
    "v_mul_f32 v248, v248, v252\n"
    "v_mul_f32 v249, v249, v252\n"
    "v_mul_f32 v253, v253, v252\n"
    "v_accvgpr_write_b32 a164, v247\n"
    "v_accvgpr_write_b32 a165, v248\n"
    "v_accvgpr_write_b32 a166, v249\n"
    "v_accvgpr_write_b32 a167, v253\n"
    "v_accvgpr_read_b32 v247, a168\n"
    "v_accvgpr_read_b32 v248, a169\n"
    "v_accvgpr_read_b32 v249, a170\n"
    "v_accvgpr_read_b32 v253, a171\n"
    "v_mul_f32 v247, v247, v250\n"
    "v_mul_f32 v248, v248, v250\n"
    "v_mul_f32 v249, v249, v250\n"
    "v_mul_f32 v253, v253, v250\n"
    "v_accvgpr_write_b32 a168, v247\n"
    "v_accvgpr_write_b32 a169, v248\n"
    "v_accvgpr_write_b32 a170, v249\n"
    "v_accvgpr_write_b32 a171, v253\n"
    "v_accvgpr_read_b32 v247, a172\n"
    "v_accvgpr_read_b32 v248, a173\n"
    "v_accvgpr_read_b32 v249, a174\n"
    "v_accvgpr_read_b32 v253, a175\n"
    "v_mul_f32 v247, v247, v251\n"
    "v_mul_f32 v248, v248, v251\n"
    "v_mul_f32 v249, v249, v251\n"
    "v_mul_f32 v253, v253, v251\n"
    "v_accvgpr_write_b32 a172, v247\n"
    "v_accvgpr_write_b32 a173, v248\n"
    "v_accvgpr_write_b32 a174, v249\n"
    "v_accvgpr_write_b32 a175, v253\n"
    "v_accvgpr_read_b32 v247, a176\n"
    "v_accvgpr_read_b32 v248, a177\n"
    "v_accvgpr_read_b32 v249, a178\n"
    "v_accvgpr_read_b32 v253, a179\n"
    "v_mul_f32 v247, v247, v252\n"
    "v_mul_f32 v248, v248, v252\n"
    "v_mul_f32 v249, v249, v252\n"
    "v_mul_f32 v253, v253, v252\n"
    "v_accvgpr_write_b32 a176, v247\n"
    "v_accvgpr_write_b32 a177, v248\n"
    "v_accvgpr_write_b32 a178, v249\n"
    "v_accvgpr_write_b32 a179, v253\n"
    "v_accvgpr_read_b32 v247, a180\n"
    "v_accvgpr_read_b32 v248, a181\n"
    "v_accvgpr_read_b32 v249, a182\n"
    "v_accvgpr_read_b32 v253, a183\n"
    "v_mul_f32 v247, v247, v250\n"
    "v_mul_f32 v248, v248, v250\n"
    "v_mul_f32 v249, v249, v250\n"
    "v_mul_f32 v253, v253, v250\n"
    "v_accvgpr_write_b32 a180, v247\n"
    "v_accvgpr_write_b32 a181, v248\n"
    "v_accvgpr_write_b32 a182, v249\n"
    "v_accvgpr_write_b32 a183, v253\n"
    "v_accvgpr_read_b32 v247, a184\n"
    "v_accvgpr_read_b32 v248, a185\n"
    "v_accvgpr_read_b32 v249, a186\n"
    "v_accvgpr_read_b32 v253, a187\n"
    "v_mul_f32 v247, v247, v251\n"
    "v_mul_f32 v248, v248, v251\n"
    "v_mul_f32 v249, v249, v251\n"
    "v_mul_f32 v253, v253, v251\n"
    "v_accvgpr_write_b32 a184, v247\n"
    "v_accvgpr_write_b32 a185, v248\n"
    "v_accvgpr_write_b32 a186, v249\n"
    "v_accvgpr_write_b32 a187, v253\n"
    "v_accvgpr_read_b32 v247, a188\n"
    "v_accvgpr_read_b32 v248, a189\n"
    "v_accvgpr_read_b32 v249, a190\n"
    "v_accvgpr_read_b32 v253, a191\n"
    "v_mul_f32 v247, v247, v252\n"
    "v_mul_f32 v248, v248, v252\n"
    "v_mul_f32 v249, v249, v252\n"
    "v_mul_f32 v253, v253, v252\n"
    "v_accvgpr_write_b32 a188, v247\n"
    "v_accvgpr_write_b32 a189, v248\n"
    "v_accvgpr_write_b32 a190, v249\n"
    "v_accvgpr_write_b32 a191, v253\n"
    "v_exp_f32 v244, v88\n"
    "v_exp_f32 v245, v89\n"
    "v_exp_f32 v246, v90\n"
    "v_exp_f32 v247, v91\n"
    "v_add_f32 v30, v244, v245\n"
    "v_cvt_pk_bf16_f32 v40, v244, v245\n"
    "v_add_f32 v30, v30, v246\n"
    "v_cvt_pk_bf16_f32 v41, v246, v247\n"
    "v_add_f32 v30, v30, v247\n"
    "v_exp_f32 v248, v92\n"
    "v_exp_f32 v249, v93\n"
    "v_exp_f32 v250, v94\n"
    "v_exp_f32 v251, v95\n"
    "v_add_f32 v31, v248, v249\n"
    "v_cvt_pk_bf16_f32 v44, v248, v249\n"
    "v_add_f32 v31, v31, v250\n"
    "v_cvt_pk_bf16_f32 v45, v250, v251\n"
    "v_add_f32 v31, v31, v251\n"
    "v_exp_f32 v252, v96\n"
    "v_exp_f32 v253, v97\n"
    "v_exp_f32 v244, v98\n"
    "v_exp_f32 v245, v99\n"
    "v_add_f32 v32, v252, v253\n"
    "v_cvt_pk_bf16_f32 v48, v252, v253\n"
    "v_add_f32 v32, v32, v244\n"
    "v_cvt_pk_bf16_f32 v49, v244, v245\n"
    "v_add_f32 v32, v32, v245\n"
    "v_exp_f32 v246, v100\n"
    "v_exp_f32 v247, v101\n"
    "v_exp_f32 v248, v102\n"
    "v_exp_f32 v249, v103\n"
    "v_add_f32 v30, v30, v246\n"
    "v_cvt_pk_bf16_f32 v42, v246, v247\n"
    "v_add_f32 v30, v30, v247\n"
    "v_add_f32 v30, v30, v248\n"
    "v_cvt_pk_bf16_f32 v43, v248, v249\n"
    "v_add_f32 v30, v30, v249\n"
    "v_exp_f32 v250, v104\n"
    "v_exp_f32 v251, v105\n"
    "v_exp_f32 v252, v106\n"
    "v_exp_f32 v253, v107\n"
    "v_add_f32 v31, v31, v250\n"
    "v_cvt_pk_bf16_f32 v46, v250, v251\n"
    "v_add_f32 v31, v31, v251\n"
    "v_add_f32 v31, v31, v252\n"
    "v_cvt_pk_bf16_f32 v47, v252, v253\n"
    "v_add_f32 v31, v31, v253\n"
    "v_exp_f32 v244, v108\n"
    "v_exp_f32 v245, v109\n"
    "v_exp_f32 v246, v110\n"
    "v_exp_f32 v247, v111\n"
    "v_add_f32 v32, v32, v244\n"
    "v_cvt_pk_bf16_f32 v50, v244, v245\n"
    "v_add_f32 v32, v32, v245\n"
    "v_add_f32 v32, v32, v246\n"
    "v_cvt_pk_bf16_f32 v51, v246, v247\n"
    "v_add_f32 v32, v32, v247\n"
    "v_exp_f32 v248, v136\n"
    "v_exp_f32 v249, v137\n"
    "v_exp_f32 v250, v138\n"
    "v_exp_f32 v251, v139\n"
    "v_add_f32 v30, v30, v248\n"
    "v_cvt_pk_bf16_f32 v52, v248, v249\n"
    "v_add_f32 v30, v30, v249\n"
    "v_add_f32 v30, v30, v250\n"
    "v_cvt_pk_bf16_f32 v53, v250, v251\n"
    "v_add_f32 v30, v30, v251\n"
    "v_exp_f32 v252, v140\n"
    "v_exp_f32 v253, v141\n"
    "v_exp_f32 v244, v142\n"
    "v_exp_f32 v245, v143\n"
    "v_add_f32 v31, v31, v252\n"
    "v_cvt_pk_bf16_f32 v56, v252, v253\n"
    "v_add_f32 v31, v31, v253\n"
    "v_add_f32 v31, v31, v244\n"
    "v_cvt_pk_bf16_f32 v57, v244, v245\n"
    "v_add_f32 v31, v31, v245\n"
    "v_exp_f32 v246, v144\n"
    "v_exp_f32 v247, v145\n"
    "v_exp_f32 v248, v146\n"
    "v_exp_f32 v249, v147\n"
    "v_add_f32 v32, v32, v246\n"
    "v_cvt_pk_bf16_f32 v60, v246, v247\n"
    "v_add_f32 v32, v32, v247\n"
    "v_add_f32 v32, v32, v248\n"
    "v_cvt_pk_bf16_f32 v61, v248, v249\n"
    "v_add_f32 v32, v32, v249\n"
    "v_exp_f32 v250, v148\n"
    "v_exp_f32 v251, v149\n"
    "v_exp_f32 v252, v150\n"
    "v_exp_f32 v253, v151\n"
    "v_add_f32 v30, v30, v250\n"
    "v_cvt_pk_bf16_f32 v54, v250, v251\n"
    "v_add_f32 v30, v30, v251\n"
    "v_add_f32 v30, v30, v252\n"
    "v_cvt_pk_bf16_f32 v55, v252, v253\n"
    "v_add_f32 v30, v30, v253\n"
    "v_exp_f32 v244, v152\n"
    "v_exp_f32 v245, v153\n"
    "v_exp_f32 v246, v154\n"
    "v_exp_f32 v247, v155\n"
    "v_add_f32 v31, v31, v244\n"
    "v_cvt_pk_bf16_f32 v58, v244, v245\n"
    "v_add_f32 v31, v31, v245\n"
    "v_add_f32 v31, v31, v246\n"
    "v_cvt_pk_bf16_f32 v59, v246, v247\n"
    "v_add_f32 v31, v31, v247\n"
    "v_exp_f32 v248, v156\n"
    "v_exp_f32 v249, v157\n"
    "v_exp_f32 v250, v158\n"
    "v_exp_f32 v251, v159\n"
    "v_add_f32 v32, v32, v248\n"
    "v_cvt_pk_bf16_f32 v62, v248, v249\n"
    "v_add_f32 v32, v32, v249\n"
    "v_add_f32 v32, v32, v250\n"
    "v_cvt_pk_bf16_f32 v63, v250, v251\n"
    "v_add_f32 v32, v32, v251\n"
    "s_nop 3\n"
    "s_setpc_b64 s[56:57]\n"
    "Lstub1_%=:\n"
    "s_swappc_b64 s[56:57], s[52:53]\n"
    "s_branch Lok1_%=\n"
    "Lstub2_%=:\n"
    "s_swappc_b64 s[56:57], s[54:55]\n"
    "s_branch Lok2_%=\n"
    "Lstub3_%=:\n"
    "s_swappc_b64 s[56:57], s[52:53]\n"
    "s_branch Lok3_%=\n"
    "Lstub4_%=:\n"
    "s_swappc_b64 s[56:57], s[54:55]\n"
    "s_branch Lok4_%=\n"
    "Lstub5_%=:\n"
    "s_swappc_b64 s[56:57], s[52:53]\n"
    "s_branch Lok5_%=\n"
    "Lstub6_%=:\n"
    "s_swappc_b64 s[56:57], s[54:55]\n"
    "s_branch Lok6_%=\n"
    "Lend_%=:\n"
    :: "s"(__builtin_amdgcn_kernarg_segment_ptr()), "s"(blockIdx.x), "s"((unsigned)(size_t)lds), "v"(threadIdx.x)
    : "v1", "v2", "v3", "v4", "v5", "v6", "v7", "v8", "v9", "v10", "v11", "v12", "v13", "v14", "v15", "v16", "v17", "v18", "v19", "v20", "v21", "v22", "v23", "v24", "v25", "v26", "v27", "v28", "v29", "v30", "v31", "v32", "v33", "v34", "v35", "v36", "v37", "v38", "v39", "v40", "v41", "v42", "v43", "v44", "v45", "v46", "v47", "v48", "v49", "v50", "v51", "v52", "v53", "v54", "v55", "v56", "v57", "v58", "v59", "v60", "v61", "v62", "v63", "v64", "v65", "v66", "v67", "v68", "v69", "v70", "v71", "v72", "v73", "v74", "v75", "v76", "v77", "v78", "v79", "v80", "v81", "v82", "v83", "v84", "v85", "v86", "v87", "v88", "v89", "v90", "v91", "v92", "v93", "v94", "v95", "v96", "v97", "v98", "v99", "v100", "v101", "v102", "v103", "v104", "v105", "v106", "v107", "v108", "v109", "v110", "v111", "v112", "v113", "v114", "v115", "v116", "v117", "v118", "v119", "v120", "v121", "v122", "v123", "v124", "v125", "v126", "v127", "v128", "v129", "v130", "v131", "v132", "v133", "v134", "v135", "v136", "v137", "v138", "v139", "v140", "v141", "v142", "v143", "v144", "v145", "v146", "v147", "v148", "v149", "v150", "v151", "v152", "v153", "v154", "v155", "v156", "v157", "v158", "v159", "v160", "v161", "v162", "v163", "v164", "v165", "v166", "v167", "v168", "v169", "v170", "v171", "v172", "v173", "v174", "v175", "v176", "v177", "v178", "v179", "v180", "v181", "v182", "v183", "v184", "v185", "v186", "v187", "v188", "v189", "v190", "v191", "v192", "v193", "v194", "v195", "v196", "v197", "v198", "v199", "v200", "v201", "v202", "v203", "v204", "v205", "v206", "v207", "v208", "v209", "v210", "v211", "v212", "v213", "v214", "v215", "v216", "v217", "v218", "v219", "v220", "v221", "v222", "v223", "v224", "v225", "v226", "v227", "v228", "v229", "v230", "v231", "v232", "v233", "v234", "v235", "v236", "v237", "v238", "v239", "v240", "v241", "v242", "v243", "v244", "v245", "v246", "v247", "v248", "v249", "v250", "v251", "v252", "v253", "v254", "v255", "a0", "a1", "a2", "a3", "a4", "a5", "a6", "a7", "a8", "a9", "a10", "a11", "a12", "a13", "a14", "a15", "a16", "a17", "a18", "a19", "a20", "a21", "a22", "a23", "a24", "a25", "a26", "a27", "a28", "a29", "a30", "a31", "a32", "a33", "a34", "a35", "a36", "a37", "a38", "a39", "a40", "a41", "a42", "a43", "a44", "a45", "a46", "a47", "a48", "a49", "a50", "a51", "a52", "a53", "a54", "a55", "a56", "a57", "a58", "a59", "a60", "a61", "a62", "a63", "a64", "a65", "a66", "a67", "a68", "a69", "a70", "a71", "a72", "a73", "a74", "a75", "a76", "a77", "a78", "a79", "a80", "a81", "a82", "a83", "a84", "a85", "a86", "a87", "a88", "a89", "a90", "a91", "a92", "a93", "a94", "a95", "a96", "a97", "a98", "a99", "a100", "a101", "a102", "a103", "a104", "a105", "a106", "a107", "a108", "a109", "a110", "a111", "a112", "a113", "a114", "a115", "a116", "a117", "a118", "a119", "a120", "a121", "a122", "a123", "a124", "a125", "a126", "a127", "a128", "a129", "a130", "a131", "a132", "a133", "a134", "a135", "a136", "a137", "a138", "a139", "a140", "a141", "a142", "a143", "a144", "a145", "a146", "a147", "a148", "a149", "a150", "a151", "a152", "a153", "a154", "a155", "a156", "a157", "a158", "a159", "a160", "a161", "a162", "a163", "a164", "a165", "a166", "a167", "a168", "a169", "a170", "a171", "a172", "a173", "a174", "a175", "a176", "a177", "a178", "a179", "a180", "a181", "a182", "a183", "a184", "a185", "a186", "a187", "a188", "a189", "a190", "a191", "a192", "a193", "a194", "a195", "a196", "a197", "a198", "a199", "a200", "a201", "a202", "a203", "a204", "a205", "a206", "a207", "a208", "a209", "a210", "a211", "a212", "a213", "a214", "a215", "a216", "a217", "a218", "a219", "a220", "a221", "a222", "a223", "a224", "a225", "a226", "a227", "a228", "a229", "a230", "a231", "a232", "a233", "a234", "a235", "a236", "a237", "a238", "a239", "a240", "a241", "a242", "a243", "a244", "a245", "a246", "a247", "a248", "a249", "a250", "a251", "a252", "a253", "a254", "a255", "s8", "s9", "s10", "s11", "s12", "s13", "s14", "s15", "s16", "s17", "s18", "s19", "s20", "s21", "s22", "s23", "s24", "s25", "s26", "s27", "s28", "s29", "s30", "s31", "s34", "s35", "s36", "s37", "s38", "s39", "s40", "s41", "s42", "s43", "s44", "s45", "s46", "s47", "s48", "s49", "s50", "s51", "s52", "s53", "s54", "s55", "s56", "s57", "s58", "s59", "s60", "s61", "s62", "s63", "s64", "s65", "s66", "s67", "s68", "s69", "s70", "s71", "s72", "s73", "s74", "s75", "s76", "s77", "s78", "s79", "s80", "s81", "s82", "s83", "s84", "s85", "s86", "s87", "s88", "s89", "s90", "s91", "s92", "s93", "s94", "s95", "s96", "s97", "s98", "s99", "vcc", "scc", "memory");
}

extern "C" void kernel_launch(void* const* d_in, const int* in_sizes, int n_in, void* d_out, int out_size, void* d_ws, size_t ws_size,
                              hipStream_t stream) {
  (void)in_sizes; (void)n_in; (void)out_size; (void)ws_size;
  fused_attn<<<256, 256, 0, stream>>>(d_in[0], d_in[1], d_in[2], d_in[3], d_in[4], d_in[5], d_in[6], d_in[7], d_in[8], d_in[9], d_out, d_ws);

}
```
